```python
import torch

def _softmax(x, axis=-1):
    return torch.softmax(x, dim=axis)

def _gelu(x):
    return torch.nn.functional.gelu(x, approximate='tanh')

def _silu(x):
    return torch.nn.functional.silu(x)

def _sigmoid(x):
    return torch.sigmoid(x)

def _layernorm(x, axis=-1, eps=1e-05):
    m = x.mean(dim=axis, keepdim=True)
    v = x.var(dim=axis, keepdim=True, unbiased=False)
    return (x - m) / torch.sqrt(v + eps)

def _scatter_add(tgt, idx, src, axis=0):
    return tgt.clone().index_add_(axis, idx, src)

def _segment_sum(src, seg, n):
    out = torch.zeros((n,) + src.shape[1:], dtype=src.dtype)
    return out.index_add_(0, seg, src)

def _pad(x, pw):
    flat = tuple((v for pair in reversed(pw) for v in pair))
    return torch.nn.functional.pad(x, flat)
kernel_name = 'concat_heads__memory_aug_recurrent_mem_tr__compressed_kv_cla__prompt'

def setup_inputs():

    _w = lambda a: (lambda t: t.to(torch.bfloat16) if t.is_floating_point() else t)(torch.as_tensor(a))

    def _A_setup():
        B, S, Dm = (1, 2048, 512)
        H, Dh = (32, 128)
        M, seg = (8, 32)
        seg = min(seg, S)
        S = S // seg * seg
        return {'x': _w((lambda *s: torch.randn(*s, dtype=torch.bfloat16) * 0.05)(B, S, Dm)), 'mem0': _w((lambda *s: torch.randn(*s, dtype=torch.bfloat16) * 0.4)(B, M, Dm)), 'Wq': _w((lambda *s: torch.randn(*s, dtype=torch.bfloat16) * 6.4)(Dm, H, Dh) * Dm ** (-0.5)), 'Wk': _w((lambda *s: torch.randn(*s, dtype=torch.bfloat16) * 6.4)(Dm, H, Dh) * Dm ** (-0.5)), 'Wv': _w((lambda *s: torch.randn(*s, dtype=torch.bfloat16) * 0.4)(Dm, H, Dh) * Dm ** (-0.5)), 'Wo': _w((lambda *s: torch.randn(*s, dtype=torch.bfloat16) * 0.4)(H, Dh, Dm) * (H * Dh) ** (-0.5)), 'seg_len': seg}
    _A_in = _A_setup()

    def _B_setup():
        B, H, Sq, Sk, D, M = (1, 32, 2048, 2048, 128, 512)
        Dc = 4 * D
        return {'x': _w((lambda *s: torch.randn(*s, dtype=torch.bfloat16) * 1.6)(B, Sq, M)), 'c_kv': _w((lambda *s: torch.randn(*s, dtype=torch.bfloat16) * 0.2)(B, Sk, Dc)), 'w_q': _w((lambda *s: torch.randn(*s, dtype=torch.bfloat16) * 3.2)(M, H, D) * M ** (-0.5)), 'w_uk': _w((lambda *s: torch.randn(*s, dtype=torch.bfloat16) * 3.2)(Dc, H, D) * Dc ** (-0.5)), 'w_uv': _w((lambda *s: torch.randn(*s, dtype=torch.bfloat16) * 0.2)(Dc, H, D) * Dc ** (-0.5)), 'w_o': _w((lambda *s: torch.randn(*s, dtype=torch.bfloat16) * 0.2)(H, D, M) * (H * D) ** (-0.5))}
    _B_in = _B_setup()
    return {'A_x': _A_in['x'], 'A_mem0': _A_in['mem0'], 'A_Wq': _A_in['Wq'], 'A_Wk': _A_in['Wk'], 'A_Wv': _A_in['Wv'], 'A_Wo': _A_in['Wo'], 'A_seg_len': _A_in['seg_len'], 'B_x': _B_in['x'], 'B_c_kv': _B_in['c_kv'], 'B_w_q': _B_in['w_q'], 'B_w_uk': _B_in['w_uk'], 'B_w_uv': _B_in['w_uv'], 'B_w_o': _B_in['w_o']}

def reference(A_x, A_mem0, A_Wq, A_Wk, A_Wv, A_Wo, A_seg_len, B_x, B_c_kv, B_w_q, B_w_uk, B_w_uv, B_w_o):

    def _A_ref(x, mem0, Wq, Wk, Wv, Wo, seg_len):
        B, S, Dm = x.shape
        H, Dh = (Wq.shape[1], Wq.shape[2])
        M = mem0.shape[1]
        scale = Dh ** (-0.5)
        n_seg = S // seg_len
        L = M + seg_len
        idx = torch.arange(L)
        mask = (idx[:, None] < M) | (idx[:, None] >= idx[None, :])
        mem = mem0
        outs = []
        for s in range(n_seg):
            xs = x[:, s * seg_len:(s + 1) * seg_len, :].clone()
            h = torch.concatenate([mem, xs], axis=1)
            q = torch.einsum('btd,dhe->bhte', h, Wq)
            k = torch.einsum('btd,dhe->bhte', h, Wk)
            v = torch.einsum('btd,dhe->bhte', h, Wv)
            scores = torch.einsum('bhqe,bhke->bhqk', q, k) * scale
            scores = torch.where(mask[None, None], scores, -1000000000.0)
            p = _softmax(scores, axis=-1)
            o = torch.einsum('bhqk,bhke->bhqe', p, v)
            o = torch.einsum('bhte,hed->btd', o, Wo)
            mem = o[:, :M, :].clone()
            outs.append(o[:, M:, :])
        return torch.concatenate(outs, axis=1)

    def _B_ref(x, c_kv, w_q, w_uk, w_uv, w_o):
        B, Sq, M = (x.shape[0], x.shape[1], x.shape[2])
        Sk = c_kv.shape[1]
        H, D = (w_q.shape[1], w_q.shape[2])
        q = torch.einsum('bqm,mhd->bhqd', x, w_q)
        k = torch.einsum('bkc,chd->bhkd', c_kv, w_uk)
        v = torch.einsum('bkc,chd->bhkd', c_kv, w_uv)
        scores = torch.einsum('bhqd,bhkd->bhqk', q, k) * D ** (-0.5)
        off = Sk - Sq
        qi = torch.arange(Sq)[:, None]
        kj = torch.arange(Sk)[None, :]
        cmask = kj <= qi + off
        scores = torch.where(cmask[None, None], scores, -1000000000.0)
        p = _softmax(scores, axis=-1)
        ctx = torch.einsum('bhqk,bhkd->bhqd', p, v)
        return torch.einsum('bhqd,hdm->bqm', ctx, w_o)
    a = _A_ref(A_x, A_mem0, A_Wq, A_Wk, A_Wv, A_Wo, A_seg_len)
    b = _B_ref(B_x, B_c_kv, B_w_q, B_w_uk, B_w_uv, B_w_o)
    return torch.concatenate([a, b], axis=-1)
```

```cpp
#include <hip/hip_runtime.h>
#include <hip/hip_bf16.h>
#include <cstdint>
#include <utility>

typedef unsigned u32;
typedef unsigned u32x2 __attribute__((ext_vector_type(2)));
typedef unsigned u32x4 __attribute__((ext_vector_type(4)));
typedef unsigned u32x8 __attribute__((ext_vector_type(8)));
typedef unsigned u32x16 __attribute__((ext_vector_type(16)));
typedef float f32x2 __attribute__((ext_vector_type(2)));
typedef float f32x4 __attribute__((ext_vector_type(4)));
typedef float f32x16 __attribute__((ext_vector_type(16)));
typedef __bf16 bf16x2 __attribute__((ext_vector_type(2)));
typedef __bf16 bf16x4 __attribute__((ext_vector_type(4)));
typedef __amdgpu_buffer_rsrc_t rsrc_t;
#define LAS __attribute__((address_space(3)))
#define GAS __attribute__((address_space(1)))
#define DI __device__ __forceinline__
#define SB() __builtin_amdgcn_sched_barrier(0)
#define RLX_AGENT __ATOMIC_RELAXED, __HIP_MEMORY_SCOPE_AGENT
template <int... Is> using iseq = std::integer_sequence<int, Is...>;
template <int N> using mkseq = std::make_integer_sequence<int, N>;
template <class F, int... Is> DI void sfor_(F&& f, iseq<Is...>) { (f(std::integral_constant<int, Is>{}), ...); }
template <int N, class F> DI void sfor(F&& f) { sfor_(f, mkseq<N>{}); }
#define CI(x) decltype(x)::value

#define ROLES 31

constexpr u32 MB = 1u << 20;
constexpr u32 CTL_BYTES = 32768;
constexpr u32 W_ERR = 0, W_READY = 256, W_POL = 512, W_FH = 4096, W_FC = 8192, W_FF1 = 12288, W_FBP = 20480, W_FBA = 24576, W_CLAIM = 28672;
constexpr u32 WS_QA = 8 * MB, WS_KA = 24 * MB, WS_VA = 40 * MB, WS_QB = 56 * MB, WS_KB = 72 * MB, WS_VB = 88 * MB;
constexpr u32 WS_OA = 104 * MB, WS_OB = 120 * MB, WS_KM = 136 * MB, WS_VM = 140 * MB, WS_PROF = 144 * MB, WS_NEED = 148 * MB;
constexpr u32 OOB = 0x80000000u;
constexpr u32 SENT = 0xFFBFFFBFu;
constexpr int SC1 = 16;
constexpr u32 SPIN_CAP = 1u << 17;
constexpr float CSC = 0.12751743082f;
constexpr float THR = 8.0f;
constexpr int LDS_BYTES = 163840;

struct Params {
    const void* ax; const void* mem0; const void* awq; const void* awk; const void* awv; const void* awo;
    const void* bx; const void* bc; const void* bwq; const void* bwk; const void* bwv; const void* bwo;
    void* out; void* ws;
};

typedef const __attribute__((address_space(4))) Params* KP;

DI rsrc_t mkrsrc(const void* p, u32 bytes) { return __builtin_amdgcn_make_buffer_rsrc((void*)p, (short)0, (int)bytes, 0x00020000); }
DI u32 lane_id() { u32 l; asm volatile("v_mbcnt_lo_u32_b32 %0, -1, 0\n\tv_mbcnt_hi_u32_b32 %0, -1, %0" : "=v"(l)); return l; }
DI u32 rfl(u32 x) { return __builtin_amdgcn_readfirstlane(x); }
DI u32 cvtpk(float lo, float hi) { f32x2 v = {lo, hi}; bf16x2 b = __builtin_convertvector(v, bf16x2); return __builtin_bit_cast(u32, b); }
DI float max3f(float a, float b, float c) { float r; asm("v_max3_f32 %0, %1, %2, %3" : "=v"(r) : "v"(a), "v"(b), "v"(c)); return r; }
DI float max2f(float a, float b) { float r; asm("v_max_f32_e32 %0, %1, %2" : "=v"(r) : "v"(a), "v"(b)); return r; }
DI float fadd(float a, float b) { float r; asm("v_add_f32_e32 %0, %1, %2" : "=v"(r) : "v"(a), "v"(b)); return r; }
DI float fsub(float a, float b) { float r; asm("v_sub_f32_e32 %0, %1, %2" : "=v"(r) : "v"(a), "v"(b)); return r; }
DI float fmul(float a, float b) { float r; asm("v_mul_f32_e32 %0, %1, %2" : "=v"(r) : "v"(a), "v"(b)); return r; }
DI float ex2(float a) { return __builtin_amdgcn_exp2f(a); }
DI u32 umax3(u32 a, u32 b, u32 c) { return max(max(a, b), c); }
DI u32 f2u(float x) { return __float_as_uint(x); }
DI float u2f(u32 x) { return __uint_as_float(x); }
DI void swap32(float& a, float& b) { auto r = __builtin_amdgcn_permlane32_swap(f2u(a), f2u(b), false, false); a = u2f(r[0]); b = u2f(r[1]); }
DI void swap16(float& a, float& b) { auto r = __builtin_amdgcn_permlane16_swap(f2u(a), f2u(b), false, false); a = u2f(r[0]); b = u2f(r[1]); }
DI u32x4 ldsr128(u32 a) { return *(const LAS u32x4*)(size_t)a; }
DI void ldsw128(u32 a, u32x4 v) { *(LAS u32x4*)(size_t)a = v; }
DI u32x2 ldstr(u32 a) { bf16x4 r = __builtin_amdgcn_ds_read_tr16_b64_v4bf16((LAS bf16x4*)(size_t)a); return __builtin_bit_cast(u32x2, r); }
DI u32x4 cat(u32x2 a, u32x2 b) { return __builtin_shufflevector(a, b, 0, 1, 2, 3); }
DI u32x8 cat(u32x4 a, u32x4 b) { return __builtin_shufflevector(a, b, 0, 1, 2, 3, 4, 5, 6, 7); }
DI u32x4 gld128(rsrc_t r, u32 vo, u32 so) { return __builtin_amdgcn_raw_buffer_load_b128(r, vo, so, SC1); }
DI void gst128(u32x4 v, rsrc_t r, u32 vo, u32 so) { __builtin_amdgcn_raw_buffer_store_b128(v, r, vo, so, SC1); asm volatile("s_nop 1" ::"v"(v)); }
DI void gst32(u32 v, rsrc_t r, u32 vo, u32 so) { __builtin_amdgcn_raw_buffer_store_b32(v, r, vo, so, SC1); }
template <int N> DI void vmwait() { asm volatile("s_waitcnt vmcnt(%0)" ::"i"(N) : "memory"); }
DI void lgkm0() { asm volatile("s_waitcnt lgkmcnt(0)" ::: "memory"); }
DI void bar() { asm volatile("s_waitcnt lgkmcnt(0)\n\ts_barrier" ::: "memory"); }
template <int OFF = 0> DI void dma16(u32 ldsaddr, u32 vo, rsrc_t r, u32 so) {
    asm volatile("s_mov_b32 m0, %0\n\ts_nop 0\n\tbuffer_load_dwordx4 %1, %2, %3 offen offset:%4 lds" ::"s"(ldsaddr), "v"(vo), "s"(r), "s"(so), "i"(OFF) : "memory");
}
#define STAMP(i)
#define STAMP_END(i)
DI void wait_words(rsrc_t ws, u32 vo, u32 need, int lane) {
    for (u32 sp = 0;; ++sp) {
        asm volatile("" ::: "memory");
        const u32 x = __builtin_amdgcn_raw_buffer_load_b32(ws, vo, 0, SC1);
        if (__all(vo >= OOB || x >= need)) break;
        if (sp > SPIN_CAP) { if (lane == 0) gst32(1u, ws, W_ERR, 0); break; }
        if ((sp & 255u) == 255u && rfl(__builtin_amdgcn_raw_buffer_load_b32(ws, W_ERR, 0, SC1))) break;
        __builtin_amdgcn_s_sleep(2);
    }
    asm volatile("" ::: "memory");
}

#define A10(p) "a" #p "0", "a" #p "1", "a" #p "2", "a" #p "3", "a" #p "4", "a" #p "5", "a" #p "6", "a" #p "7", "a" #p "8", "a" #p "9"
#define ACLOB "a0", "a1", "a2", "a3", "a4", "a5", "a6", "a7", "a8", "a9", A10(1), A10(2), A10(3), A10(4), A10(5), A10(6), A10(7), A10(8), A10(9), \
    A10(10), A10(11), A10(12), A10(13), A10(14), A10(15), A10(16), A10(17), A10(18), A10(19), A10(20), A10(21), A10(22), A10(23), A10(24), \
    "a250", "a251", "a252", "a253", "a254", "a255"
#define V10(p) "v" #p "0", "v" #p "1", "v" #p "2", "v" #p "3", "v" #p "4", "v" #p "5", "v" #p "6", "v" #p "7", "v" #p "8", "v" #p "9"
#define S10(p) "s" #p "0", "s" #p "1", "s" #p "2", "s" #p "3", "s" #p "4", "s" #p "5", "s" #p "6", "s" #p "7", "s" #p "8", "s" #p "9"
#define VCLOB "v32", "v33", "v34", "v35", "v36", "v37", "v38", "v39", V10(4), V10(5), V10(6), V10(7), V10(8), V10(9), V10(10), V10(11), "v128", "v129", \
    V10(13), V10(14), V10(15), V10(16), V10(17), V10(18), V10(19), V10(20), V10(21), V10(22), V10(23), V10(24), "v250", "v251", "v252", "v253", "v254", "v255"
#define V120 "v120", "v121", "v122", "v123", "v124", "v125", "v126", "v127"
DI void own_agprs() { asm volatile("" ::: ACLOB); }
template <int A> DI void gld_a(u32 vo, rsrc_t r, u32 so) { asm volatile("buffer_load_dwordx4 a[%c3:%c4], %0, %1, %2 offen" ::"v"(vo), "s"(r), "s"(so), "i"(A), "i"(A + 3) : "memory"); }
template <int A, int OFF> DI void dsw_a(u32 ad) { asm volatile("ds_write_b128 %0, a[%c1:%c2] offset:%c3" ::"v"(ad), "i"(A), "i"(A + 3), "i"(OFF) : "memory"); }
template <int A, int OFF> DI void tr_a(u32 ad) { asm volatile("ds_read_b64_tr_b16 a[%c1:%c2], %0 offset:%c3" ::"v"(ad), "i"(A), "i"(A + 1), "i"(OFF) : "memory"); }
template <int A, bool Z> DI void m32_av(f32x16& d, u32x4 b) {
    if constexpr (Z) asm volatile("v_mfma_f32_32x32x16_bf16 %0, a[%c2:%c3], %1, 0" : "=&v"(d) : "v"(b), "i"(A), "i"(A + 3));
    else asm volatile("v_mfma_f32_32x32x16_bf16 %0, a[%c2:%c3], %1, %0" : "+v"(d) : "v"(b), "i"(A), "i"(A + 3));
}
template <bool Z> DI void m32_vv(f32x16& d, u32x4 a, u32x4 b) {
    if constexpr (Z) asm volatile("v_mfma_f32_32x32x16_bf16 %0, %1, %2, 0" : "=&v"(d) : "v"(a), "v"(b));
    else asm volatile("v_mfma_f32_32x32x16_bf16 %0, %1, %2, %0" : "+v"(d) : "v"(a), "v"(b));
}
template <int B> DI void m32_va(f32x16& d, u32x4 a) { asm volatile("v_mfma_f32_32x32x16_bf16 %0, %1, a[%c2:%c3], %0" : "+v"(d) : "v"(a), "i"(B), "i"(B + 3)); }
template <int B> DI void m32_va_c(f32x16& d, u32x4 a, const f32x16& c) { asm volatile("v_mfma_f32_32x32x16_bf16 %0, %1, a[%c3:%c4], %2" : "=&v"(d) : "v"(a), "v"(c), "i"(B), "i"(B + 3)); }
template <int D> DI void m32_acc(u32x4 a, u32x4 b) { asm volatile("v_mfma_f32_32x32x16_bf16 a[%c2:%c3], %0, %1, a[%c2:%c3]" ::"v"(a), "v"(b), "i"(D), "i"(D + 15)); }
template <bool Z> DI void m16_vv(f32x4& d, u32x4 a, u32x4 b) {
    if constexpr (Z) asm volatile("v_mfma_f32_16x16x32_bf16 %0, %1, %2, 0" : "=&v"(d) : "v"(a), "v"(b));
    else asm volatile("v_mfma_f32_16x16x32_bf16 %0, %1, %2, %0" : "+v"(d) : "v"(a), "v"(b));
}
DI void m16_vv_c(f32x4& d, u32x4 a, u32x4 b, f32x4 c) { asm volatile("v_mfma_f32_16x16x32_bf16 %0, %1, %2, %3" : "=&v"(d) : "v"(a), "v"(b), "v"(c)); }
template <int A, bool Z> DI void m16_av(f32x4& d, u32x4 b) {
    if constexpr (Z) asm volatile("v_mfma_f32_16x16x32_bf16 %0, a[%c2:%c3], %1, 0" : "=&v"(d) : "v"(b), "i"(A), "i"(A + 3));
    else asm volatile("v_mfma_f32_16x16x32_bf16 %0, a[%c2:%c3], %1, %0" : "+v"(d) : "v"(b), "i"(A), "i"(A + 3));
}
DI void m16k16(f32x4& d, u32x2 a, u32x2 b) { asm volatile("v_mfma_f32_16x16x16_bf16 %0, %1, %2, %0" : "+v"(d) : "v"(a), "v"(b)); }
template <int B> DI void sm_a(f32x4& d, u32x4 a, u32 idx) { asm volatile("v_smfmac_f32_16x16x64_bf16 %0, %1, a[%c3:%c4], %2" : "+v"(d) : "v"(a), "v"(idx), "i"(B), "i"(B + 7)); }
DI void sm_v(f32x4& d, u32x4 a, u32x8 b, u32 idx) { asm volatile("v_smfmac_f32_16x16x64_bf16 %0, %1, %2, %3" : "+v"(d) : "v"(a), "v"(b), "v"(idx)); }
template <int A> DI void ascale2(float f) {
    float t0, t1;
    asm volatile("v_accvgpr_read_b32 %0, a%c3\n\tv_accvgpr_read_b32 %1, a%c4\n\tv_mul_f32 %0, %2, %0\n\tv_mul_f32 %1, %2, %1\n\tv_accvgpr_write_b32 a%c3, %0\n\tv_accvgpr_write_b32 a%c4, %1"
                 : "=&v"(t0), "=&v"(t1) : "v"(f), "i"(A), "i"(A + 1));
}
template <int A> DI float aread() { float r; asm volatile("v_accvgpr_read_b32 %0, a%c1" : "=v"(r) : "i"(A)); return r; }
template <int N, class T0, class T1, class T2, class T3> DI void nop4(T0& a, T1& b, T2& c, T3& d) { asm volatile("s_nop %4" : "+v"(a), "+v"(b), "+v"(c), "+v"(d) : "i"(N)); }
template <int N, class T> DI void nop(T& x) { asm volatile("s_nop %1" : "+v"(x) : "i"(N)); }
template <class T> DI void pin(T& x) { asm volatile("" : "+v"(x)); }
DI u32x4 pack8(const f32x16& x, int b, float s) {
    return u32x4{cvtpk(x[b] * s, x[b + 1] * s), cvtpk(x[b + 2] * s, x[b + 3] * s), cvtpk(x[b + 4] * s, x[b + 5] * s), cvtpk(x[b + 6] * s, x[b + 7] * s)};
}
DI u32x4 pack8(const f32x16& x, int b) { return u32x4{cvtpk(x[b], x[b + 1]), cvtpk(x[b + 2], x[b + 3]), cvtpk(x[b + 4], x[b + 5]), cvtpk(x[b + 6], x[b + 7])}; }

constexpr int SCR_STRIDE = 1056, SCR_BYTES = 4 * SCR_STRIDE;
template <int KIND> DI u32 tr_base(int l) {
    const u32 q = (l >> 2) & 3, p = l & 3, g = l >> 4;
    if constexpr (KIND == 0) return (8 * (l >> 5) + q) * 64 + (g & 1) * 32 + p * 8;
    else if constexpr (KIND == 1) return (q & 1) * SCR_STRIDE + (4 * (g >> 1) + 2 * (q >> 1) + (g & 1)) * 64 + p * 8;
    else return (g >> 1) * SCR_STRIDE + (4 * (g & 1) + q) * 64 + p * 8;
}
template <int KIND, int I> constexpr int tr_off() {
    if constexpr (KIND == 0) return (I >> 1) * SCR_STRIDE + (I & 1) * 256;
    else return (I >> 2) * 2 * SCR_STRIDE + (I & 1) * 512 + ((I >> 1) & 1) * 32;
}
template <int KIND, int A> DI void chunk_a(u32 wad, u32 rad) {
    sfor<4>([&](auto i) { dsw_a<A + 4 * CI(i), CI(i) * SCR_STRIDE>(wad); });
    sfor<8>([&](auto i) { tr_a<A + 2 * CI(i), tr_off<KIND, CI(i)>()>(rad); });
}

template <int NSRC>
DI void proj(KP& kp, int h, int kb0, int qb0, int qb1, int wid) {
    const void *wq = NSRC == 1 ? kp->awq : kp->bwq, *wk = NSRC == 1 ? kp->awk : kp->bwk, *wv = NSRC == 1 ? kp->awv : kp->bwv;
    const void *x = NSRC == 1 ? kp->ax : kp->bx, *c = NSRC == 1 ? kp->ax : kp->bc;
    const rsrc_t ws = mkrsrc(kp->ws, WS_NEED);
    const u32 lane = lane_id(), r = lane & 31, hi = lane >> 5, q4 = lane >> 4, qq = (lane >> 2) & 3;
    u32x16 c0;
    c0[0] = r * 512 + ((hi ^ (r & 15)) << 4);
    sfor<4>([&](auto i) { c0[1 + CI(i)] = (hi + CI(i)) * 1024 + ((r ^ ((8 * wid + 2 * CI(i) + hi) & 15)) << 4); });
    c0[5] = wid * 2048 + lane * 16;
    c0[6] = q4 * 8192 + h * 256 + (((lane & 15) ^ (4 * q4)) << 4); c0[7] = lane;
    c0[8] = 65536 + (8 * hi + qq) * 256 + (((4 * wid + 2 * (q4 & 1) + ((lane >> 1) & 1)) ^ (4 * qq)) << 4) + (lane & 1) * 8; c0[9] = c0[8] + 49152;
    c0[10] = c0[11] = c0[12] = c0[13] = c0[14] = c0[15] = lane;
    const u32x4 sb = {(u32)wid, (u32)h, (u32)kb0, (u32)qb0};
    const u32x2 sc = {(u32)qb1, W_FH + h * 128 + wid * 4};
    const u32 prof = WS_PROF + (blockIdx.x * 4 + wid) * 1024;
#define PROJ_OPS : "+{v[0:15]}"(c0), "+s"(kp) \
    : "{s[36:39]}"(sb), "{s[40:43]}"(ws), "{s[44:47]}"(mkrsrc(wq, 4 * MB)), "{s[48:51]}"(mkrsrc(wk, 4 * MB)), "{s[52:55]}"(mkrsrc(wv, 4 * MB)), \
      "{s[56:59]}"(mkrsrc(x, 2 * MB)), "{s[60:63]}"(mkrsrc(c, 2 * MB)), "{s[64:65]}"(sc), "{s81}"(prof) \
    : "memory", "vcc", "scc", "s66", "s67", "s68", "s69", S10(7), "s80", "s82", "s83", "s84", "s85", "v16", "v17", "v18", "v19", V10(2), "v30", "v31", VCLOB, V120, ACLOB
    if constexpr (NSRC == 1) asm volatile(
    "s_lshl_b32 s77, s36, 13\n"
    "s_lshl_b32 s67, s36, 12\n"
    "s_mov_b32 s74, -1\n"
    "s_add_u32 s68, s74, 1\n"
    "s_min_u32 s68, s68, 63\n"
    "s_add_u32 s79, s68, s38\n"
    "s_mov_b32 s78, s79\n"
    "s_lshl_b32 s69, s78, 15\n"
    "s_add_u32 s69, s69, s77\n"
    "s_add_u32 m0, s67, 0\n"
    "s_nop 0\n"
    "buffer_load_dwordx4 v1, s[56:59], s69 offen offset:0 lds\n"
    "buffer_load_dwordx4 v2, s[56:59], s69 offen offset:1024 lds\n"
    "buffer_load_dwordx4 v3, s[56:59], s69 offen offset:2048 lds\n"
    "buffer_load_dwordx4 v4, s[56:59], s69 offen offset:3072 lds\n"
    "s_add_u32 m0, s67, 32768\n"
    "s_add_u32 s69, s69, 512\n"
    "buffer_load_dwordx4 v1, s[56:59], s69 offen offset:0 lds\n"
    "buffer_load_dwordx4 v2, s[56:59], s69 offen offset:1024 lds\n"
    "buffer_load_dwordx4 v3, s[56:59], s69 offen offset:2048 lds\n"
    "buffer_load_dwordx4 v4, s[56:59], s69 offen offset:3072 lds\n"
    "s_mov_b32 s75, s79\n"
    "s_mov_b32 s76, s78\n"
    "s_lshl_b32 s66, s36, 17\n"
    "s_add_u32 s80, s67, 0x10000\n"
    "s_add_u32 m0, s80, 0\n"
    "s_add_u32 s68, s66, 0x0\n"
    "buffer_load_dwordx4 v6, s[48:51], s68 offen offset:0 nt lds\n"
    "s_add_u32 s68, s66, 0x7c00\n"
    "buffer_load_dwordx4 v6, s[48:51], s68 offen offset:1024 nt lds\n"
    "s_add_u32 s68, s66, 0xf800\n"
    "buffer_load_dwordx4 v6, s[48:51], s68 offen offset:2048 nt lds\n"
    "s_add_u32 s68, s66, 0x17400\n"
    "buffer_load_dwordx4 v6, s[48:51], s68 offen offset:3072 nt lds\n"
    "s_add_u32 m0, s80, 16384\n"
    "s_add_u32 s68, s66, 0x80000\n"
    "buffer_load_dwordx4 v6, s[48:51], s68 offen offset:0 nt lds\n"
    "s_add_u32 s68, s66, 0x87c00\n"
    "buffer_load_dwordx4 v6, s[48:51], s68 offen offset:1024 nt lds\n"
    "s_add_u32 s68, s66, 0x8f800\n"
    "buffer_load_dwordx4 v6, s[48:51], s68 offen offset:2048 nt lds\n"
    "s_add_u32 s68, s66, 0x97400\n"
    "buffer_load_dwordx4 v6, s[48:51], s68 offen offset:3072 nt lds\n"
    "s_add_u32 m0, s80, 32768\n"
    "s_add_u32 s68, s66, 0x100000\n"
    "buffer_load_dwordx4 v6, s[48:51], s68 offen offset:0 nt lds\n"
    "s_add_u32 s68, s66, 0x107c00\n"
    "buffer_load_dwordx4 v6, s[48:51], s68 offen offset:1024 nt lds\n"
    "s_add_u32 s68, s66, 0x10f800\n"
    "buffer_load_dwordx4 v6, s[48:51], s68 offen offset:2048 nt lds\n"
    "s_add_u32 s68, s66, 0x117400\n"
    "buffer_load_dwordx4 v6, s[48:51], s68 offen offset:3072 nt lds\n"
    "s_add_u32 m0, s80, 49152\n"
    "s_add_u32 s68, s66, 0x180000\n"
    "buffer_load_dwordx4 v6, s[48:51], s68 offen offset:0 nt lds\n"
    "s_add_u32 s68, s66, 0x187c00\n"
    "buffer_load_dwordx4 v6, s[48:51], s68 offen offset:1024 nt lds\n"
    "s_add_u32 s68, s66, 0x18f800\n"
    "buffer_load_dwordx4 v6, s[48:51], s68 offen offset:2048 nt lds\n"
    "s_add_u32 s68, s66, 0x197400\n"
    "buffer_load_dwordx4 v6, s[48:51], s68 offen offset:3072 nt lds\n"
    "s_add_u32 m0, s80, 65536\n"
    "s_add_u32 s68, s66, 0x200000\n"
    "buffer_load_dwordx4 v6, s[48:51], s68 offen offset:0 nt lds\n"
    "s_add_u32 s68, s66, 0x207c00\n"
    "buffer_load_dwordx4 v6, s[48:51], s68 offen offset:1024 nt lds\n"
    "s_add_u32 s68, s66, 0x20f800\n"
    "buffer_load_dwordx4 v6, s[48:51], s68 offen offset:2048 nt lds\n"
    "s_add_u32 s68, s66, 0x217400\n"
    "buffer_load_dwordx4 v6, s[48:51], s68 offen offset:3072 nt lds\n"
    "s_waitcnt vmcnt(16)\n"
    "s_waitcnt lgkmcnt(0)\n"
    "s_barrier\n"
    "s_add_u32 m0, s80, 81920\n"
    "s_add_u32 s68, s66, 0x280000\n"
    "buffer_load_dwordx4 v6, s[48:51], s68 offen offset:0 nt lds\n"
    "s_add_u32 s68, s66, 0x287c00\n"
    "buffer_load_dwordx4 v6, s[48:51], s68 offen offset:1024 nt lds\n"
    "s_add_u32 s68, s66, 0x28f800\n"
    "buffer_load_dwordx4 v6, s[48:51], s68 offen offset:2048 nt lds\n"
    "s_add_u32 s68, s66, 0x297400\n"
    "buffer_load_dwordx4 v6, s[48:51], s68 offen offset:3072 nt lds\n"
    "ds_read_b64_tr_b16 a[128:129], v8 offset:0\n"
    "ds_read_b64_tr_b16 a[130:131], v8 offset:1024\n"
    "ds_read_b64_tr_b16 a[132:133], v8 offset:4096\n"
    "ds_read_b64_tr_b16 a[134:135], v8 offset:5120\n"
    "ds_read_b64_tr_b16 a[136:137], v8 offset:8192\n"
    "ds_read_b64_tr_b16 a[138:139], v8 offset:9216\n"
    "ds_read_b64_tr_b16 a[140:141], v8 offset:12288\n"
    "ds_read_b64_tr_b16 a[142:143], v8 offset:13312\n"
    "s_waitcnt vmcnt(16)\n"
    "s_waitcnt lgkmcnt(0)\n"
    "s_barrier\n"
    "s_add_u32 m0, s80, 0\n"
    "s_add_u32 s68, s66, 0x300000\n"
    "buffer_load_dwordx4 v6, s[48:51], s68 offen offset:0 nt lds\n"
    "s_add_u32 s68, s66, 0x307c00\n"
    "buffer_load_dwordx4 v6, s[48:51], s68 offen offset:1024 nt lds\n"
    "s_add_u32 s68, s66, 0x30f800\n"
    "buffer_load_dwordx4 v6, s[48:51], s68 offen offset:2048 nt lds\n"
    "s_add_u32 s68, s66, 0x317400\n"
    "buffer_load_dwordx4 v6, s[48:51], s68 offen offset:3072 nt lds\n"
    "ds_read_b64_tr_b16 a[144:145], v8 offset:16384\n"
    "ds_read_b64_tr_b16 a[146:147], v8 offset:17408\n"
    "ds_read_b64_tr_b16 a[148:149], v8 offset:20480\n"
    "ds_read_b64_tr_b16 a[150:151], v8 offset:21504\n"
    "ds_read_b64_tr_b16 a[152:153], v8 offset:24576\n"
    "ds_read_b64_tr_b16 a[154:155], v8 offset:25600\n"
    "ds_read_b64_tr_b16 a[156:157], v8 offset:28672\n"
    "ds_read_b64_tr_b16 a[158:159], v8 offset:29696\n"
    "s_waitcnt vmcnt(16)\n"
    "s_waitcnt lgkmcnt(0)\n"
    "s_barrier\n"
    "s_add_u32 m0, s80, 16384\n"
    "s_add_u32 s68, s66, 0x380000\n"
    "buffer_load_dwordx4 v6, s[48:51], s68 offen offset:0 nt lds\n"
    "s_add_u32 s68, s66, 0x387c00\n"
    "buffer_load_dwordx4 v6, s[48:51], s68 offen offset:1024 nt lds\n"
    "s_add_u32 s68, s66, 0x38f800\n"
    "buffer_load_dwordx4 v6, s[48:51], s68 offen offset:2048 nt lds\n"
    "s_add_u32 s68, s66, 0x397400\n"
    "buffer_load_dwordx4 v6, s[48:51], s68 offen offset:3072 nt lds\n"
    "ds_read_b64_tr_b16 a[160:161], v8 offset:32768\n"
    "ds_read_b64_tr_b16 a[162:163], v8 offset:33792\n"
    "ds_read_b64_tr_b16 a[164:165], v8 offset:36864\n"
    "ds_read_b64_tr_b16 a[166:167], v8 offset:37888\n"
    "ds_read_b64_tr_b16 a[168:169], v8 offset:40960\n"
    "ds_read_b64_tr_b16 a[170:171], v8 offset:41984\n"
    "ds_read_b64_tr_b16 a[172:173], v8 offset:45056\n"
    "ds_read_b64_tr_b16 a[174:175], v8 offset:46080\n"
    "s_waitcnt vmcnt(16)\n"
    "s_waitcnt lgkmcnt(0)\n"
    "s_barrier\n"
    "s_add_u32 m0, s80, 32768\n"
    "s_add_u32 s68, s66, 0x0\n"
    "buffer_load_dwordx4 v6, s[52:55], s68 offen offset:0 nt lds\n"
    "s_add_u32 s68, s66, 0x7c00\n"
    "buffer_load_dwordx4 v6, s[52:55], s68 offen offset:1024 nt lds\n"
    "s_add_u32 s68, s66, 0xf800\n"
    "buffer_load_dwordx4 v6, s[52:55], s68 offen offset:2048 nt lds\n"
    "s_add_u32 s68, s66, 0x17400\n"
    "buffer_load_dwordx4 v6, s[52:55], s68 offen offset:3072 nt lds\n"
    "ds_read_b64_tr_b16 a[176:177], v9 offset:0\n"
    "ds_read_b64_tr_b16 a[178:179], v9 offset:1024\n"
    "ds_read_b64_tr_b16 a[180:181], v9 offset:4096\n"
    "ds_read_b64_tr_b16 a[182:183], v9 offset:5120\n"
    "ds_read_b64_tr_b16 a[184:185], v9 offset:8192\n"
    "ds_read_b64_tr_b16 a[186:187], v9 offset:9216\n"
    "ds_read_b64_tr_b16 a[188:189], v9 offset:12288\n"
    "ds_read_b64_tr_b16 a[190:191], v9 offset:13312\n"
    "s_waitcnt vmcnt(16)\n"
    "s_waitcnt lgkmcnt(0)\n"
    "s_barrier\n"
    "s_add_u32 m0, s80, 49152\n"
    "s_add_u32 s68, s66, 0x80000\n"
    "buffer_load_dwordx4 v6, s[52:55], s68 offen offset:0 nt lds\n"
    "s_add_u32 s68, s66, 0x87c00\n"
    "buffer_load_dwordx4 v6, s[52:55], s68 offen offset:1024 nt lds\n"
    "s_add_u32 s68, s66, 0x8f800\n"
    "buffer_load_dwordx4 v6, s[52:55], s68 offen offset:2048 nt lds\n"
    "s_add_u32 s68, s66, 0x97400\n"
    "buffer_load_dwordx4 v6, s[52:55], s68 offen offset:3072 nt lds\n"
    "ds_read_b64_tr_b16 a[192:193], v9 offset:16384\n"
    "ds_read_b64_tr_b16 a[194:195], v9 offset:17408\n"
    "ds_read_b64_tr_b16 a[196:197], v9 offset:20480\n"
    "ds_read_b64_tr_b16 a[198:199], v9 offset:21504\n"
    "ds_read_b64_tr_b16 a[200:201], v9 offset:24576\n"
    "ds_read_b64_tr_b16 a[202:203], v9 offset:25600\n"
    "ds_read_b64_tr_b16 a[204:205], v9 offset:28672\n"
    "ds_read_b64_tr_b16 a[206:207], v9 offset:29696\n"
    "s_waitcnt vmcnt(16)\n"
    "s_waitcnt lgkmcnt(0)\n"
    "s_barrier\n"
    "s_add_u32 m0, s80, 65536\n"
    "s_add_u32 s68, s66, 0x100000\n"
    "buffer_load_dwordx4 v6, s[52:55], s68 offen offset:0 nt lds\n"
    "s_add_u32 s68, s66, 0x107c00\n"
    "buffer_load_dwordx4 v6, s[52:55], s68 offen offset:1024 nt lds\n"
    "s_add_u32 s68, s66, 0x10f800\n"
    "buffer_load_dwordx4 v6, s[52:55], s68 offen offset:2048 nt lds\n"
    "s_add_u32 s68, s66, 0x117400\n"
    "buffer_load_dwordx4 v6, s[52:55], s68 offen offset:3072 nt lds\n"
    "ds_read_b64_tr_b16 a[208:209], v9 offset:32768\n"
    "ds_read_b64_tr_b16 a[210:211], v9 offset:33792\n"
    "ds_read_b64_tr_b16 a[212:213], v9 offset:36864\n"
    "ds_read_b64_tr_b16 a[214:215], v9 offset:37888\n"
    "ds_read_b64_tr_b16 a[216:217], v9 offset:40960\n"
    "ds_read_b64_tr_b16 a[218:219], v9 offset:41984\n"
    "ds_read_b64_tr_b16 a[220:221], v9 offset:45056\n"
    "ds_read_b64_tr_b16 a[222:223], v9 offset:46080\n"
    "s_waitcnt vmcnt(16)\n"
    "s_waitcnt lgkmcnt(0)\n"
    "s_barrier\n"
    "s_add_u32 m0, s80, 81920\n"
    "s_add_u32 s68, s66, 0x180000\n"
    "buffer_load_dwordx4 v6, s[52:55], s68 offen offset:0 nt lds\n"
    "s_add_u32 s68, s66, 0x187c00\n"
    "buffer_load_dwordx4 v6, s[52:55], s68 offen offset:1024 nt lds\n"
    "s_add_u32 s68, s66, 0x18f800\n"
    "buffer_load_dwordx4 v6, s[52:55], s68 offen offset:2048 nt lds\n"
    "s_add_u32 s68, s66, 0x197400\n"
    "buffer_load_dwordx4 v6, s[52:55], s68 offen offset:3072 nt lds\n"
    "ds_read_b64_tr_b16 a[224:225], v8 offset:0\n"
    "ds_read_b64_tr_b16 a[226:227], v8 offset:1024\n"
    "ds_read_b64_tr_b16 a[228:229], v8 offset:4096\n"
    "ds_read_b64_tr_b16 a[230:231], v8 offset:5120\n"
    "ds_read_b64_tr_b16 a[232:233], v8 offset:8192\n"
    "ds_read_b64_tr_b16 a[234:235], v8 offset:9216\n"
    "ds_read_b64_tr_b16 a[236:237], v8 offset:12288\n"
    "ds_read_b64_tr_b16 a[238:239], v8 offset:13312\n"
    "s_waitcnt vmcnt(16)\n"
    "s_waitcnt lgkmcnt(0)\n"
    "s_barrier\n"
    "s_add_u32 m0, s80, 0\n"
    "s_add_u32 s68, s66, 0x200000\n"
    "buffer_load_dwordx4 v6, s[52:55], s68 offen offset:0 nt lds\n"
    "s_add_u32 s68, s66, 0x207c00\n"
    "buffer_load_dwordx4 v6, s[52:55], s68 offen offset:1024 nt lds\n"
    "s_add_u32 s68, s66, 0x20f800\n"
    "buffer_load_dwordx4 v6, s[52:55], s68 offen offset:2048 nt lds\n"
    "s_add_u32 s68, s66, 0x217400\n"
    "buffer_load_dwordx4 v6, s[52:55], s68 offen offset:3072 nt lds\n"
    "ds_read_b64_tr_b16 a[240:241], v8 offset:16384\n"
    "ds_read_b64_tr_b16 a[242:243], v8 offset:17408\n"
    "ds_read_b64_tr_b16 a[244:245], v8 offset:20480\n"
    "ds_read_b64_tr_b16 a[246:247], v8 offset:21504\n"
    "ds_read_b64_tr_b16 a[248:249], v8 offset:24576\n"
    "ds_read_b64_tr_b16 a[250:251], v8 offset:25600\n"
    "ds_read_b64_tr_b16 a[252:253], v8 offset:28672\n"
    "ds_read_b64_tr_b16 a[254:255], v8 offset:29696\n"
    "s_waitcnt vmcnt(16)\n"
    "s_waitcnt lgkmcnt(0)\n"
    "s_barrier\n"
    "s_add_u32 m0, s80, 16384\n"
    "s_add_u32 s68, s66, 0x280000\n"
    "buffer_load_dwordx4 v6, s[52:55], s68 offen offset:0 nt lds\n"
    "s_add_u32 s68, s66, 0x287c00\n"
    "buffer_load_dwordx4 v6, s[52:55], s68 offen offset:1024 nt lds\n"
    "s_add_u32 s68, s66, 0x28f800\n"
    "buffer_load_dwordx4 v6, s[52:55], s68 offen offset:2048 nt lds\n"
    "s_add_u32 s68, s66, 0x297400\n"
    "buffer_load_dwordx4 v6, s[52:55], s68 offen offset:3072 nt lds\n"
    "ds_read_b64_tr_b16 v[128:129], v8 offset:32768\n"
    "ds_read_b64_tr_b16 v[130:131], v8 offset:33792\n"
    "ds_read_b64_tr_b16 v[132:133], v8 offset:36864\n"
    "ds_read_b64_tr_b16 v[134:135], v8 offset:37888\n"
    "ds_read_b64_tr_b16 v[136:137], v8 offset:40960\n"
    "ds_read_b64_tr_b16 v[138:139], v8 offset:41984\n"
    "ds_read_b64_tr_b16 v[140:141], v8 offset:45056\n"
    "ds_read_b64_tr_b16 v[142:143], v8 offset:46080\n"
    "s_waitcnt vmcnt(16)\n"
    "s_waitcnt lgkmcnt(0)\n"
    "s_barrier\n"
    "s_add_u32 m0, s80, 32768\n"
    "s_add_u32 s68, s66, 0x300000\n"
    "buffer_load_dwordx4 v6, s[52:55], s68 offen offset:0 nt lds\n"
    "s_add_u32 s68, s66, 0x307c00\n"
    "buffer_load_dwordx4 v6, s[52:55], s68 offen offset:1024 nt lds\n"
    "s_add_u32 s68, s66, 0x30f800\n"
    "buffer_load_dwordx4 v6, s[52:55], s68 offen offset:2048 nt lds\n"
    "s_add_u32 s68, s66, 0x317400\n"
    "buffer_load_dwordx4 v6, s[52:55], s68 offen offset:3072 nt lds\n"
    "ds_read_b64_tr_b16 v[144:145], v9 offset:0\n"
    "ds_read_b64_tr_b16 v[146:147], v9 offset:1024\n"
    "ds_read_b64_tr_b16 v[148:149], v9 offset:4096\n"
    "ds_read_b64_tr_b16 v[150:151], v9 offset:5120\n"
    "ds_read_b64_tr_b16 v[152:153], v9 offset:8192\n"
    "ds_read_b64_tr_b16 v[154:155], v9 offset:9216\n"
    "ds_read_b64_tr_b16 v[156:157], v9 offset:12288\n"
    "ds_read_b64_tr_b16 v[158:159], v9 offset:13312\n"
    "s_waitcnt vmcnt(16)\n"
    "s_waitcnt lgkmcnt(0)\n"
    "s_barrier\n"
    "s_add_u32 m0, s80, 49152\n"
    "s_add_u32 s68, s66, 0x380000\n"
    "buffer_load_dwordx4 v6, s[52:55], s68 offen offset:0 nt lds\n"
    "s_add_u32 s68, s66, 0x387c00\n"
    "buffer_load_dwordx4 v6, s[52:55], s68 offen offset:1024 nt lds\n"
    "s_add_u32 s68, s66, 0x38f800\n"
    "buffer_load_dwordx4 v6, s[52:55], s68 offen offset:2048 nt lds\n"
    "s_add_u32 s68, s66, 0x397400\n"
    "buffer_load_dwordx4 v6, s[52:55], s68 offen offset:3072 nt lds\n"
    "ds_read_b64_tr_b16 v[160:161], v9 offset:16384\n"
    "ds_read_b64_tr_b16 v[162:163], v9 offset:17408\n"
    "ds_read_b64_tr_b16 v[164:165], v9 offset:20480\n"
    "ds_read_b64_tr_b16 v[166:167], v9 offset:21504\n"
    "ds_read_b64_tr_b16 v[168:169], v9 offset:24576\n"
    "ds_read_b64_tr_b16 v[170:171], v9 offset:25600\n"
    "ds_read_b64_tr_b16 v[172:173], v9 offset:28672\n"
    "ds_read_b64_tr_b16 v[174:175], v9 offset:29696\n"
    "s_waitcnt vmcnt(16)\n"
    "s_waitcnt lgkmcnt(0)\n"
    "s_barrier\n"
    "s_add_u32 m0, s80, 65536\n"
    "s_add_u32 s68, s66, 0x0\n"
    "buffer_load_dwordx4 v6, s[44:47], s68 offen offset:0 nt lds\n"
    "s_add_u32 s68, s66, 0x7c00\n"
    "buffer_load_dwordx4 v6, s[44:47], s68 offen offset:1024 nt lds\n"
    "s_add_u32 s68, s66, 0xf800\n"
    "buffer_load_dwordx4 v6, s[44:47], s68 offen offset:2048 nt lds\n"
    "s_add_u32 s68, s66, 0x17400\n"
    "buffer_load_dwordx4 v6, s[44:47], s68 offen offset:3072 nt lds\n"
    "ds_read_b64_tr_b16 v[176:177], v9 offset:32768\n"
    "ds_read_b64_tr_b16 v[178:179], v9 offset:33792\n"
    "ds_read_b64_tr_b16 v[180:181], v9 offset:36864\n"
    "ds_read_b64_tr_b16 v[182:183], v9 offset:37888\n"
    "ds_read_b64_tr_b16 v[184:185], v9 offset:40960\n"
    "ds_read_b64_tr_b16 v[186:187], v9 offset:41984\n"
    "ds_read_b64_tr_b16 v[188:189], v9 offset:45056\n"
    "ds_read_b64_tr_b16 v[190:191], v9 offset:46080\n"
    "s_waitcnt vmcnt(16)\n"
    "s_waitcnt lgkmcnt(0)\n"
    "s_barrier\n"
    "s_add_u32 m0, s80, 81920\n"
    "s_add_u32 s68, s66, 0x80000\n"
    "buffer_load_dwordx4 v6, s[44:47], s68 offen offset:0 nt lds\n"
    "s_add_u32 s68, s66, 0x87c00\n"
    "buffer_load_dwordx4 v6, s[44:47], s68 offen offset:1024 nt lds\n"
    "s_add_u32 s68, s66, 0x8f800\n"
    "buffer_load_dwordx4 v6, s[44:47], s68 offen offset:2048 nt lds\n"
    "s_add_u32 s68, s66, 0x97400\n"
    "buffer_load_dwordx4 v6, s[44:47], s68 offen offset:3072 nt lds\n"
    "ds_read_b64_tr_b16 v[192:193], v8 offset:0\n"
    "ds_read_b64_tr_b16 v[194:195], v8 offset:1024\n"
    "ds_read_b64_tr_b16 v[196:197], v8 offset:4096\n"
    "ds_read_b64_tr_b16 v[198:199], v8 offset:5120\n"
    "ds_read_b64_tr_b16 v[200:201], v8 offset:8192\n"
    "ds_read_b64_tr_b16 v[202:203], v8 offset:9216\n"
    "ds_read_b64_tr_b16 v[204:205], v8 offset:12288\n"
    "ds_read_b64_tr_b16 v[206:207], v8 offset:13312\n"
    "s_waitcnt vmcnt(16)\n"
    "s_waitcnt lgkmcnt(0)\n"
    "s_barrier\n"
    "s_add_u32 m0, s80, 0\n"
    "s_add_u32 s68, s66, 0x100000\n"
    "buffer_load_dwordx4 v6, s[44:47], s68 offen offset:0 nt lds\n"
    "s_add_u32 s68, s66, 0x107c00\n"
    "buffer_load_dwordx4 v6, s[44:47], s68 offen offset:1024 nt lds\n"
    "s_add_u32 s68, s66, 0x10f800\n"
    "buffer_load_dwordx4 v6, s[44:47], s68 offen offset:2048 nt lds\n"
    "s_add_u32 s68, s66, 0x117400\n"
    "buffer_load_dwordx4 v6, s[44:47], s68 offen offset:3072 nt lds\n"
    "ds_read_b64_tr_b16 v[208:209], v8 offset:16384\n"
    "ds_read_b64_tr_b16 v[210:211], v8 offset:17408\n"
    "ds_read_b64_tr_b16 v[212:213], v8 offset:20480\n"
    "ds_read_b64_tr_b16 v[214:215], v8 offset:21504\n"
    "ds_read_b64_tr_b16 v[216:217], v8 offset:24576\n"
    "ds_read_b64_tr_b16 v[218:219], v8 offset:25600\n"
    "ds_read_b64_tr_b16 v[220:221], v8 offset:28672\n"
    "ds_read_b64_tr_b16 v[222:223], v8 offset:29696\n"
    "s_waitcnt vmcnt(16)\n"
    "s_waitcnt lgkmcnt(0)\n"
    "s_barrier\n"
    "s_add_u32 m0, s80, 16384\n"
    "s_add_u32 s68, s66, 0x180000\n"
    "buffer_load_dwordx4 v6, s[44:47], s68 offen offset:0 nt lds\n"
    "s_add_u32 s68, s66, 0x187c00\n"
    "buffer_load_dwordx4 v6, s[44:47], s68 offen offset:1024 nt lds\n"
    "s_add_u32 s68, s66, 0x18f800\n"
    "buffer_load_dwordx4 v6, s[44:47], s68 offen offset:2048 nt lds\n"
    "s_add_u32 s68, s66, 0x197400\n"
    "buffer_load_dwordx4 v6, s[44:47], s68 offen offset:3072 nt lds\n"
    "ds_read_b64_tr_b16 v[224:225], v8 offset:32768\n"
    "ds_read_b64_tr_b16 v[226:227], v8 offset:33792\n"
    "ds_read_b64_tr_b16 v[228:229], v8 offset:36864\n"
    "ds_read_b64_tr_b16 v[230:231], v8 offset:37888\n"
    "ds_read_b64_tr_b16 v[232:233], v8 offset:40960\n"
    "ds_read_b64_tr_b16 v[234:235], v8 offset:41984\n"
    "ds_read_b64_tr_b16 v[236:237], v8 offset:45056\n"
    "ds_read_b64_tr_b16 v[238:239], v8 offset:46080\n"
    "s_waitcnt vmcnt(16)\n"
    "s_waitcnt lgkmcnt(0)\n"
    "s_barrier\n"
    "s_add_u32 m0, s80, 32768\n"
    "s_add_u32 s68, s66, 0x200000\n"
    "buffer_load_dwordx4 v6, s[44:47], s68 offen offset:0 nt lds\n"
    "s_add_u32 s68, s66, 0x207c00\n"
    "buffer_load_dwordx4 v6, s[44:47], s68 offen offset:1024 nt lds\n"
    "s_add_u32 s68, s66, 0x20f800\n"
    "buffer_load_dwordx4 v6, s[44:47], s68 offen offset:2048 nt lds\n"
    "s_add_u32 s68, s66, 0x217400\n"
    "buffer_load_dwordx4 v6, s[44:47], s68 offen offset:3072 nt lds\n"
    "ds_read_b64_tr_b16 v[240:241], v9 offset:0\n"
    "ds_read_b64_tr_b16 v[242:243], v9 offset:1024\n"
    "ds_read_b64_tr_b16 v[244:245], v9 offset:4096\n"
    "ds_read_b64_tr_b16 v[246:247], v9 offset:5120\n"
    "ds_read_b64_tr_b16 v[248:249], v9 offset:8192\n"
    "ds_read_b64_tr_b16 v[250:251], v9 offset:9216\n"
    "ds_read_b64_tr_b16 v[252:253], v9 offset:12288\n"
    "ds_read_b64_tr_b16 v[254:255], v9 offset:13312\n"
    "s_waitcnt vmcnt(16)\n"
    "s_waitcnt lgkmcnt(0)\n"
    "s_barrier\n"
    "s_add_u32 m0, s80, 49152\n"
    "s_add_u32 s68, s66, 0x280000\n"
    "buffer_load_dwordx4 v6, s[44:47], s68 offen offset:0 nt lds\n"
    "s_add_u32 s68, s66, 0x287c00\n"
    "buffer_load_dwordx4 v6, s[44:47], s68 offen offset:1024 nt lds\n"
    "s_add_u32 s68, s66, 0x28f800\n"
    "buffer_load_dwordx4 v6, s[44:47], s68 offen offset:2048 nt lds\n"
    "s_add_u32 s68, s66, 0x297400\n"
    "buffer_load_dwordx4 v6, s[44:47], s68 offen offset:3072 nt lds\n"
    "ds_read_b64_tr_b16 a[0:1], v9 offset:16384\n"
    "ds_read_b64_tr_b16 a[2:3], v9 offset:17408\n"
    "ds_read_b64_tr_b16 a[4:5], v9 offset:20480\n"
    "ds_read_b64_tr_b16 a[6:7], v9 offset:21504\n"
    "ds_read_b64_tr_b16 a[8:9], v9 offset:24576\n"
    "ds_read_b64_tr_b16 a[10:11], v9 offset:25600\n"
    "ds_read_b64_tr_b16 a[12:13], v9 offset:28672\n"
    "ds_read_b64_tr_b16 a[14:15], v9 offset:29696\n"
    "s_waitcnt vmcnt(16)\n"
    "s_waitcnt lgkmcnt(0)\n"
    "s_barrier\n"
    "s_add_u32 m0, s80, 65536\n"
    "s_add_u32 s68, s66, 0x300000\n"
    "buffer_load_dwordx4 v6, s[44:47], s68 offen offset:0 nt lds\n"
    "s_add_u32 s68, s66, 0x307c00\n"
    "buffer_load_dwordx4 v6, s[44:47], s68 offen offset:1024 nt lds\n"
    "s_add_u32 s68, s66, 0x30f800\n"
    "buffer_load_dwordx4 v6, s[44:47], s68 offen offset:2048 nt lds\n"
    "s_add_u32 s68, s66, 0x317400\n"
    "buffer_load_dwordx4 v6, s[44:47], s68 offen offset:3072 nt lds\n"
    "ds_read_b64_tr_b16 a[16:17], v9 offset:32768\n"
    "ds_read_b64_tr_b16 a[18:19], v9 offset:33792\n"
    "ds_read_b64_tr_b16 a[20:21], v9 offset:36864\n"
    "ds_read_b64_tr_b16 a[22:23], v9 offset:37888\n"
    "ds_read_b64_tr_b16 a[24:25], v9 offset:40960\n"
    "ds_read_b64_tr_b16 a[26:27], v9 offset:41984\n"
    "ds_read_b64_tr_b16 a[28:29], v9 offset:45056\n"
    "ds_read_b64_tr_b16 a[30:31], v9 offset:46080\n"
    "s_waitcnt vmcnt(16)\n"
    "s_waitcnt lgkmcnt(0)\n"
    "s_barrier\n"
    "s_add_u32 m0, s80, 81920\n"
    "s_add_u32 s68, s66, 0x380000\n"
    "buffer_load_dwordx4 v6, s[44:47], s68 offen offset:0 nt lds\n"
    "s_add_u32 s68, s66, 0x387c00\n"
    "buffer_load_dwordx4 v6, s[44:47], s68 offen offset:1024 nt lds\n"
    "s_add_u32 s68, s66, 0x38f800\n"
    "buffer_load_dwordx4 v6, s[44:47], s68 offen offset:2048 nt lds\n"
    "s_add_u32 s68, s66, 0x397400\n"
    "buffer_load_dwordx4 v6, s[44:47], s68 offen offset:3072 nt lds\n"
    "ds_read_b64_tr_b16 a[32:33], v8 offset:0\n"
    "ds_read_b64_tr_b16 a[34:35], v8 offset:1024\n"
    "ds_read_b64_tr_b16 a[36:37], v8 offset:4096\n"
    "ds_read_b64_tr_b16 a[38:39], v8 offset:5120\n"
    "ds_read_b64_tr_b16 a[40:41], v8 offset:8192\n"
    "ds_read_b64_tr_b16 a[42:43], v8 offset:9216\n"
    "ds_read_b64_tr_b16 a[44:45], v8 offset:12288\n"
    "ds_read_b64_tr_b16 a[46:47], v8 offset:13312\n"
    "s_waitcnt vmcnt(16)\n"
    "s_waitcnt lgkmcnt(0)\n"
    "s_barrier\n"
    "ds_read_b64_tr_b16 a[48:49], v8 offset:16384\n"
    "ds_read_b64_tr_b16 a[50:51], v8 offset:17408\n"
    "ds_read_b64_tr_b16 a[52:53], v8 offset:20480\n"
    "ds_read_b64_tr_b16 a[54:55], v8 offset:21504\n"
    "ds_read_b64_tr_b16 a[56:57], v8 offset:24576\n"
    "ds_read_b64_tr_b16 a[58:59], v8 offset:25600\n"
    "ds_read_b64_tr_b16 a[60:61], v8 offset:28672\n"
    "ds_read_b64_tr_b16 a[62:63], v8 offset:29696\n"
    "s_waitcnt vmcnt(12)\n"
    "s_waitcnt lgkmcnt(0)\n"
    "s_barrier\n"
    "ds_read_b64_tr_b16 a[64:65], v8 offset:32768\n"
    "ds_read_b64_tr_b16 a[66:67], v8 offset:33792\n"
    "ds_read_b64_tr_b16 a[68:69], v8 offset:36864\n"
    "ds_read_b64_tr_b16 a[70:71], v8 offset:37888\n"
    "ds_read_b64_tr_b16 a[72:73], v8 offset:40960\n"
    "ds_read_b64_tr_b16 a[74:75], v8 offset:41984\n"
    "ds_read_b64_tr_b16 a[76:77], v8 offset:45056\n"
    "ds_read_b64_tr_b16 a[78:79], v8 offset:46080\n"
    "s_waitcnt vmcnt(8)\n"
    "s_waitcnt lgkmcnt(0)\n"
    "s_barrier\n"
    "ds_read_b64_tr_b16 a[80:81], v9 offset:0\n"
    "ds_read_b64_tr_b16 a[82:83], v9 offset:1024\n"
    "ds_read_b64_tr_b16 a[84:85], v9 offset:4096\n"
    "ds_read_b64_tr_b16 a[86:87], v9 offset:5120\n"
    "ds_read_b64_tr_b16 a[88:89], v9 offset:8192\n"
    "ds_read_b64_tr_b16 a[90:91], v9 offset:9216\n"
    "ds_read_b64_tr_b16 a[92:93], v9 offset:12288\n"
    "ds_read_b64_tr_b16 a[94:95], v9 offset:13312\n"
    "s_waitcnt vmcnt(4)\n"
    "s_waitcnt lgkmcnt(0)\n"
    "s_barrier\n"
    "ds_read_b64_tr_b16 a[96:97], v9 offset:16384\n"
    "ds_read_b64_tr_b16 a[98:99], v9 offset:17408\n"
    "ds_read_b64_tr_b16 a[100:101], v9 offset:20480\n"
    "ds_read_b64_tr_b16 a[102:103], v9 offset:21504\n"
    "ds_read_b64_tr_b16 a[104:105], v9 offset:24576\n"
    "ds_read_b64_tr_b16 a[106:107], v9 offset:25600\n"
    "ds_read_b64_tr_b16 a[108:109], v9 offset:28672\n"
    "ds_read_b64_tr_b16 a[110:111], v9 offset:29696\n"
    "s_waitcnt vmcnt(0)\n"
    "s_waitcnt lgkmcnt(0)\n"
    "s_barrier\n"
    "ds_read_b64_tr_b16 a[112:113], v9 offset:32768\n"
    "ds_read_b64_tr_b16 a[114:115], v9 offset:33792\n"
    "ds_read_b64_tr_b16 a[116:117], v9 offset:36864\n"
    "ds_read_b64_tr_b16 a[118:119], v9 offset:37888\n"
    "ds_read_b64_tr_b16 a[120:121], v9 offset:40960\n"
    "ds_read_b64_tr_b16 a[122:123], v9 offset:41984\n"
    "ds_read_b64_tr_b16 a[124:125], v9 offset:45056\n"
    "ds_read_b64_tr_b16 a[126:127], v9 offset:46080\n"
    "s_waitcnt lgkmcnt(0)\n"
    "s_waitcnt vmcnt(0)\n"
    "s_barrier\n"
    "s_mov_b32 s74, 0\n"
    "s_brev_b32 s71, 1\n"
    "s_mov_b32 s72, s71\n"
    "s_mov_b32 s73, s71\n"
    "s_mov_b32 s80, 0\n"
    "s_mov_b32 s82, 0x3e0293ee\n"
    "s_mov_b32 s83, 0x3e0293ee\n"
    "v_xor_b32 v6, 0x0, v0\n"
    "ds_read_b128 v[8:11], v6 offset:0\n"
    "ds_read_b128 v[12:15], v6 offset:256\n"
    "s_waitcnt lgkmcnt(0)\n"
    ".Lp1_loop_%=:\n"
    "s_waitcnt lgkmcnt(1)\n"
    "v_mfma_f32_32x32x16_bf16 v[48:63], a[128:131], v[8:11], 0\n"
    "v_xor_b32 v6, 0x20, v0\n"
    "ds_read_b128 v[16:19], v6 offset:0\n"
    "s_add_u32 s68, s74, 1\n"
    "s_min_u32 s68, s68, 63\n"
    "s_add_u32 s79, s68, s38\n"
    "s_mov_b32 s78, s79\n"
    "s_lshl_b32 s69, s78, 15\n"
    "s_add_u32 s69, s69, s77\n"
    "v_mfma_f32_32x32x16_bf16 v[64:79], v[8:11], v[128:131], 0\n"
    "v_mfma_f32_32x32x16_bf16 v[32:47], a[0:3], v[8:11], 0\n"
    "s_add_u32 m0, s67, 65536\n"
    "s_nop 0\n"
    "buffer_load_dwordx4 v1, s[56:59], s69 offen offset:0 lds\n"
    "s_waitcnt lgkmcnt(1)\n"
    "v_mfma_f32_32x32x16_bf16 v[48:63], a[160:163], v[12:15], v[48:63]\n"
    "ds_read_b128 v[20:23], v6 offset:256\n"
    "buffer_load_dwordx4 v2, s[56:59], s69 offen offset:1024 lds\n"
    "v_mfma_f32_32x32x16_bf16 v[64:79], v[12:15], v[160:163], v[64:79]\n"
    "v_mfma_f32_32x32x16_bf16 v[32:47], a[32:35], v[12:15], v[32:47]\n"
    "buffer_load_dwordx4 v3, s[56:59], s69 offen offset:2048 lds\n"
    "s_waitcnt lgkmcnt(1)\n"
    "v_mfma_f32_32x32x16_bf16 v[48:63], a[132:135], v[16:19], v[48:63]\n"
    "v_xor_b32 v6, 0x40, v0\n"
    "ds_read_b128 v[8:11], v6 offset:0\n"
    "buffer_load_dwordx4 v4, s[56:59], s69 offen offset:3072 lds\n"
    "v_mfma_f32_32x32x16_bf16 v[64:79], v[16:19], v[132:135], v[64:79]\n"
    "v_mfma_f32_32x32x16_bf16 v[32:47], a[4:7], v[16:19], v[32:47]\n"
    "s_waitcnt lgkmcnt(1)\n"
    "v_mfma_f32_32x32x16_bf16 v[48:63], a[164:167], v[20:23], v[48:63]\n"
    "ds_read_b128 v[12:15], v6 offset:256\n"
    "v_cvt_pk_bf16_f32 v96, v96, v97\n"
    "v_mfma_f32_32x32x16_bf16 v[64:79], v[20:23], v[164:167], v[64:79]\n"
    "v_cvt_pk_bf16_f32 v97, v98, v99\n"
    "v_cvt_pk_bf16_f32 v98, v100, v101\n"
    "v_cvt_pk_bf16_f32 v99, v102, v103\n"
    "v_mfma_f32_32x32x16_bf16 v[32:47], a[36:39], v[20:23], v[32:47]\n"
    "buffer_store_dwordx4 v[96:99], v5, s[40:43], s72 offen offset:0 sc1\n"
    "v_cvt_pk_bf16_f32 v104, v104, v105\n"
    "v_cvt_pk_bf16_f32 v105, v106, v107\n"
    "s_waitcnt lgkmcnt(1)\n"
    "v_mfma_f32_32x32x16_bf16 v[48:63], a[136:139], v[8:11], v[48:63]\n"
    "v_xor_b32 v6, 0x60, v0\n"
    "ds_read_b128 v[16:19], v6 offset:0\n"
    "v_cvt_pk_bf16_f32 v106, v108, v109\n"
    "v_mfma_f32_32x32x16_bf16 v[64:79], v[8:11], v[136:139], v[64:79]\n"
    "v_cvt_pk_bf16_f32 v107, v110, v111\n"
    "buffer_store_dwordx4 v[104:107], v5, s[40:43], s72 offen offset:1024 sc1\n"
    "v_cvt_pk_bf16_f32 v112, v112, v113\n"
    "v_mfma_f32_32x32x16_bf16 v[32:47], a[8:11], v[8:11], v[32:47]\n"
    "v_cvt_pk_bf16_f32 v113, v114, v115\n"
    "v_cvt_pk_bf16_f32 v114, v116, v117\n"
    "v_cvt_pk_bf16_f32 v115, v118, v119\n"
    "s_waitcnt lgkmcnt(1)\n"
    "v_mfma_f32_32x32x16_bf16 v[48:63], a[168:171], v[12:15], v[48:63]\n"
    "ds_read_b128 v[20:23], v6 offset:256\n"
    "buffer_store_dwordx4 v[112:115], v5, s[40:43], s73 offen offset:0 sc1\n"
    "v_mfma_f32_32x32x16_bf16 v[64:79], v[12:15], v[168:171], v[64:79]\n"
    "v_cvt_pk_bf16_f32 v120, v120, v121\n"
    "v_cvt_pk_bf16_f32 v121, v122, v123\n"
    "v_cvt_pk_bf16_f32 v122, v124, v125\n"
    "v_mfma_f32_32x32x16_bf16 v[32:47], a[40:43], v[12:15], v[32:47]\n"
    "v_cvt_pk_bf16_f32 v123, v126, v127\n"
    "buffer_store_dwordx4 v[120:123], v5, s[40:43], s73 offen offset:1024 sc1\n"
    "v_mul_f32 v80, s82, v80\n"
    "s_waitcnt lgkmcnt(1)\n"
    "v_mfma_f32_32x32x16_bf16 v[48:63], a[140:143], v[16:19], v[48:63]\n"
    "v_xor_b32 v6, 0x80, v0\n"
    "ds_read_b128 v[8:11], v6 offset:0\n"
    "v_mul_f32 v81, s82, v81\n"
    "v_mfma_f32_32x32x16_bf16 v[64:79], v[16:19], v[140:143], v[64:79]\n"
    "v_mul_f32 v82, s82, v82\n"
    "v_mul_f32 v83, s82, v83\n"
    "v_mul_f32 v84, s82, v84\n"
    "v_mfma_f32_32x32x16_bf16 v[32:47], a[12:15], v[16:19], v[32:47]\n"
    "v_mul_f32 v85, s82, v85\n"
    "v_mul_f32 v86, s82, v86\n"
    "v_mul_f32 v87, s82, v87\n"
    "s_waitcnt lgkmcnt(1)\n"
    "v_mfma_f32_32x32x16_bf16 v[48:63], a[172:175], v[20:23], v[48:63]\n"
    "ds_read_b128 v[12:15], v6 offset:256\n"
    "v_cvt_pk_bf16_f32 v80, v80, v81\n"
    "v_mfma_f32_32x32x16_bf16 v[64:79], v[20:23], v[172:175], v[64:79]\n"
    "v_cvt_pk_bf16_f32 v81, v82, v83\n"
    "v_cvt_pk_bf16_f32 v82, v84, v85\n"
    "v_cvt_pk_bf16_f32 v83, v86, v87\n"
    "v_mfma_f32_32x32x16_bf16 v[32:47], a[44:47], v[20:23], v[32:47]\n"
    "buffer_store_dwordx4 v[80:83], v5, s[40:43], s71 offen offset:0 sc1\n"
    "v_mul_f32 v88, s82, v88\n"
    "v_mul_f32 v89, s82, v89\n"
    "s_waitcnt lgkmcnt(1)\n"
    "v_mfma_f32_32x32x16_bf16 v[48:63], a[144:147], v[8:11], v[48:63]\n"
    "v_xor_b32 v6, 0xa0, v0\n"
    "ds_read_b128 v[16:19], v6 offset:0\n"
    "v_mul_f32 v90, s82, v90\n"
    "v_mfma_f32_32x32x16_bf16 v[64:79], v[8:11], v[144:147], v[64:79]\n"
    "v_mul_f32 v91, s82, v91\n"
    "v_mul_f32 v92, s82, v92\n"
    "v_mul_f32 v93, s82, v93\n"
    "v_mfma_f32_32x32x16_bf16 v[32:47], a[16:19], v[8:11], v[32:47]\n"
    "v_mul_f32 v94, s82, v94\n"
    "v_mul_f32 v95, s82, v95\n"
    "v_cvt_pk_bf16_f32 v88, v88, v89\n"
    "s_waitcnt lgkmcnt(1)\n"
    "v_mfma_f32_32x32x16_bf16 v[48:63], a[176:179], v[12:15], v[48:63]\n"
    "ds_read_b128 v[20:23], v6 offset:256\n"
    "v_cvt_pk_bf16_f32 v89, v90, v91\n"
    "v_mfma_f32_32x32x16_bf16 v[64:79], v[12:15], v[176:179], v[64:79]\n"
    "v_cvt_pk_bf16_f32 v90, v92, v93\n"
    "v_cvt_pk_bf16_f32 v91, v94, v95\n"
    "buffer_store_dwordx4 v[88:91], v5, s[40:43], s71 offen offset:1024 sc1\n"
    "v_mfma_f32_32x32x16_bf16 v[32:47], a[48:51], v[12:15], v[32:47]\n"
    "s_waitcnt lgkmcnt(1)\n"
    "v_mfma_f32_32x32x16_bf16 v[48:63], a[148:151], v[16:19], v[48:63]\n"
    "v_xor_b32 v6, 0xc0, v0\n"
    "ds_read_b128 v[8:11], v6 offset:0\n"
    "v_mfma_f32_32x32x16_bf16 v[64:79], v[16:19], v[148:151], v[64:79]\n"
    "v_mfma_f32_32x32x16_bf16 v[32:47], a[20:23], v[16:19], v[32:47]\n"
    "s_waitcnt lgkmcnt(1)\n"
    "v_mfma_f32_32x32x16_bf16 v[48:63], a[180:183], v[20:23], v[48:63]\n"
    "ds_read_b128 v[12:15], v6 offset:256\n"
    "v_mfma_f32_32x32x16_bf16 v[64:79], v[20:23], v[180:183], v[64:79]\n"
    "v_mfma_f32_32x32x16_bf16 v[32:47], a[52:55], v[20:23], v[32:47]\n"
    "s_waitcnt lgkmcnt(1)\n"
    "v_mfma_f32_32x32x16_bf16 v[48:63], a[152:155], v[8:11], v[48:63]\n"
    "v_xor_b32 v6, 0xe0, v0\n"
    "ds_read_b128 v[16:19], v6 offset:0\n"
    "v_mfma_f32_32x32x16_bf16 v[64:79], v[8:11], v[152:155], v[64:79]\n"
    "v_mfma_f32_32x32x16_bf16 v[32:47], a[24:27], v[8:11], v[32:47]\n"
    "s_waitcnt lgkmcnt(1)\n"
    "v_mfma_f32_32x32x16_bf16 v[48:63], a[184:187], v[12:15], v[48:63]\n"
    "ds_read_b128 v[20:23], v6 offset:256\n"
    "v_mfma_f32_32x32x16_bf16 v[64:79], v[12:15], v[184:187], v[64:79]\n"
    "v_mfma_f32_32x32x16_bf16 v[32:47], a[56:59], v[12:15], v[32:47]\n"
    "s_waitcnt vmcnt(11)\n"
    "s_barrier\n"
    "s_waitcnt lgkmcnt(1)\n"
    "v_mfma_f32_32x32x16_bf16 v[48:63], a[156:159], v[16:19], v[48:63]\n"
    "v_xor_b32 v6, 0x0, v0\n"
    "ds_read_b128 v[8:11], v6 offset:32768\n"
    "v_mfma_f32_32x32x16_bf16 v[64:79], v[16:19], v[156:159], v[64:79]\n"
    "v_mfma_f32_32x32x16_bf16 v[32:47], a[28:31], v[16:19], v[32:47]\n"
    "s_waitcnt lgkmcnt(1)\n"
    "v_mfma_f32_32x32x16_bf16 v[48:63], a[188:191], v[20:23], v[48:63]\n"
    "ds_read_b128 v[12:15], v6 offset:33024\n"
    "v_mfma_f32_32x32x16_bf16 v[64:79], v[20:23], v[188:191], v[64:79]\n"
    "v_mfma_f32_32x32x16_bf16 v[32:47], a[60:63], v[20:23], v[32:47]\n"
    "s_waitcnt lgkmcnt(1)\n"
    "v_mfma_f32_32x32x16_bf16 v[48:63], a[192:195], v[8:11], v[48:63]\n"
    "v_xor_b32 v6, 0x20, v0\n"
    "ds_read_b128 v[16:19], v6 offset:32768\n"
    "v_mfma_f32_32x32x16_bf16 v[64:79], v[8:11], v[192:195], v[64:79]\n"
    "v_mov_b32 v24, s74\n"
    "v_mfma_f32_32x32x16_bf16 v[32:47], a[64:67], v[8:11], v[32:47]\n"
    "s_add_u32 m0, s67, 98304\n"
    "s_add_u32 s69, s69, 512\n"
    "buffer_load_dwordx4 v1, s[56:59], s69 offen offset:0 lds\n"
    "s_waitcnt lgkmcnt(1)\n"
    "v_mfma_f32_32x32x16_bf16 v[48:63], a[224:227], v[12:15], v[48:63]\n"
    "ds_read_b128 v[20:23], v6 offset:33024\n"
    "buffer_load_dwordx4 v2, s[56:59], s69 offen offset:1024 lds\n"
    "v_mfma_f32_32x32x16_bf16 v[64:79], v[12:15], v[224:227], v[64:79]\n"
    "v_mfma_f32_32x32x16_bf16 v[32:47], a[96:99], v[12:15], v[32:47]\n"
    "buffer_load_dwordx4 v3, s[56:59], s69 offen offset:2048 lds\n"
    "s_waitcnt lgkmcnt(1)\n"
    "v_mfma_f32_32x32x16_bf16 v[48:63], a[196:199], v[16:19], v[48:63]\n"
    "v_xor_b32 v6, 0x40, v0\n"
    "ds_read_b128 v[8:11], v6 offset:32768\n"
    "buffer_load_dwordx4 v4, s[56:59], s69 offen offset:3072 lds\n"
    "v_mfma_f32_32x32x16_bf16 v[64:79], v[16:19], v[196:199], v[64:79]\n"
    "v_mfma_f32_32x32x16_bf16 v[32:47], a[68:71], v[16:19], v[32:47]\n"
    "s_waitcnt lgkmcnt(1)\n"
    "v_mfma_f32_32x32x16_bf16 v[48:63], a[228:231], v[20:23], v[48:63]\n"
    "ds_read_b128 v[12:15], v6 offset:33024\n"
    "v_mfma_f32_32x32x16_bf16 v[64:79], v[20:23], v[228:231], v[64:79]\n"
    "v_mfma_f32_32x32x16_bf16 v[32:47], a[100:103], v[20:23], v[32:47]\n"
    "s_waitcnt lgkmcnt(1)\n"
    "v_mfma_f32_32x32x16_bf16 v[48:63], a[200:203], v[8:11], v[48:63]\n"
    "v_xor_b32 v6, 0x60, v0\n"
    "ds_read_b128 v[16:19], v6 offset:32768\n"
    "v_mfma_f32_32x32x16_bf16 v[64:79], v[8:11], v[200:203], v[64:79]\n"
    "v_mfma_f32_32x32x16_bf16 v[32:47], a[72:75], v[8:11], v[32:47]\n"
    "s_waitcnt lgkmcnt(1)\n"
    "v_mfma_f32_32x32x16_bf16 v[48:63], a[232:235], v[12:15], v[48:63]\n"
    "ds_read_b128 v[20:23], v6 offset:33024\n"
    "v_mfma_f32_32x32x16_bf16 v[64:79], v[12:15], v[232:235], v[64:79]\n"
    "v_mfma_f32_32x32x16_bf16 v[32:47], a[104:107], v[12:15], v[32:47]\n"
    "s_waitcnt lgkmcnt(1)\n"
    "v_mfma_f32_32x32x16_bf16 v[48:63], a[204:207], v[16:19], v[48:63]\n"
    "v_xor_b32 v6, 0x80, v0\n"
    "ds_read_b128 v[8:11], v6 offset:32768\n"
    "v_mfma_f32_32x32x16_bf16 v[64:79], v[16:19], v[204:207], v[64:79]\n"
    "v_mfma_f32_32x32x16_bf16 v[32:47], a[76:79], v[16:19], v[32:47]\n"
    "s_waitcnt lgkmcnt(1)\n"
    "v_mfma_f32_32x32x16_bf16 v[48:63], a[236:239], v[20:23], v[48:63]\n"
    "ds_read_b128 v[12:15], v6 offset:33024\n"
    "v_mfma_f32_32x32x16_bf16 v[64:79], v[20:23], v[236:239], v[64:79]\n"
    "v_mfma_f32_32x32x16_bf16 v[32:47], a[108:111], v[20:23], v[32:47]\n"
    "s_waitcnt lgkmcnt(1)\n"
    "v_mfma_f32_32x32x16_bf16 v[48:63], a[208:211], v[8:11], v[48:63]\n"
    "v_xor_b32 v6, 0xa0, v0\n"
    "ds_read_b128 v[16:19], v6 offset:32768\n"
    "v_mfma_f32_32x32x16_bf16 v[64:79], v[8:11], v[208:211], v[64:79]\n"
    "v_mfma_f32_32x32x16_bf16 v[32:47], a[80:83], v[8:11], v[32:47]\n"
    "s_waitcnt lgkmcnt(1)\n"
    "v_mfma_f32_32x32x16_bf16 v[48:63], a[240:243], v[12:15], v[48:63]\n"
    "ds_read_b128 v[20:23], v6 offset:33024\n"
    "v_mfma_f32_32x32x16_bf16 v[64:79], v[12:15], v[240:243], v[64:79]\n"
    "v_mfma_f32_32x32x16_bf16 v[32:47], a[112:115], v[12:15], v[32:47]\n"
    "s_lshl_b32 s68, s37, 6\n"
    "s_add_u32 s72, s68, s75\n"
    "s_add_u32 s71, s68, s76\n"
    "s_lshl_b32 s72, s72, 13\n"
    "s_lshl_b32 s71, s71, 13\n"
    "s_waitcnt lgkmcnt(1)\n"
    "v_mfma_f32_32x32x16_bf16 v[48:63], a[212:215], v[16:19], v[48:63]\n"
    "v_xor_b32 v6, 0xc0, v0\n"
    "ds_read_b128 v[8:11], v6 offset:32768\n"
    "v_mfma_f32_32x32x16_bf16 v[64:79], v[16:19], v[212:215], v[64:79]\n"
    "v_mfma_f32_32x32x16_bf16 v[32:47], a[84:87], v[16:19], v[32:47]\n"
    "s_add_u32 s71, s71, 0x800000\n"
    "s_add_u32 s72, s72, 0x1800000\n"
    "s_add_u32 s73, s72, 0x1000000\n"
    "s_mov_b32 s75, s79\n"
    "s_mov_b32 s76, s78\n"
    "s_add_u32 s74, s74, 1\n"
    "s_waitcnt lgkmcnt(1)\n"
    "v_mfma_f32_32x32x16_bf16 v[48:63], a[244:247], v[20:23], v[48:63]\n"
    "ds_read_b128 v[12:15], v6 offset:33024\n"
    "v_mfma_f32_32x32x16_bf16 v[64:79], v[20:23], v[244:247], v[64:79]\n"
    "v_mfma_f32_32x32x16_bf16 v[32:47], a[116:119], v[20:23], v[32:47]\n"
    "s_waitcnt lgkmcnt(1)\n"
    "v_mfma_f32_32x32x16_bf16 v[48:63], a[216:219], v[8:11], v[48:63]\n"
    "v_xor_b32 v6, 0xe0, v0\n"
    "ds_read_b128 v[16:19], v6 offset:32768\n"
    "v_mfma_f32_32x32x16_bf16 v[64:79], v[8:11], v[216:219], v[64:79]\n"
    "v_mfma_f32_32x32x16_bf16 v[32:47], a[88:91], v[8:11], v[32:47]\n"
    "s_waitcnt vmcnt(4)\n"
    "buffer_store_dword v24, off, s[40:43], s65 sc1\n"
    "s_waitcnt lgkmcnt(1)\n"
    "v_mfma_f32_32x32x16_bf16 v[48:63], a[248:251], v[12:15], v[48:63]\n"
    "ds_read_b128 v[20:23], v6 offset:33024\n"
    "v_mfma_f32_32x32x16_bf16 v[64:79], v[12:15], v[248:251], v[64:79]\n"
    "v_mfma_f32_32x32x16_bf16 v[32:47], a[120:123], v[12:15], v[32:47]\n"
    "s_barrier\n"
    "s_waitcnt lgkmcnt(1)\n"
    "v_mfma_f32_32x32x16_bf16 v[48:63], a[220:223], v[16:19], v[48:63]\n"
    "v_xor_b32 v6, 0x10000, v0\n"
    "ds_read_b128 v[8:11], v6 offset:0\n"
    "v_mfma_f32_32x32x16_bf16 v[64:79], v[16:19], v[220:223], v[64:79]\n"
    "v_mfma_f32_32x32x16_bf16 v[32:47], a[92:95], v[16:19], v[32:47]\n"
    "s_waitcnt lgkmcnt(1)\n"
    "v_mfma_f32_32x32x16_bf16 v[48:63], a[252:255], v[20:23], v[48:63]\n"
    "ds_read_b128 v[12:15], v6 offset:256\n"
    "v_mfma_f32_32x32x16_bf16 v[64:79], v[20:23], v[252:255], v[64:79]\n"
    "v_mfma_f32_32x32x16_bf16 v[32:47], a[124:127], v[20:23], v[32:47]\n"
    "s_waitcnt lgkmcnt(1)\n"
    "v_mfma_f32_32x32x16_bf16 v[96:111], a[128:131], v[8:11], 0\n"
    "v_xor_b32 v6, 0x10020, v0\n"
    "ds_read_b128 v[16:19], v6 offset:0\n"
    "s_add_u32 s68, s74, 1\n"
    "s_min_u32 s68, s68, 63\n"
    "s_add_u32 s79, s68, s38\n"
    "s_mov_b32 s78, s79\n"
    "s_lshl_b32 s69, s78, 15\n"
    "s_add_u32 s69, s69, s77\n"
    "v_mfma_f32_32x32x16_bf16 v[112:127], v[8:11], v[128:131], 0\n"
    "v_mfma_f32_32x32x16_bf16 v[80:95], a[0:3], v[8:11], 0\n"
    "s_add_u32 m0, s67, 0\n"
    "s_nop 0\n"
    "buffer_load_dwordx4 v1, s[56:59], s69 offen offset:0 lds\n"
    "s_waitcnt lgkmcnt(1)\n"
    "v_mfma_f32_32x32x16_bf16 v[96:111], a[160:163], v[12:15], v[96:111]\n"
    "ds_read_b128 v[20:23], v6 offset:256\n"
    "buffer_load_dwordx4 v2, s[56:59], s69 offen offset:1024 lds\n"
    "v_mfma_f32_32x32x16_bf16 v[112:127], v[12:15], v[160:163], v[112:127]\n"
    "v_mfma_f32_32x32x16_bf16 v[80:95], a[32:35], v[12:15], v[80:95]\n"
    "buffer_load_dwordx4 v3, s[56:59], s69 offen offset:2048 lds\n"
    "s_waitcnt lgkmcnt(1)\n"
    "v_mfma_f32_32x32x16_bf16 v[96:111], a[132:135], v[16:19], v[96:111]\n"
    "v_xor_b32 v6, 0x10040, v0\n"
    "ds_read_b128 v[8:11], v6 offset:0\n"
    "buffer_load_dwordx4 v4, s[56:59], s69 offen offset:3072 lds\n"
    "v_mfma_f32_32x32x16_bf16 v[112:127], v[16:19], v[132:135], v[112:127]\n"
    "v_mfma_f32_32x32x16_bf16 v[80:95], a[4:7], v[16:19], v[80:95]\n"
    "s_waitcnt lgkmcnt(1)\n"
    "v_mfma_f32_32x32x16_bf16 v[96:111], a[164:167], v[20:23], v[96:111]\n"
    "ds_read_b128 v[12:15], v6 offset:256\n"
    "v_cvt_pk_bf16_f32 v48, v48, v49\n"
    "v_mfma_f32_32x32x16_bf16 v[112:127], v[20:23], v[164:167], v[112:127]\n"
    "v_cvt_pk_bf16_f32 v49, v50, v51\n"
    "v_cvt_pk_bf16_f32 v50, v52, v53\n"
    "v_cvt_pk_bf16_f32 v51, v54, v55\n"
    "v_mfma_f32_32x32x16_bf16 v[80:95], a[36:39], v[20:23], v[80:95]\n"
    "buffer_store_dwordx4 v[48:51], v5, s[40:43], s72 offen offset:0 sc1\n"
    "v_cvt_pk_bf16_f32 v56, v56, v57\n"
    "v_cvt_pk_bf16_f32 v57, v58, v59\n"
    "s_waitcnt lgkmcnt(1)\n"
    "v_mfma_f32_32x32x16_bf16 v[96:111], a[136:139], v[8:11], v[96:111]\n"
    "v_xor_b32 v6, 0x10060, v0\n"
    "ds_read_b128 v[16:19], v6 offset:0\n"
    "v_cvt_pk_bf16_f32 v58, v60, v61\n"
    "v_mfma_f32_32x32x16_bf16 v[112:127], v[8:11], v[136:139], v[112:127]\n"
    "v_cvt_pk_bf16_f32 v59, v62, v63\n"
    "buffer_store_dwordx4 v[56:59], v5, s[40:43], s72 offen offset:1024 sc1\n"
    "v_cvt_pk_bf16_f32 v64, v64, v65\n"
    "v_mfma_f32_32x32x16_bf16 v[80:95], a[8:11], v[8:11], v[80:95]\n"
    "v_cvt_pk_bf16_f32 v65, v66, v67\n"
    "v_cvt_pk_bf16_f32 v66, v68, v69\n"
    "v_cvt_pk_bf16_f32 v67, v70, v71\n"
    "s_waitcnt lgkmcnt(1)\n"
    "v_mfma_f32_32x32x16_bf16 v[96:111], a[168:171], v[12:15], v[96:111]\n"
    "ds_read_b128 v[20:23], v6 offset:256\n"
    "buffer_store_dwordx4 v[64:67], v5, s[40:43], s73 offen offset:0 sc1\n"
    "v_mfma_f32_32x32x16_bf16 v[112:127], v[12:15], v[168:171], v[112:127]\n"
    "v_cvt_pk_bf16_f32 v72, v72, v73\n"
    "v_cvt_pk_bf16_f32 v73, v74, v75\n"
    "v_cvt_pk_bf16_f32 v74, v76, v77\n"
    "v_mfma_f32_32x32x16_bf16 v[80:95], a[40:43], v[12:15], v[80:95]\n"
    "v_cvt_pk_bf16_f32 v75, v78, v79\n"
    "buffer_store_dwordx4 v[72:75], v5, s[40:43], s73 offen offset:1024 sc1\n"
    "v_mul_f32 v32, s82, v32\n"
    "s_waitcnt lgkmcnt(1)\n"
    "v_mfma_f32_32x32x16_bf16 v[96:111], a[140:143], v[16:19], v[96:111]\n"
    "v_xor_b32 v6, 0x10080, v0\n"
    "ds_read_b128 v[8:11], v6 offset:0\n"
    "v_mul_f32 v33, s82, v33\n"
    "v_mfma_f32_32x32x16_bf16 v[112:127], v[16:19], v[140:143], v[112:127]\n"
    "v_mul_f32 v34, s82, v34\n"
    "v_mul_f32 v35, s82, v35\n"
    "v_mul_f32 v36, s82, v36\n"
    "v_mfma_f32_32x32x16_bf16 v[80:95], a[12:15], v[16:19], v[80:95]\n"
    "v_mul_f32 v37, s82, v37\n"
    "v_mul_f32 v38, s82, v38\n"
    "v_mul_f32 v39, s82, v39\n"
    "s_waitcnt lgkmcnt(1)\n"
    "v_mfma_f32_32x32x16_bf16 v[96:111], a[172:175], v[20:23], v[96:111]\n"
    "ds_read_b128 v[12:15], v6 offset:256\n"
    "v_cvt_pk_bf16_f32 v32, v32, v33\n"
    "v_mfma_f32_32x32x16_bf16 v[112:127], v[20:23], v[172:175], v[112:127]\n"
    "v_cvt_pk_bf16_f32 v33, v34, v35\n"
    "v_cvt_pk_bf16_f32 v34, v36, v37\n"
    "v_cvt_pk_bf16_f32 v35, v38, v39\n"
    "v_mfma_f32_32x32x16_bf16 v[80:95], a[44:47], v[20:23], v[80:95]\n"
    "buffer_store_dwordx4 v[32:35], v5, s[40:43], s71 offen offset:0 sc1\n"
    "v_mul_f32 v40, s82, v40\n"
    "v_mul_f32 v41, s82, v41\n"
    "s_waitcnt lgkmcnt(1)\n"
    "v_mfma_f32_32x32x16_bf16 v[96:111], a[144:147], v[8:11], v[96:111]\n"
    "v_xor_b32 v6, 0x100a0, v0\n"
    "ds_read_b128 v[16:19], v6 offset:0\n"
    "v_mul_f32 v42, s82, v42\n"
    "v_mfma_f32_32x32x16_bf16 v[112:127], v[8:11], v[144:147], v[112:127]\n"
    "v_mul_f32 v43, s82, v43\n"
    "v_mul_f32 v44, s82, v44\n"
    "v_mul_f32 v45, s82, v45\n"
    "v_mfma_f32_32x32x16_bf16 v[80:95], a[16:19], v[8:11], v[80:95]\n"
    "v_mul_f32 v46, s82, v46\n"
    "v_mul_f32 v47, s82, v47\n"
    "v_cvt_pk_bf16_f32 v40, v40, v41\n"
    "s_waitcnt lgkmcnt(1)\n"
    "v_mfma_f32_32x32x16_bf16 v[96:111], a[176:179], v[12:15], v[96:111]\n"
    "ds_read_b128 v[20:23], v6 offset:256\n"
    "v_cvt_pk_bf16_f32 v41, v42, v43\n"
    "v_mfma_f32_32x32x16_bf16 v[112:127], v[12:15], v[176:179], v[112:127]\n"
    "v_cvt_pk_bf16_f32 v42, v44, v45\n"
    "v_cvt_pk_bf16_f32 v43, v46, v47\n"
    "buffer_store_dwordx4 v[40:43], v5, s[40:43], s71 offen offset:1024 sc1\n"
    "v_mfma_f32_32x32x16_bf16 v[80:95], a[48:51], v[12:15], v[80:95]\n"
    "s_waitcnt lgkmcnt(1)\n"
    "v_mfma_f32_32x32x16_bf16 v[96:111], a[148:151], v[16:19], v[96:111]\n"
    "v_xor_b32 v6, 0x100c0, v0\n"
    "ds_read_b128 v[8:11], v6 offset:0\n"
    "v_mfma_f32_32x32x16_bf16 v[112:127], v[16:19], v[148:151], v[112:127]\n"
    "v_mfma_f32_32x32x16_bf16 v[80:95], a[20:23], v[16:19], v[80:95]\n"
    "s_waitcnt lgkmcnt(1)\n"
    "v_mfma_f32_32x32x16_bf16 v[96:111], a[180:183], v[20:23], v[96:111]\n"
    "ds_read_b128 v[12:15], v6 offset:256\n"
    "v_mfma_f32_32x32x16_bf16 v[112:127], v[20:23], v[180:183], v[112:127]\n"
    "v_mfma_f32_32x32x16_bf16 v[80:95], a[52:55], v[20:23], v[80:95]\n"
    "s_waitcnt lgkmcnt(1)\n"
    "v_mfma_f32_32x32x16_bf16 v[96:111], a[152:155], v[8:11], v[96:111]\n"
    "v_xor_b32 v6, 0x100e0, v0\n"
    "ds_read_b128 v[16:19], v6 offset:0\n"
    "v_mfma_f32_32x32x16_bf16 v[112:127], v[8:11], v[152:155], v[112:127]\n"
    "v_mfma_f32_32x32x16_bf16 v[80:95], a[24:27], v[8:11], v[80:95]\n"
    "s_waitcnt lgkmcnt(1)\n"
    "v_mfma_f32_32x32x16_bf16 v[96:111], a[184:187], v[12:15], v[96:111]\n"
    "ds_read_b128 v[20:23], v6 offset:256\n"
    "v_mfma_f32_32x32x16_bf16 v[112:127], v[12:15], v[184:187], v[112:127]\n"
    "v_mfma_f32_32x32x16_bf16 v[80:95], a[56:59], v[12:15], v[80:95]\n"
    "s_waitcnt vmcnt(11)\n"
    "s_barrier\n"
    "s_waitcnt lgkmcnt(1)\n"
    "v_mfma_f32_32x32x16_bf16 v[96:111], a[156:159], v[16:19], v[96:111]\n"
    "v_xor_b32 v6, 0x10000, v0\n"
    "ds_read_b128 v[8:11], v6 offset:32768\n"
    "v_mfma_f32_32x32x16_bf16 v[112:127], v[16:19], v[156:159], v[112:127]\n"
    "v_mfma_f32_32x32x16_bf16 v[80:95], a[28:31], v[16:19], v[80:95]\n"
    "s_waitcnt lgkmcnt(1)\n"
    "v_mfma_f32_32x32x16_bf16 v[96:111], a[188:191], v[20:23], v[96:111]\n"
    "ds_read_b128 v[12:15], v6 offset:33024\n"
    "v_mfma_f32_32x32x16_bf16 v[112:127], v[20:23], v[188:191], v[112:127]\n"
    "v_mfma_f32_32x32x16_bf16 v[80:95], a[60:63], v[20:23], v[80:95]\n"
    "s_waitcnt lgkmcnt(1)\n"
    "v_mfma_f32_32x32x16_bf16 v[96:111], a[192:195], v[8:11], v[96:111]\n"
    "v_xor_b32 v6, 0x10020, v0\n"
    "ds_read_b128 v[16:19], v6 offset:32768\n"
    "v_mfma_f32_32x32x16_bf16 v[112:127], v[8:11], v[192:195], v[112:127]\n"
    "v_mov_b32 v24, s74\n"
    "v_mfma_f32_32x32x16_bf16 v[80:95], a[64:67], v[8:11], v[80:95]\n"
    "s_add_u32 m0, s67, 32768\n"
    "s_add_u32 s69, s69, 512\n"
    "buffer_load_dwordx4 v1, s[56:59], s69 offen offset:0 lds\n"
    "s_waitcnt lgkmcnt(1)\n"
    "v_mfma_f32_32x32x16_bf16 v[96:111], a[224:227], v[12:15], v[96:111]\n"
    "ds_read_b128 v[20:23], v6 offset:33024\n"
    "buffer_load_dwordx4 v2, s[56:59], s69 offen offset:1024 lds\n"
    "v_mfma_f32_32x32x16_bf16 v[112:127], v[12:15], v[224:227], v[112:127]\n"
    "v_mfma_f32_32x32x16_bf16 v[80:95], a[96:99], v[12:15], v[80:95]\n"
    "buffer_load_dwordx4 v3, s[56:59], s69 offen offset:2048 lds\n"
    "s_waitcnt lgkmcnt(1)\n"
    "v_mfma_f32_32x32x16_bf16 v[96:111], a[196:199], v[16:19], v[96:111]\n"
    "v_xor_b32 v6, 0x10040, v0\n"
    "ds_read_b128 v[8:11], v6 offset:32768\n"
    "buffer_load_dwordx4 v4, s[56:59], s69 offen offset:3072 lds\n"
    "v_mfma_f32_32x32x16_bf16 v[112:127], v[16:19], v[196:199], v[112:127]\n"
    "v_mfma_f32_32x32x16_bf16 v[80:95], a[68:71], v[16:19], v[80:95]\n"
    "s_waitcnt lgkmcnt(1)\n"
    "v_mfma_f32_32x32x16_bf16 v[96:111], a[228:231], v[20:23], v[96:111]\n"
    "ds_read_b128 v[12:15], v6 offset:33024\n"
    "v_mfma_f32_32x32x16_bf16 v[112:127], v[20:23], v[228:231], v[112:127]\n"
    "v_mfma_f32_32x32x16_bf16 v[80:95], a[100:103], v[20:23], v[80:95]\n"
    "s_waitcnt lgkmcnt(1)\n"
    "v_mfma_f32_32x32x16_bf16 v[96:111], a[200:203], v[8:11], v[96:111]\n"
    "v_xor_b32 v6, 0x10060, v0\n"
    "ds_read_b128 v[16:19], v6 offset:32768\n"
    "v_mfma_f32_32x32x16_bf16 v[112:127], v[8:11], v[200:203], v[112:127]\n"
    "v_mfma_f32_32x32x16_bf16 v[80:95], a[72:75], v[8:11], v[80:95]\n"
    "s_waitcnt lgkmcnt(1)\n"
    "v_mfma_f32_32x32x16_bf16 v[96:111], a[232:235], v[12:15], v[96:111]\n"
    "ds_read_b128 v[20:23], v6 offset:33024\n"
    "v_mfma_f32_32x32x16_bf16 v[112:127], v[12:15], v[232:235], v[112:127]\n"
    "v_mfma_f32_32x32x16_bf16 v[80:95], a[104:107], v[12:15], v[80:95]\n"
    "s_waitcnt lgkmcnt(1)\n"
    "v_mfma_f32_32x32x16_bf16 v[96:111], a[204:207], v[16:19], v[96:111]\n"
    "v_xor_b32 v6, 0x10080, v0\n"
    "ds_read_b128 v[8:11], v6 offset:32768\n"
    "v_mfma_f32_32x32x16_bf16 v[112:127], v[16:19], v[204:207], v[112:127]\n"
    "v_mfma_f32_32x32x16_bf16 v[80:95], a[76:79], v[16:19], v[80:95]\n"
    "s_waitcnt lgkmcnt(1)\n"
    "v_mfma_f32_32x32x16_bf16 v[96:111], a[236:239], v[20:23], v[96:111]\n"
    "ds_read_b128 v[12:15], v6 offset:33024\n"
    "v_mfma_f32_32x32x16_bf16 v[112:127], v[20:23], v[236:239], v[112:127]\n"
    "v_mfma_f32_32x32x16_bf16 v[80:95], a[108:111], v[20:23], v[80:95]\n"
    "s_waitcnt lgkmcnt(1)\n"
    "v_mfma_f32_32x32x16_bf16 v[96:111], a[208:211], v[8:11], v[96:111]\n"
    "v_xor_b32 v6, 0x100a0, v0\n"
    "ds_read_b128 v[16:19], v6 offset:32768\n"
    "v_mfma_f32_32x32x16_bf16 v[112:127], v[8:11], v[208:211], v[112:127]\n"
    "v_mfma_f32_32x32x16_bf16 v[80:95], a[80:83], v[8:11], v[80:95]\n"
    "s_waitcnt lgkmcnt(1)\n"
    "v_mfma_f32_32x32x16_bf16 v[96:111], a[240:243], v[12:15], v[96:111]\n"
    "ds_read_b128 v[20:23], v6 offset:33024\n"
    "v_mfma_f32_32x32x16_bf16 v[112:127], v[12:15], v[240:243], v[112:127]\n"
    "v_mfma_f32_32x32x16_bf16 v[80:95], a[112:115], v[12:15], v[80:95]\n"
    "s_lshl_b32 s68, s37, 6\n"
    "s_add_u32 s72, s68, s75\n"
    "s_add_u32 s71, s68, s76\n"
    "s_lshl_b32 s72, s72, 13\n"
    "s_lshl_b32 s71, s71, 13\n"
    "s_waitcnt lgkmcnt(1)\n"
    "v_mfma_f32_32x32x16_bf16 v[96:111], a[212:215], v[16:19], v[96:111]\n"
    "v_xor_b32 v6, 0x100c0, v0\n"
    "ds_read_b128 v[8:11], v6 offset:32768\n"
    "v_mfma_f32_32x32x16_bf16 v[112:127], v[16:19], v[212:215], v[112:127]\n"
    "v_mfma_f32_32x32x16_bf16 v[80:95], a[84:87], v[16:19], v[80:95]\n"
    "s_add_u32 s71, s71, 0x800000\n"
    "s_add_u32 s72, s72, 0x1800000\n"
    "s_add_u32 s73, s72, 0x1000000\n"
    "s_mov_b32 s75, s79\n"
    "s_mov_b32 s76, s78\n"
    "s_add_u32 s74, s74, 1\n"
    "s_waitcnt lgkmcnt(1)\n"
    "v_mfma_f32_32x32x16_bf16 v[96:111], a[244:247], v[20:23], v[96:111]\n"
    "ds_read_b128 v[12:15], v6 offset:33024\n"
    "v_mfma_f32_32x32x16_bf16 v[112:127], v[20:23], v[244:247], v[112:127]\n"
    "v_mfma_f32_32x32x16_bf16 v[80:95], a[116:119], v[20:23], v[80:95]\n"
    "s_waitcnt lgkmcnt(1)\n"
    "v_mfma_f32_32x32x16_bf16 v[96:111], a[216:219], v[8:11], v[96:111]\n"
    "v_xor_b32 v6, 0x100e0, v0\n"
    "ds_read_b128 v[16:19], v6 offset:32768\n"
    "v_mfma_f32_32x32x16_bf16 v[112:127], v[8:11], v[216:219], v[112:127]\n"
    "v_mfma_f32_32x32x16_bf16 v[80:95], a[88:91], v[8:11], v[80:95]\n"
    "s_waitcnt vmcnt(4)\n"
    "buffer_store_dword v24, off, s[40:43], s65 sc1\n"
    "s_waitcnt lgkmcnt(1)\n"
    "v_mfma_f32_32x32x16_bf16 v[96:111], a[248:251], v[12:15], v[96:111]\n"
    "ds_read_b128 v[20:23], v6 offset:33024\n"
    "v_mfma_f32_32x32x16_bf16 v[112:127], v[12:15], v[248:251], v[112:127]\n"
    "v_mfma_f32_32x32x16_bf16 v[80:95], a[120:123], v[12:15], v[80:95]\n"
    "s_barrier\n"
    "s_waitcnt lgkmcnt(1)\n"
    "v_mfma_f32_32x32x16_bf16 v[96:111], a[220:223], v[16:19], v[96:111]\n"
    "v_xor_b32 v6, 0x0, v0\n"
    "ds_read_b128 v[8:11], v6 offset:0\n"
    "v_mfma_f32_32x32x16_bf16 v[112:127], v[16:19], v[220:223], v[112:127]\n"
    "v_mfma_f32_32x32x16_bf16 v[80:95], a[92:95], v[16:19], v[80:95]\n"
    "s_waitcnt lgkmcnt(1)\n"
    "v_mfma_f32_32x32x16_bf16 v[96:111], a[252:255], v[20:23], v[96:111]\n"
    "ds_read_b128 v[12:15], v6 offset:256\n"
    "v_mfma_f32_32x32x16_bf16 v[112:127], v[20:23], v[252:255], v[112:127]\n"
    "v_mfma_f32_32x32x16_bf16 v[80:95], a[124:127], v[20:23], v[80:95]\n"
    "s_cmp_lt_u32 s74, 64\n"
    "s_cbranch_scc1 .Lp1_loop_%=\n"
    "s_nop 11\n"
    "v_cvt_pk_bf16_f32 v96, v96, v97\n"
    "v_cvt_pk_bf16_f32 v97, v98, v99\n"
    "v_cvt_pk_bf16_f32 v98, v100, v101\n"
    "v_cvt_pk_bf16_f32 v99, v102, v103\n"
    "buffer_store_dwordx4 v[96:99], v5, s[40:43], s72 offen offset:0 sc1\n"
    "v_cvt_pk_bf16_f32 v104, v104, v105\n"
    "v_cvt_pk_bf16_f32 v105, v106, v107\n"
    "v_cvt_pk_bf16_f32 v106, v108, v109\n"
    "v_cvt_pk_bf16_f32 v107, v110, v111\n"
    "buffer_store_dwordx4 v[104:107], v5, s[40:43], s72 offen offset:1024 sc1\n"
    "v_cvt_pk_bf16_f32 v112, v112, v113\n"
    "v_cvt_pk_bf16_f32 v113, v114, v115\n"
    "v_cvt_pk_bf16_f32 v114, v116, v117\n"
    "v_cvt_pk_bf16_f32 v115, v118, v119\n"
    "buffer_store_dwordx4 v[112:115], v5, s[40:43], s73 offen offset:0 sc1\n"
    "v_cvt_pk_bf16_f32 v120, v120, v121\n"
    "v_cvt_pk_bf16_f32 v121, v122, v123\n"
    "v_cvt_pk_bf16_f32 v122, v124, v125\n"
    "v_cvt_pk_bf16_f32 v123, v126, v127\n"
    "buffer_store_dwordx4 v[120:123], v5, s[40:43], s73 offen offset:1024 sc1\n"
    "v_mul_f32 v80, s82, v80\n"
    "v_mul_f32 v81, s82, v81\n"
    "v_mul_f32 v82, s82, v82\n"
    "v_mul_f32 v83, s82, v83\n"
    "v_mul_f32 v84, s82, v84\n"
    "v_mul_f32 v85, s82, v85\n"
    "v_mul_f32 v86, s82, v86\n"
    "v_mul_f32 v87, s82, v87\n"
    "v_cvt_pk_bf16_f32 v80, v80, v81\n"
    "v_cvt_pk_bf16_f32 v81, v82, v83\n"
    "v_cvt_pk_bf16_f32 v82, v84, v85\n"
    "v_cvt_pk_bf16_f32 v83, v86, v87\n"
    "buffer_store_dwordx4 v[80:83], v5, s[40:43], s71 offen offset:0 sc1\n"
    "v_mul_f32 v88, s82, v88\n"
    "v_mul_f32 v89, s82, v89\n"
    "v_mul_f32 v90, s82, v90\n"
    "v_mul_f32 v91, s82, v91\n"
    "v_mul_f32 v92, s82, v92\n"
    "v_mul_f32 v93, s82, v93\n"
    "v_mul_f32 v94, s82, v94\n"
    "v_mul_f32 v95, s82, v95\n"
    "v_cvt_pk_bf16_f32 v88, v88, v89\n"
    "v_cvt_pk_bf16_f32 v89, v90, v91\n"
    "v_cvt_pk_bf16_f32 v90, v92, v93\n"
    "v_cvt_pk_bf16_f32 v91, v94, v95\n"
    "buffer_store_dwordx4 v[88:91], v5, s[40:43], s71 offen offset:1024 sc1\n"
    "s_waitcnt vmcnt(0) lgkmcnt(0)\n"
    "v_mov_b32 v7, 64\n"
    "buffer_store_dword v7, off, s[40:43], s65 sc1\n"
    PROJ_OPS);
    else asm volatile(
    "s_lshl_b32 s77, s36, 13\n"
    "s_lshl_b32 s67, s36, 12\n"
    "s_mov_b32 s74, -1\n"
    "s_add_u32 s68, s74, 1\n"
    "s_min_u32 s68, s68, 15\n"
    "s_add_u32 s79, s68, s38\n"
    "s_add_u32 s78, s68, s39\n"
    "s_add_u32 s69, s68, s64\n"
    "s_sub_u32 s69, s69, 8\n"
    "s_cmp_lt_u32 s68, 8\n"
    "s_cselect_b32 s78, s78, s69\n"
    "s_lshl_b32 s70, s79, 15\n"
    "s_add_u32 s70, s70, s77\n"
    "s_lshl_b32 s69, s78, 15\n"
    "s_add_u32 s69, s69, s77\n"
    "s_add_u32 m0, s67, 0\n"
    "s_nop 0\n"
    "buffer_load_dwordx4 v1, s[60:63], s70 offen offset:0 lds\n"
    "buffer_load_dwordx4 v2, s[60:63], s70 offen offset:1024 lds\n"
    "buffer_load_dwordx4 v3, s[60:63], s70 offen offset:2048 lds\n"
    "buffer_load_dwordx4 v4, s[60:63], s70 offen offset:3072 lds\n"
    "s_add_u32 m0, s67, 16384\n"
    "s_nop 0\n"
    "buffer_load_dwordx4 v1, s[56:59], s69 offen offset:0 lds\n"
    "buffer_load_dwordx4 v2, s[56:59], s69 offen offset:1024 lds\n"
    "buffer_load_dwordx4 v3, s[56:59], s69 offen offset:2048 lds\n"
    "buffer_load_dwordx4 v4, s[56:59], s69 offen offset:3072 lds\n"
    "s_add_u32 m0, s67, 32768\n"
    "s_add_u32 s70, s70, 512\n"
    "buffer_load_dwordx4 v1, s[60:63], s70 offen offset:0 lds\n"
    "buffer_load_dwordx4 v2, s[60:63], s70 offen offset:1024 lds\n"
    "buffer_load_dwordx4 v3, s[60:63], s70 offen offset:2048 lds\n"
    "buffer_load_dwordx4 v4, s[60:63], s70 offen offset:3072 lds\n"
    "s_add_u32 m0, s67, 49152\n"
    "s_add_u32 s69, s69, 512\n"
    "buffer_load_dwordx4 v1, s[56:59], s69 offen offset:0 lds\n"
    "buffer_load_dwordx4 v2, s[56:59], s69 offen offset:1024 lds\n"
    "buffer_load_dwordx4 v3, s[56:59], s69 offen offset:2048 lds\n"
    "buffer_load_dwordx4 v4, s[56:59], s69 offen offset:3072 lds\n"
    "s_mov_b32 s75, s79\n"
    "s_mov_b32 s76, s78\n"
    "s_lshl_b32 s66, s36, 17\n"
    "s_add_u32 s80, s67, 0x10000\n"
    "s_add_u32 m0, s80, 0\n"
    "s_add_u32 s68, s66, 0x0\n"
    "buffer_load_dwordx4 v6, s[48:51], s68 offen offset:0 nt lds\n"
    "s_add_u32 s68, s66, 0x7c00\n"
    "buffer_load_dwordx4 v6, s[48:51], s68 offen offset:1024 nt lds\n"
    "s_add_u32 s68, s66, 0xf800\n"
    "buffer_load_dwordx4 v6, s[48:51], s68 offen offset:2048 nt lds\n"
    "s_add_u32 s68, s66, 0x17400\n"
    "buffer_load_dwordx4 v6, s[48:51], s68 offen offset:3072 nt lds\n"
    "s_add_u32 m0, s80, 16384\n"
    "s_add_u32 s68, s66, 0x80000\n"
    "buffer_load_dwordx4 v6, s[48:51], s68 offen offset:0 nt lds\n"
    "s_add_u32 s68, s66, 0x87c00\n"
    "buffer_load_dwordx4 v6, s[48:51], s68 offen offset:1024 nt lds\n"
    "s_add_u32 s68, s66, 0x8f800\n"
    "buffer_load_dwordx4 v6, s[48:51], s68 offen offset:2048 nt lds\n"
    "s_add_u32 s68, s66, 0x97400\n"
    "buffer_load_dwordx4 v6, s[48:51], s68 offen offset:3072 nt lds\n"
    "s_add_u32 m0, s80, 32768\n"
    "s_add_u32 s68, s66, 0x100000\n"
    "buffer_load_dwordx4 v6, s[48:51], s68 offen offset:0 nt lds\n"
    "s_add_u32 s68, s66, 0x107c00\n"
    "buffer_load_dwordx4 v6, s[48:51], s68 offen offset:1024 nt lds\n"
    "s_add_u32 s68, s66, 0x10f800\n"
    "buffer_load_dwordx4 v6, s[48:51], s68 offen offset:2048 nt lds\n"
    "s_add_u32 s68, s66, 0x117400\n"
    "buffer_load_dwordx4 v6, s[48:51], s68 offen offset:3072 nt lds\n"
    "s_add_u32 m0, s80, 49152\n"
    "s_add_u32 s68, s66, 0x180000\n"
    "buffer_load_dwordx4 v6, s[48:51], s68 offen offset:0 nt lds\n"
    "s_add_u32 s68, s66, 0x187c00\n"
    "buffer_load_dwordx4 v6, s[48:51], s68 offen offset:1024 nt lds\n"
    "s_add_u32 s68, s66, 0x18f800\n"
    "buffer_load_dwordx4 v6, s[48:51], s68 offen offset:2048 nt lds\n"
    "s_add_u32 s68, s66, 0x197400\n"
    "buffer_load_dwordx4 v6, s[48:51], s68 offen offset:3072 nt lds\n"
    "s_add_u32 m0, s80, 65536\n"
    "s_add_u32 s68, s66, 0x200000\n"
    "buffer_load_dwordx4 v6, s[48:51], s68 offen offset:0 nt lds\n"
    "s_add_u32 s68, s66, 0x207c00\n"
    "buffer_load_dwordx4 v6, s[48:51], s68 offen offset:1024 nt lds\n"
    "s_add_u32 s68, s66, 0x20f800\n"
    "buffer_load_dwordx4 v6, s[48:51], s68 offen offset:2048 nt lds\n"
    "s_add_u32 s68, s66, 0x217400\n"
    "buffer_load_dwordx4 v6, s[48:51], s68 offen offset:3072 nt lds\n"
    "s_waitcnt vmcnt(16)\n"
    "s_waitcnt lgkmcnt(0)\n"
    "s_barrier\n"
    "s_add_u32 m0, s80, 81920\n"
    "s_add_u32 s68, s66, 0x280000\n"
    "buffer_load_dwordx4 v6, s[48:51], s68 offen offset:0 nt lds\n"
    "s_add_u32 s68, s66, 0x287c00\n"
    "buffer_load_dwordx4 v6, s[48:51], s68 offen offset:1024 nt lds\n"
    "s_add_u32 s68, s66, 0x28f800\n"
    "buffer_load_dwordx4 v6, s[48:51], s68 offen offset:2048 nt lds\n"
    "s_add_u32 s68, s66, 0x297400\n"
    "buffer_load_dwordx4 v6, s[48:51], s68 offen offset:3072 nt lds\n"
    "ds_read_b64_tr_b16 a[128:129], v8 offset:0\n"
    "ds_read_b64_tr_b16 a[130:131], v8 offset:1024\n"
    "ds_read_b64_tr_b16 a[132:133], v8 offset:4096\n"
    "ds_read_b64_tr_b16 a[134:135], v8 offset:5120\n"
    "ds_read_b64_tr_b16 a[136:137], v8 offset:8192\n"
    "ds_read_b64_tr_b16 a[138:139], v8 offset:9216\n"
    "ds_read_b64_tr_b16 a[140:141], v8 offset:12288\n"
    "ds_read_b64_tr_b16 a[142:143], v8 offset:13312\n"
    "s_waitcnt vmcnt(16)\n"
    "s_waitcnt lgkmcnt(0)\n"
    "s_barrier\n"
    "s_add_u32 m0, s80, 0\n"
    "s_add_u32 s68, s66, 0x300000\n"
    "buffer_load_dwordx4 v6, s[48:51], s68 offen offset:0 nt lds\n"
    "s_add_u32 s68, s66, 0x307c00\n"
    "buffer_load_dwordx4 v6, s[48:51], s68 offen offset:1024 nt lds\n"
    "s_add_u32 s68, s66, 0x30f800\n"
    "buffer_load_dwordx4 v6, s[48:51], s68 offen offset:2048 nt lds\n"
    "s_add_u32 s68, s66, 0x317400\n"
    "buffer_load_dwordx4 v6, s[48:51], s68 offen offset:3072 nt lds\n"
    "ds_read_b64_tr_b16 a[144:145], v8 offset:16384\n"
    "ds_read_b64_tr_b16 a[146:147], v8 offset:17408\n"
    "ds_read_b64_tr_b16 a[148:149], v8 offset:20480\n"
    "ds_read_b64_tr_b16 a[150:151], v8 offset:21504\n"
    "ds_read_b64_tr_b16 a[152:153], v8 offset:24576\n"
    "ds_read_b64_tr_b16 a[154:155], v8 offset:25600\n"
    "ds_read_b64_tr_b16 a[156:157], v8 offset:28672\n"
    "ds_read_b64_tr_b16 a[158:159], v8 offset:29696\n"
    "s_waitcnt vmcnt(16)\n"
    "s_waitcnt lgkmcnt(0)\n"
    "s_barrier\n"
    "s_add_u32 m0, s80, 16384\n"
    "s_add_u32 s68, s66, 0x380000\n"
    "buffer_load_dwordx4 v6, s[48:51], s68 offen offset:0 nt lds\n"
    "s_add_u32 s68, s66, 0x387c00\n"
    "buffer_load_dwordx4 v6, s[48:51], s68 offen offset:1024 nt lds\n"
    "s_add_u32 s68, s66, 0x38f800\n"
    "buffer_load_dwordx4 v6, s[48:51], s68 offen offset:2048 nt lds\n"
    "s_add_u32 s68, s66, 0x397400\n"
    "buffer_load_dwordx4 v6, s[48:51], s68 offen offset:3072 nt lds\n"
    "ds_read_b64_tr_b16 a[160:161], v8 offset:32768\n"
    "ds_read_b64_tr_b16 a[162:163], v8 offset:33792\n"
    "ds_read_b64_tr_b16 a[164:165], v8 offset:36864\n"
    "ds_read_b64_tr_b16 a[166:167], v8 offset:37888\n"
    "ds_read_b64_tr_b16 a[168:169], v8 offset:40960\n"
    "ds_read_b64_tr_b16 a[170:171], v8 offset:41984\n"
    "ds_read_b64_tr_b16 a[172:173], v8 offset:45056\n"
    "ds_read_b64_tr_b16 a[174:175], v8 offset:46080\n"
    "s_waitcnt vmcnt(16)\n"
    "s_waitcnt lgkmcnt(0)\n"
    "s_barrier\n"
    "s_add_u32 m0, s80, 32768\n"
    "s_add_u32 s68, s66, 0x0\n"
    "buffer_load_dwordx4 v6, s[52:55], s68 offen offset:0 nt lds\n"
    "s_add_u32 s68, s66, 0x7c00\n"
    "buffer_load_dwordx4 v6, s[52:55], s68 offen offset:1024 nt lds\n"
    "s_add_u32 s68, s66, 0xf800\n"
    "buffer_load_dwordx4 v6, s[52:55], s68 offen offset:2048 nt lds\n"
    "s_add_u32 s68, s66, 0x17400\n"
    "buffer_load_dwordx4 v6, s[52:55], s68 offen offset:3072 nt lds\n"
    "ds_read_b64_tr_b16 a[176:177], v9 offset:0\n"
    "ds_read_b64_tr_b16 a[178:179], v9 offset:1024\n"
    "ds_read_b64_tr_b16 a[180:181], v9 offset:4096\n"
    "ds_read_b64_tr_b16 a[182:183], v9 offset:5120\n"
    "ds_read_b64_tr_b16 a[184:185], v9 offset:8192\n"
    "ds_read_b64_tr_b16 a[186:187], v9 offset:9216\n"
    "ds_read_b64_tr_b16 a[188:189], v9 offset:12288\n"
    "ds_read_b64_tr_b16 a[190:191], v9 offset:13312\n"
    "s_waitcnt vmcnt(16)\n"
    "s_waitcnt lgkmcnt(0)\n"
    "s_barrier\n"
    "s_add_u32 m0, s80, 49152\n"
    "s_add_u32 s68, s66, 0x80000\n"
    "buffer_load_dwordx4 v6, s[52:55], s68 offen offset:0 nt lds\n"
    "s_add_u32 s68, s66, 0x87c00\n"
    "buffer_load_dwordx4 v6, s[52:55], s68 offen offset:1024 nt lds\n"
    "s_add_u32 s68, s66, 0x8f800\n"
    "buffer_load_dwordx4 v6, s[52:55], s68 offen offset:2048 nt lds\n"
    "s_add_u32 s68, s66, 0x97400\n"
    "buffer_load_dwordx4 v6, s[52:55], s68 offen offset:3072 nt lds\n"
    "ds_read_b64_tr_b16 a[192:193], v9 offset:16384\n"
    "ds_read_b64_tr_b16 a[194:195], v9 offset:17408\n"
    "ds_read_b64_tr_b16 a[196:197], v9 offset:20480\n"
    "ds_read_b64_tr_b16 a[198:199], v9 offset:21504\n"
    "ds_read_b64_tr_b16 a[200:201], v9 offset:24576\n"
    "ds_read_b64_tr_b16 a[202:203], v9 offset:25600\n"
    "ds_read_b64_tr_b16 a[204:205], v9 offset:28672\n"
    "ds_read_b64_tr_b16 a[206:207], v9 offset:29696\n"
    "s_waitcnt vmcnt(16)\n"
    "s_waitcnt lgkmcnt(0)\n"
    "s_barrier\n"
    "s_add_u32 m0, s80, 65536\n"
    "s_add_u32 s68, s66, 0x100000\n"
    "buffer_load_dwordx4 v6, s[52:55], s68 offen offset:0 nt lds\n"
    "s_add_u32 s68, s66, 0x107c00\n"
    "buffer_load_dwordx4 v6, s[52:55], s68 offen offset:1024 nt lds\n"
    "s_add_u32 s68, s66, 0x10f800\n"
    "buffer_load_dwordx4 v6, s[52:55], s68 offen offset:2048 nt lds\n"
    "s_add_u32 s68, s66, 0x117400\n"
    "buffer_load_dwordx4 v6, s[52:55], s68 offen offset:3072 nt lds\n"
    "ds_read_b64_tr_b16 a[208:209], v9 offset:32768\n"
    "ds_read_b64_tr_b16 a[210:211], v9 offset:33792\n"
    "ds_read_b64_tr_b16 a[212:213], v9 offset:36864\n"
    "ds_read_b64_tr_b16 a[214:215], v9 offset:37888\n"
    "ds_read_b64_tr_b16 a[216:217], v9 offset:40960\n"
    "ds_read_b64_tr_b16 a[218:219], v9 offset:41984\n"
    "ds_read_b64_tr_b16 a[220:221], v9 offset:45056\n"
    "ds_read_b64_tr_b16 a[222:223], v9 offset:46080\n"
    "s_waitcnt vmcnt(16)\n"
    "s_waitcnt lgkmcnt(0)\n"
    "s_barrier\n"
    "s_add_u32 m0, s80, 81920\n"
    "s_add_u32 s68, s66, 0x180000\n"
    "buffer_load_dwordx4 v6, s[52:55], s68 offen offset:0 nt lds\n"
    "s_add_u32 s68, s66, 0x187c00\n"
    "buffer_load_dwordx4 v6, s[52:55], s68 offen offset:1024 nt lds\n"
    "s_add_u32 s68, s66, 0x18f800\n"
    "buffer_load_dwordx4 v6, s[52:55], s68 offen offset:2048 nt lds\n"
    "s_add_u32 s68, s66, 0x197400\n"
    "buffer_load_dwordx4 v6, s[52:55], s68 offen offset:3072 nt lds\n"
    "ds_read_b64_tr_b16 a[224:225], v8 offset:0\n"
    "ds_read_b64_tr_b16 a[226:227], v8 offset:1024\n"
    "ds_read_b64_tr_b16 a[228:229], v8 offset:4096\n"
    "ds_read_b64_tr_b16 a[230:231], v8 offset:5120\n"
    "ds_read_b64_tr_b16 a[232:233], v8 offset:8192\n"
    "ds_read_b64_tr_b16 a[234:235], v8 offset:9216\n"
    "ds_read_b64_tr_b16 a[236:237], v8 offset:12288\n"
    "ds_read_b64_tr_b16 a[238:239], v8 offset:13312\n"
    "s_waitcnt vmcnt(16)\n"
    "s_waitcnt lgkmcnt(0)\n"
    "s_barrier\n"
    "s_add_u32 m0, s80, 0\n"
    "s_add_u32 s68, s66, 0x200000\n"
    "buffer_load_dwordx4 v6, s[52:55], s68 offen offset:0 nt lds\n"
    "s_add_u32 s68, s66, 0x207c00\n"
    "buffer_load_dwordx4 v6, s[52:55], s68 offen offset:1024 nt lds\n"
    "s_add_u32 s68, s66, 0x20f800\n"
    "buffer_load_dwordx4 v6, s[52:55], s68 offen offset:2048 nt lds\n"
    "s_add_u32 s68, s66, 0x217400\n"
    "buffer_load_dwordx4 v6, s[52:55], s68 offen offset:3072 nt lds\n"
    "ds_read_b64_tr_b16 a[240:241], v8 offset:16384\n"
    "ds_read_b64_tr_b16 a[242:243], v8 offset:17408\n"
    "ds_read_b64_tr_b16 a[244:245], v8 offset:20480\n"
    "ds_read_b64_tr_b16 a[246:247], v8 offset:21504\n"
    "ds_read_b64_tr_b16 a[248:249], v8 offset:24576\n"
    "ds_read_b64_tr_b16 a[250:251], v8 offset:25600\n"
    "ds_read_b64_tr_b16 a[252:253], v8 offset:28672\n"
    "ds_read_b64_tr_b16 a[254:255], v8 offset:29696\n"
    "s_waitcnt vmcnt(16)\n"
    "s_waitcnt lgkmcnt(0)\n"
    "s_barrier\n"
    "s_add_u32 m0, s80, 16384\n"
    "s_add_u32 s68, s66, 0x280000\n"
    "buffer_load_dwordx4 v6, s[52:55], s68 offen offset:0 nt lds\n"
    "s_add_u32 s68, s66, 0x287c00\n"
    "buffer_load_dwordx4 v6, s[52:55], s68 offen offset:1024 nt lds\n"
    "s_add_u32 s68, s66, 0x28f800\n"
    "buffer_load_dwordx4 v6, s[52:55], s68 offen offset:2048 nt lds\n"
    "s_add_u32 s68, s66, 0x297400\n"
    "buffer_load_dwordx4 v6, s[52:55], s68 offen offset:3072 nt lds\n"
    "ds_read_b64_tr_b16 v[128:129], v8 offset:32768\n"
    "ds_read_b64_tr_b16 v[130:131], v8 offset:33792\n"
    "ds_read_b64_tr_b16 v[132:133], v8 offset:36864\n"
    "ds_read_b64_tr_b16 v[134:135], v8 offset:37888\n"
    "ds_read_b64_tr_b16 v[136:137], v8 offset:40960\n"
    "ds_read_b64_tr_b16 v[138:139], v8 offset:41984\n"
    "ds_read_b64_tr_b16 v[140:141], v8 offset:45056\n"
    "ds_read_b64_tr_b16 v[142:143], v8 offset:46080\n"
    "s_waitcnt vmcnt(16)\n"
    "s_waitcnt lgkmcnt(0)\n"
    "s_barrier\n"
    "s_add_u32 m0, s80, 32768\n"
    "s_add_u32 s68, s66, 0x300000\n"
    "buffer_load_dwordx4 v6, s[52:55], s68 offen offset:0 nt lds\n"
    "s_add_u32 s68, s66, 0x307c00\n"
    "buffer_load_dwordx4 v6, s[52:55], s68 offen offset:1024 nt lds\n"
    "s_add_u32 s68, s66, 0x30f800\n"
    "buffer_load_dwordx4 v6, s[52:55], s68 offen offset:2048 nt lds\n"
    "s_add_u32 s68, s66, 0x317400\n"
    "buffer_load_dwordx4 v6, s[52:55], s68 offen offset:3072 nt lds\n"
    "ds_read_b64_tr_b16 v[144:145], v9 offset:0\n"
    "ds_read_b64_tr_b16 v[146:147], v9 offset:1024\n"
    "ds_read_b64_tr_b16 v[148:149], v9 offset:4096\n"
    "ds_read_b64_tr_b16 v[150:151], v9 offset:5120\n"
    "ds_read_b64_tr_b16 v[152:153], v9 offset:8192\n"
    "ds_read_b64_tr_b16 v[154:155], v9 offset:9216\n"
    "ds_read_b64_tr_b16 v[156:157], v9 offset:12288\n"
    "ds_read_b64_tr_b16 v[158:159], v9 offset:13312\n"
    "s_waitcnt vmcnt(16)\n"
    "s_waitcnt lgkmcnt(0)\n"
    "s_barrier\n"
    "s_add_u32 m0, s80, 49152\n"
    "s_add_u32 s68, s66, 0x380000\n"
    "buffer_load_dwordx4 v6, s[52:55], s68 offen offset:0 nt lds\n"
    "s_add_u32 s68, s66, 0x387c00\n"
    "buffer_load_dwordx4 v6, s[52:55], s68 offen offset:1024 nt lds\n"
    "s_add_u32 s68, s66, 0x38f800\n"
    "buffer_load_dwordx4 v6, s[52:55], s68 offen offset:2048 nt lds\n"
    "s_add_u32 s68, s66, 0x397400\n"
    "buffer_load_dwordx4 v6, s[52:55], s68 offen offset:3072 nt lds\n"
    "ds_read_b64_tr_b16 v[160:161], v9 offset:16384\n"
    "ds_read_b64_tr_b16 v[162:163], v9 offset:17408\n"
    "ds_read_b64_tr_b16 v[164:165], v9 offset:20480\n"
    "ds_read_b64_tr_b16 v[166:167], v9 offset:21504\n"
    "ds_read_b64_tr_b16 v[168:169], v9 offset:24576\n"
    "ds_read_b64_tr_b16 v[170:171], v9 offset:25600\n"
    "ds_read_b64_tr_b16 v[172:173], v9 offset:28672\n"
    "ds_read_b64_tr_b16 v[174:175], v9 offset:29696\n"
    "s_waitcnt vmcnt(16)\n"
    "s_waitcnt lgkmcnt(0)\n"
    "s_barrier\n"
    "s_add_u32 m0, s80, 65536\n"
    "s_add_u32 s68, s66, 0x0\n"
    "buffer_load_dwordx4 v6, s[44:47], s68 offen offset:0 nt lds\n"
    "s_add_u32 s68, s66, 0x7c00\n"
    "buffer_load_dwordx4 v6, s[44:47], s68 offen offset:1024 nt lds\n"
    "s_add_u32 s68, s66, 0xf800\n"
    "buffer_load_dwordx4 v6, s[44:47], s68 offen offset:2048 nt lds\n"
    "s_add_u32 s68, s66, 0x17400\n"
    "buffer_load_dwordx4 v6, s[44:47], s68 offen offset:3072 nt lds\n"
    "ds_read_b64_tr_b16 v[176:177], v9 offset:32768\n"
    "ds_read_b64_tr_b16 v[178:179], v9 offset:33792\n"
    "ds_read_b64_tr_b16 v[180:181], v9 offset:36864\n"
    "ds_read_b64_tr_b16 v[182:183], v9 offset:37888\n"
    "ds_read_b64_tr_b16 v[184:185], v9 offset:40960\n"
    "ds_read_b64_tr_b16 v[186:187], v9 offset:41984\n"
    "ds_read_b64_tr_b16 v[188:189], v9 offset:45056\n"
    "ds_read_b64_tr_b16 v[190:191], v9 offset:46080\n"
    "s_waitcnt vmcnt(16)\n"
    "s_waitcnt lgkmcnt(0)\n"
    "s_barrier\n"
    "s_add_u32 m0, s80, 81920\n"
    "s_add_u32 s68, s66, 0x80000\n"
    "buffer_load_dwordx4 v6, s[44:47], s68 offen offset:0 nt lds\n"
    "s_add_u32 s68, s66, 0x87c00\n"
    "buffer_load_dwordx4 v6, s[44:47], s68 offen offset:1024 nt lds\n"
    "s_add_u32 s68, s66, 0x8f800\n"
    "buffer_load_dwordx4 v6, s[44:47], s68 offen offset:2048 nt lds\n"
    "s_add_u32 s68, s66, 0x97400\n"
    "buffer_load_dwordx4 v6, s[44:47], s68 offen offset:3072 nt lds\n"
    "ds_read_b64_tr_b16 v[192:193], v8 offset:0\n"
    "ds_read_b64_tr_b16 v[194:195], v8 offset:1024\n"
    "ds_read_b64_tr_b16 v[196:197], v8 offset:4096\n"
    "ds_read_b64_tr_b16 v[198:199], v8 offset:5120\n"
    "ds_read_b64_tr_b16 v[200:201], v8 offset:8192\n"
    "ds_read_b64_tr_b16 v[202:203], v8 offset:9216\n"
    "ds_read_b64_tr_b16 v[204:205], v8 offset:12288\n"
    "ds_read_b64_tr_b16 v[206:207], v8 offset:13312\n"
    "s_waitcnt vmcnt(16)\n"
    "s_waitcnt lgkmcnt(0)\n"
    "s_barrier\n"
    "s_add_u32 m0, s80, 0\n"
    "s_add_u32 s68, s66, 0x100000\n"
    "buffer_load_dwordx4 v6, s[44:47], s68 offen offset:0 nt lds\n"
    "s_add_u32 s68, s66, 0x107c00\n"
    "buffer_load_dwordx4 v6, s[44:47], s68 offen offset:1024 nt lds\n"
    "s_add_u32 s68, s66, 0x10f800\n"
    "buffer_load_dwordx4 v6, s[44:47], s68 offen offset:2048 nt lds\n"
    "s_add_u32 s68, s66, 0x117400\n"
    "buffer_load_dwordx4 v6, s[44:47], s68 offen offset:3072 nt lds\n"
    "ds_read_b64_tr_b16 v[208:209], v8 offset:16384\n"
    "ds_read_b64_tr_b16 v[210:211], v8 offset:17408\n"
    "ds_read_b64_tr_b16 v[212:213], v8 offset:20480\n"
    "ds_read_b64_tr_b16 v[214:215], v8 offset:21504\n"
    "ds_read_b64_tr_b16 v[216:217], v8 offset:24576\n"
    "ds_read_b64_tr_b16 v[218:219], v8 offset:25600\n"
    "ds_read_b64_tr_b16 v[220:221], v8 offset:28672\n"
    "ds_read_b64_tr_b16 v[222:223], v8 offset:29696\n"
    "s_waitcnt vmcnt(16)\n"
    "s_waitcnt lgkmcnt(0)\n"
    "s_barrier\n"
    "s_add_u32 m0, s80, 16384\n"
    "s_add_u32 s68, s66, 0x180000\n"
    "buffer_load_dwordx4 v6, s[44:47], s68 offen offset:0 nt lds\n"
    "s_add_u32 s68, s66, 0x187c00\n"
    "buffer_load_dwordx4 v6, s[44:47], s68 offen offset:1024 nt lds\n"
    "s_add_u32 s68, s66, 0x18f800\n"
    "buffer_load_dwordx4 v6, s[44:47], s68 offen offset:2048 nt lds\n"
    "s_add_u32 s68, s66, 0x197400\n"
    "buffer_load_dwordx4 v6, s[44:47], s68 offen offset:3072 nt lds\n"
    "ds_read_b64_tr_b16 v[224:225], v8 offset:32768\n"
    "ds_read_b64_tr_b16 v[226:227], v8 offset:33792\n"
    "ds_read_b64_tr_b16 v[228:229], v8 offset:36864\n"
    "ds_read_b64_tr_b16 v[230:231], v8 offset:37888\n"
    "ds_read_b64_tr_b16 v[232:233], v8 offset:40960\n"
    "ds_read_b64_tr_b16 v[234:235], v8 offset:41984\n"
    "ds_read_b64_tr_b16 v[236:237], v8 offset:45056\n"
    "ds_read_b64_tr_b16 v[238:239], v8 offset:46080\n"
    "s_waitcnt vmcnt(16)\n"
    "s_waitcnt lgkmcnt(0)\n"
    "s_barrier\n"
    "s_add_u32 m0, s80, 32768\n"
    "s_add_u32 s68, s66, 0x200000\n"
    "buffer_load_dwordx4 v6, s[44:47], s68 offen offset:0 nt lds\n"
    "s_add_u32 s68, s66, 0x207c00\n"
    "buffer_load_dwordx4 v6, s[44:47], s68 offen offset:1024 nt lds\n"
    "s_add_u32 s68, s66, 0x20f800\n"
    "buffer_load_dwordx4 v6, s[44:47], s68 offen offset:2048 nt lds\n"
    "s_add_u32 s68, s66, 0x217400\n"
    "buffer_load_dwordx4 v6, s[44:47], s68 offen offset:3072 nt lds\n"
    "ds_read_b64_tr_b16 v[240:241], v9 offset:0\n"
    "ds_read_b64_tr_b16 v[242:243], v9 offset:1024\n"
    "ds_read_b64_tr_b16 v[244:245], v9 offset:4096\n"
    "ds_read_b64_tr_b16 v[246:247], v9 offset:5120\n"
    "ds_read_b64_tr_b16 v[248:249], v9 offset:8192\n"
    "ds_read_b64_tr_b16 v[250:251], v9 offset:9216\n"
    "ds_read_b64_tr_b16 v[252:253], v9 offset:12288\n"
    "ds_read_b64_tr_b16 v[254:255], v9 offset:13312\n"
    "s_waitcnt vmcnt(16)\n"
    "s_waitcnt lgkmcnt(0)\n"
    "s_barrier\n"
    "s_add_u32 m0, s80, 49152\n"
    "s_add_u32 s68, s66, 0x280000\n"
    "buffer_load_dwordx4 v6, s[44:47], s68 offen offset:0 nt lds\n"
    "s_add_u32 s68, s66, 0x287c00\n"
    "buffer_load_dwordx4 v6, s[44:47], s68 offen offset:1024 nt lds\n"
    "s_add_u32 s68, s66, 0x28f800\n"
    "buffer_load_dwordx4 v6, s[44:47], s68 offen offset:2048 nt lds\n"
    "s_add_u32 s68, s66, 0x297400\n"
    "buffer_load_dwordx4 v6, s[44:47], s68 offen offset:3072 nt lds\n"
    "ds_read_b64_tr_b16 a[0:1], v9 offset:16384\n"
    "ds_read_b64_tr_b16 a[2:3], v9 offset:17408\n"
    "ds_read_b64_tr_b16 a[4:5], v9 offset:20480\n"
    "ds_read_b64_tr_b16 a[6:7], v9 offset:21504\n"
    "ds_read_b64_tr_b16 a[8:9], v9 offset:24576\n"
    "ds_read_b64_tr_b16 a[10:11], v9 offset:25600\n"
    "ds_read_b64_tr_b16 a[12:13], v9 offset:28672\n"
    "ds_read_b64_tr_b16 a[14:15], v9 offset:29696\n"
    "s_waitcnt vmcnt(16)\n"
    "s_waitcnt lgkmcnt(0)\n"
    "s_barrier\n"
    "s_add_u32 m0, s80, 65536\n"
    "s_add_u32 s68, s66, 0x300000\n"
    "buffer_load_dwordx4 v6, s[44:47], s68 offen offset:0 nt lds\n"
    "s_add_u32 s68, s66, 0x307c00\n"
    "buffer_load_dwordx4 v6, s[44:47], s68 offen offset:1024 nt lds\n"
    "s_add_u32 s68, s66, 0x30f800\n"
    "buffer_load_dwordx4 v6, s[44:47], s68 offen offset:2048 nt lds\n"
    "s_add_u32 s68, s66, 0x317400\n"
    "buffer_load_dwordx4 v6, s[44:47], s68 offen offset:3072 nt lds\n"
    "ds_read_b64_tr_b16 a[16:17], v9 offset:32768\n"
    "ds_read_b64_tr_b16 a[18:19], v9 offset:33792\n"
    "ds_read_b64_tr_b16 a[20:21], v9 offset:36864\n"
    "ds_read_b64_tr_b16 a[22:23], v9 offset:37888\n"
    "ds_read_b64_tr_b16 a[24:25], v9 offset:40960\n"
    "ds_read_b64_tr_b16 a[26:27], v9 offset:41984\n"
    "ds_read_b64_tr_b16 a[28:29], v9 offset:45056\n"
    "ds_read_b64_tr_b16 a[30:31], v9 offset:46080\n"
    "s_waitcnt vmcnt(16)\n"
    "s_waitcnt lgkmcnt(0)\n"
    "s_barrier\n"
    "s_add_u32 m0, s80, 81920\n"
    "s_add_u32 s68, s66, 0x380000\n"
    "buffer_load_dwordx4 v6, s[44:47], s68 offen offset:0 nt lds\n"
    "s_add_u32 s68, s66, 0x387c00\n"
    "buffer_load_dwordx4 v6, s[44:47], s68 offen offset:1024 nt lds\n"
    "s_add_u32 s68, s66, 0x38f800\n"
    "buffer_load_dwordx4 v6, s[44:47], s68 offen offset:2048 nt lds\n"
    "s_add_u32 s68, s66, 0x397400\n"
    "buffer_load_dwordx4 v6, s[44:47], s68 offen offset:3072 nt lds\n"
    "ds_read_b64_tr_b16 a[32:33], v8 offset:0\n"
    "ds_read_b64_tr_b16 a[34:35], v8 offset:1024\n"
    "ds_read_b64_tr_b16 a[36:37], v8 offset:4096\n"
    "ds_read_b64_tr_b16 a[38:39], v8 offset:5120\n"
    "ds_read_b64_tr_b16 a[40:41], v8 offset:8192\n"
    "ds_read_b64_tr_b16 a[42:43], v8 offset:9216\n"
    "ds_read_b64_tr_b16 a[44:45], v8 offset:12288\n"
    "ds_read_b64_tr_b16 a[46:47], v8 offset:13312\n"
    "s_waitcnt vmcnt(16)\n"
    "s_waitcnt lgkmcnt(0)\n"
    "s_barrier\n"
    "ds_read_b64_tr_b16 a[48:49], v8 offset:16384\n"
    "ds_read_b64_tr_b16 a[50:51], v8 offset:17408\n"
    "ds_read_b64_tr_b16 a[52:53], v8 offset:20480\n"
    "ds_read_b64_tr_b16 a[54:55], v8 offset:21504\n"
    "ds_read_b64_tr_b16 a[56:57], v8 offset:24576\n"
    "ds_read_b64_tr_b16 a[58:59], v8 offset:25600\n"
    "ds_read_b64_tr_b16 a[60:61], v8 offset:28672\n"
    "ds_read_b64_tr_b16 a[62:63], v8 offset:29696\n"
    "s_waitcnt vmcnt(12)\n"
    "s_waitcnt lgkmcnt(0)\n"
    "s_barrier\n"
    "ds_read_b64_tr_b16 a[64:65], v8 offset:32768\n"
    "ds_read_b64_tr_b16 a[66:67], v8 offset:33792\n"
    "ds_read_b64_tr_b16 a[68:69], v8 offset:36864\n"
    "ds_read_b64_tr_b16 a[70:71], v8 offset:37888\n"
    "ds_read_b64_tr_b16 a[72:73], v8 offset:40960\n"
    "ds_read_b64_tr_b16 a[74:75], v8 offset:41984\n"
    "ds_read_b64_tr_b16 a[76:77], v8 offset:45056\n"
    "ds_read_b64_tr_b16 a[78:79], v8 offset:46080\n"
    "s_waitcnt vmcnt(8)\n"
    "s_waitcnt lgkmcnt(0)\n"
    "s_barrier\n"
    "ds_read_b64_tr_b16 a[80:81], v9 offset:0\n"
    "ds_read_b64_tr_b16 a[82:83], v9 offset:1024\n"
    "ds_read_b64_tr_b16 a[84:85], v9 offset:4096\n"
    "ds_read_b64_tr_b16 a[86:87], v9 offset:5120\n"
    "ds_read_b64_tr_b16 a[88:89], v9 offset:8192\n"
    "ds_read_b64_tr_b16 a[90:91], v9 offset:9216\n"
    "ds_read_b64_tr_b16 a[92:93], v9 offset:12288\n"
    "ds_read_b64_tr_b16 a[94:95], v9 offset:13312\n"
    "s_waitcnt vmcnt(4)\n"
    "s_waitcnt lgkmcnt(0)\n"
    "s_barrier\n"
    "ds_read_b64_tr_b16 a[96:97], v9 offset:16384\n"
    "ds_read_b64_tr_b16 a[98:99], v9 offset:17408\n"
    "ds_read_b64_tr_b16 a[100:101], v9 offset:20480\n"
    "ds_read_b64_tr_b16 a[102:103], v9 offset:21504\n"
    "ds_read_b64_tr_b16 a[104:105], v9 offset:24576\n"
    "ds_read_b64_tr_b16 a[106:107], v9 offset:25600\n"
    "ds_read_b64_tr_b16 a[108:109], v9 offset:28672\n"
    "ds_read_b64_tr_b16 a[110:111], v9 offset:29696\n"
    "s_waitcnt vmcnt(0)\n"
    "s_waitcnt lgkmcnt(0)\n"
    "s_barrier\n"
    "ds_read_b64_tr_b16 a[112:113], v9 offset:32768\n"
    "ds_read_b64_tr_b16 a[114:115], v9 offset:33792\n"
    "ds_read_b64_tr_b16 a[116:117], v9 offset:36864\n"
    "ds_read_b64_tr_b16 a[118:119], v9 offset:37888\n"
    "ds_read_b64_tr_b16 a[120:121], v9 offset:40960\n"
    "ds_read_b64_tr_b16 a[122:123], v9 offset:41984\n"
    "ds_read_b64_tr_b16 a[124:125], v9 offset:45056\n"
    "ds_read_b64_tr_b16 a[126:127], v9 offset:46080\n"
    "s_waitcnt lgkmcnt(0)\n"
    "s_waitcnt vmcnt(0)\n"
    "s_barrier\n"
    "s_mov_b32 s74, 0\n"
    "s_brev_b32 s71, 1\n"
    "s_mov_b32 s72, s71\n"
    "s_mov_b32 s73, s71\n"
    "s_mov_b32 s80, 0\n"
    "s_mov_b32 s82, 0x3e0293ee\n"
    "s_mov_b32 s83, 0x3e0293ee\n"
    "v_xor_b32 v6, 0x0, v0\n"
    "ds_read_b128 v[8:11], v6 offset:0\n"
    "ds_read_b128 v[12:15], v6 offset:256\n"
    "v_xor_b32 v7, 0x0, v0\n"
    "ds_read_b128 v[24:27], v7 offset:16384\n"
    "s_waitcnt lgkmcnt(0)\n"
    ".Lp2_loop_%=:\n"
    "v_mfma_f32_32x32x16_bf16 v[48:63], a[128:131], v[8:11], 0\n"
    "v_xor_b32 v6, 0x20, v0\n"
    "ds_read_b128 v[16:19], v6 offset:0\n"
    "ds_read_b128 v[28:31], v7 offset:16640\n"
    "s_add_u32 s68, s74, 1\n"
    "s_min_u32 s68, s68, 15\n"
    "s_add_u32 s79, s68, s38\n"
    "s_add_u32 s78, s68, s39\n"
    "s_add_u32 s69, s68, s64\n"
    "s_sub_u32 s69, s69, 8\n"
    "s_cmp_lt_u32 s68, 8\n"
    "s_cselect_b32 s78, s78, s69\n"
    "s_lshl_b32 s70, s79, 15\n"
    "s_add_u32 s70, s70, s77\n"
    "s_lshl_b32 s69, s78, 15\n"
    "s_add_u32 s69, s69, s77\n"
    "v_mfma_f32_32x32x16_bf16 v[64:79], v[8:11], v[128:131], 0\n"
    "s_waitcnt lgkmcnt(2)\n"
    "v_mfma_f32_32x32x16_bf16 v[32:47], a[0:3], v[24:27], 0\n"
    "s_add_u32 m0, s67, 65536\n"
    "s_nop 0\n"
    "buffer_load_dwordx4 v1, s[60:63], s70 offen offset:0 lds\n"
    "v_mfma_f32_32x32x16_bf16 v[48:63], a[160:163], v[12:15], v[48:63]\n"
    "ds_read_b128 v[20:23], v6 offset:256\n"
    "v_xor_b32 v7, 0x20, v0\n"
    "ds_read_b128 v[24:27], v7 offset:16384\n"
    "buffer_load_dwordx4 v2, s[60:63], s70 offen offset:1024 lds\n"
    "v_mfma_f32_32x32x16_bf16 v[64:79], v[12:15], v[160:163], v[64:79]\n"
    "s_waitcnt lgkmcnt(2)\n"
    "v_mfma_f32_32x32x16_bf16 v[32:47], a[32:35], v[28:31], v[32:47]\n"
    "buffer_load_dwordx4 v3, s[60:63], s70 offen offset:2048 lds\n"
    "v_mfma_f32_32x32x16_bf16 v[48:63], a[132:135], v[16:19], v[48:63]\n"
    "v_xor_b32 v6, 0x40, v0\n"
    "ds_read_b128 v[8:11], v6 offset:0\n"
    "ds_read_b128 v[28:31], v7 offset:16640\n"
    "buffer_load_dwordx4 v4, s[60:63], s70 offen offset:3072 lds\n"
    "v_mfma_f32_32x32x16_bf16 v[64:79], v[16:19], v[132:135], v[64:79]\n"
    "s_waitcnt lgkmcnt(2)\n"
    "v_mfma_f32_32x32x16_bf16 v[32:47], a[4:7], v[24:27], v[32:47]\n"
    "s_add_u32 m0, s67, 81920\n"
    "s_nop 0\n"
    "buffer_load_dwordx4 v1, s[56:59], s69 offen offset:0 lds\n"
    "v_mfma_f32_32x32x16_bf16 v[48:63], a[164:167], v[20:23], v[48:63]\n"
    "ds_read_b128 v[12:15], v6 offset:256\n"
    "v_xor_b32 v7, 0x40, v0\n"
    "ds_read_b128 v[24:27], v7 offset:16384\n"
    "buffer_load_dwordx4 v2, s[56:59], s69 offen offset:1024 lds\n"
    "v_mfma_f32_32x32x16_bf16 v[64:79], v[20:23], v[164:167], v[64:79]\n"
    "s_waitcnt lgkmcnt(2)\n"
    "v_mfma_f32_32x32x16_bf16 v[32:47], a[36:39], v[28:31], v[32:47]\n"
    "buffer_load_dwordx4 v3, s[56:59], s69 offen offset:2048 lds\n"
    "v_mfma_f32_32x32x16_bf16 v[48:63], a[136:139], v[8:11], v[48:63]\n"
    "v_xor_b32 v6, 0x60, v0\n"
    "ds_read_b128 v[16:19], v6 offset:0\n"
    "ds_read_b128 v[28:31], v7 offset:16640\n"
    "buffer_load_dwordx4 v4, s[56:59], s69 offen offset:3072 lds\n"
    "v_mfma_f32_32x32x16_bf16 v[64:79], v[8:11], v[136:139], v[64:79]\n"
    "s_waitcnt lgkmcnt(2)\n"
    "v_mfma_f32_32x32x16_bf16 v[32:47], a[8:11], v[24:27], v[32:47]\n"
    "v_mfma_f32_32x32x16_bf16 v[48:63], a[168:171], v[12:15], v[48:63]\n"
    "ds_read_b128 v[20:23], v6 offset:256\n"
    "v_xor_b32 v7, 0x60, v0\n"
    "ds_read_b128 v[24:27], v7 offset:16384\n"
    "v_cvt_pk_bf16_f32 v96, v96, v97\n"
    "v_mfma_f32_32x32x16_bf16 v[64:79], v[12:15], v[168:171], v[64:79]\n"
    "v_cvt_pk_bf16_f32 v97, v98, v99\n"
    "v_cvt_pk_bf16_f32 v98, v100, v101\n"
    "v_cvt_pk_bf16_f32 v99, v102, v103\n"
    "s_waitcnt lgkmcnt(2)\n"
    "v_mfma_f32_32x32x16_bf16 v[32:47], a[40:43], v[28:31], v[32:47]\n"
    "buffer_store_dwordx4 v[96:99], v5, s[40:43], s72 offen offset:0 sc1\n"
    "v_cvt_pk_bf16_f32 v104, v104, v105\n"
    "v_cvt_pk_bf16_f32 v105, v106, v107\n"
    "v_mfma_f32_32x32x16_bf16 v[48:63], a[140:143], v[16:19], v[48:63]\n"
    "v_xor_b32 v6, 0x80, v0\n"
    "ds_read_b128 v[8:11], v6 offset:0\n"
    "ds_read_b128 v[28:31], v7 offset:16640\n"
    "v_cvt_pk_bf16_f32 v106, v108, v109\n"
    "v_mfma_f32_32x32x16_bf16 v[64:79], v[16:19], v[140:143], v[64:79]\n"
    "v_cvt_pk_bf16_f32 v107, v110, v111\n"
    "buffer_store_dwordx4 v[104:107], v5, s[40:43], s72 offen offset:1024 sc1\n"
    "v_cvt_pk_bf16_f32 v112, v112, v113\n"
    "s_waitcnt lgkmcnt(2)\n"
    "v_mfma_f32_32x32x16_bf16 v[32:47], a[12:15], v[24:27], v[32:47]\n"
    "v_cvt_pk_bf16_f32 v113, v114, v115\n"
    "v_cvt_pk_bf16_f32 v114, v116, v117\n"
    "v_cvt_pk_bf16_f32 v115, v118, v119\n"
    "v_mfma_f32_32x32x16_bf16 v[48:63], a[172:175], v[20:23], v[48:63]\n"
    "ds_read_b128 v[12:15], v6 offset:256\n"
    "v_xor_b32 v7, 0x80, v0\n"
    "ds_read_b128 v[24:27], v7 offset:16384\n"
    "buffer_store_dwordx4 v[112:115], v5, s[40:43], s73 offen offset:0 sc1\n"
    "v_mfma_f32_32x32x16_bf16 v[64:79], v[20:23], v[172:175], v[64:79]\n"
    "v_cvt_pk_bf16_f32 v120, v120, v121\n"
    "v_cvt_pk_bf16_f32 v121, v122, v123\n"
    "v_cvt_pk_bf16_f32 v122, v124, v125\n"
    "s_waitcnt lgkmcnt(2)\n"
    "v_mfma_f32_32x32x16_bf16 v[32:47], a[44:47], v[28:31], v[32:47]\n"
    "v_cvt_pk_bf16_f32 v123, v126, v127\n"
    "buffer_store_dwordx4 v[120:123], v5, s[40:43], s73 offen offset:1024 sc1\n"
    "v_mul_f32 v80, s82, v80\n"
    "v_mfma_f32_32x32x16_bf16 v[48:63], a[144:147], v[8:11], v[48:63]\n"
    "v_xor_b32 v6, 0xa0, v0\n"
    "ds_read_b128 v[16:19], v6 offset:0\n"
    "ds_read_b128 v[28:31], v7 offset:16640\n"
    "v_mul_f32 v81, s82, v81\n"
    "v_mfma_f32_32x32x16_bf16 v[64:79], v[8:11], v[144:147], v[64:79]\n"
    "v_mul_f32 v82, s82, v82\n"
    "v_mul_f32 v83, s82, v83\n"
    "v_mul_f32 v84, s82, v84\n"
    "s_waitcnt lgkmcnt(2)\n"
    "v_mfma_f32_32x32x16_bf16 v[32:47], a[16:19], v[24:27], v[32:47]\n"
    "v_mul_f32 v85, s82, v85\n"
    "v_mul_f32 v86, s82, v86\n"
    "v_mul_f32 v87, s82, v87\n"
    "v_mfma_f32_32x32x16_bf16 v[48:63], a[176:179], v[12:15], v[48:63]\n"
    "ds_read_b128 v[20:23], v6 offset:256\n"
    "v_xor_b32 v7, 0xa0, v0\n"
    "ds_read_b128 v[24:27], v7 offset:16384\n"
    "v_cvt_pk_bf16_f32 v80, v80, v81\n"
    "v_mfma_f32_32x32x16_bf16 v[64:79], v[12:15], v[176:179], v[64:79]\n"
    "v_cvt_pk_bf16_f32 v81, v82, v83\n"
    "v_cvt_pk_bf16_f32 v82, v84, v85\n"
    "v_cvt_pk_bf16_f32 v83, v86, v87\n"
    "s_waitcnt lgkmcnt(2)\n"
    "v_mfma_f32_32x32x16_bf16 v[32:47], a[48:51], v[28:31], v[32:47]\n"
    "buffer_store_dwordx4 v[80:83], v5, s[40:43], s71 offen offset:0 sc1\n"
    "v_mul_f32 v88, s82, v88\n"
    "v_mul_f32 v89, s82, v89\n"
    "v_mfma_f32_32x32x16_bf16 v[48:63], a[148:151], v[16:19], v[48:63]\n"
    "v_xor_b32 v6, 0xc0, v0\n"
    "ds_read_b128 v[8:11], v6 offset:0\n"
    "ds_read_b128 v[28:31], v7 offset:16640\n"
    "v_mul_f32 v90, s82, v90\n"
    "v_mfma_f32_32x32x16_bf16 v[64:79], v[16:19], v[148:151], v[64:79]\n"
    "v_mul_f32 v91, s82, v91\n"
    "v_mul_f32 v92, s82, v92\n"
    "v_mul_f32 v93, s82, v93\n"
    "s_waitcnt lgkmcnt(2)\n"
    "v_mfma_f32_32x32x16_bf16 v[32:47], a[20:23], v[24:27], v[32:47]\n"
    "v_mul_f32 v94, s82, v94\n"
    "v_mul_f32 v95, s82, v95\n"
    "v_cvt_pk_bf16_f32 v88, v88, v89\n"
    "v_mfma_f32_32x32x16_bf16 v[48:63], a[180:183], v[20:23], v[48:63]\n"
    "ds_read_b128 v[12:15], v6 offset:256\n"
    "v_xor_b32 v7, 0xc0, v0\n"
    "ds_read_b128 v[24:27], v7 offset:16384\n"
    "v_cvt_pk_bf16_f32 v89, v90, v91\n"
    "v_mfma_f32_32x32x16_bf16 v[64:79], v[20:23], v[180:183], v[64:79]\n"
    "v_cvt_pk_bf16_f32 v90, v92, v93\n"
    "v_cvt_pk_bf16_f32 v91, v94, v95\n"
    "buffer_store_dwordx4 v[88:91], v5, s[40:43], s71 offen offset:1024 sc1\n"
    "s_waitcnt lgkmcnt(2)\n"
    "v_mfma_f32_32x32x16_bf16 v[32:47], a[52:55], v[28:31], v[32:47]\n"
    "v_mfma_f32_32x32x16_bf16 v[48:63], a[152:155], v[8:11], v[48:63]\n"
    "v_xor_b32 v6, 0xe0, v0\n"
    "ds_read_b128 v[16:19], v6 offset:0\n"
    "ds_read_b128 v[28:31], v7 offset:16640\n"
    "v_mfma_f32_32x32x16_bf16 v[64:79], v[8:11], v[152:155], v[64:79]\n"
    "s_waitcnt lgkmcnt(2)\n"
    "v_mfma_f32_32x32x16_bf16 v[32:47], a[24:27], v[24:27], v[32:47]\n"
    "v_mfma_f32_32x32x16_bf16 v[48:63], a[184:187], v[12:15], v[48:63]\n"
    "ds_read_b128 v[20:23], v6 offset:256\n"
    "v_xor_b32 v7, 0xe0, v0\n"
    "ds_read_b128 v[24:27], v7 offset:16384\n"
    "v_mfma_f32_32x32x16_bf16 v[64:79], v[12:15], v[184:187], v[64:79]\n"
    "s_waitcnt lgkmcnt(2)\n"
    "v_mfma_f32_32x32x16_bf16 v[32:47], a[56:59], v[28:31], v[32:47]\n"
    "s_waitcnt vmcnt(14)\n"
    "s_barrier\n"
    "v_mfma_f32_32x32x16_bf16 v[48:63], a[156:159], v[16:19], v[48:63]\n"
    "v_xor_b32 v6, 0x0, v0\n"
    "ds_read_b128 v[8:11], v6 offset:32768\n"
    "ds_read_b128 v[28:31], v7 offset:16640\n"
    "v_mfma_f32_32x32x16_bf16 v[64:79], v[16:19], v[156:159], v[64:79]\n"
    "s_waitcnt lgkmcnt(2)\n"
    "v_mfma_f32_32x32x16_bf16 v[32:47], a[28:31], v[24:27], v[32:47]\n"
    "v_mfma_f32_32x32x16_bf16 v[48:63], a[188:191], v[20:23], v[48:63]\n"
    "ds_read_b128 v[12:15], v6 offset:33024\n"
    "v_xor_b32 v7, 0x0, v0\n"
    "ds_read_b128 v[24:27], v7 offset:49152\n"
    "v_mfma_f32_32x32x16_bf16 v[64:79], v[20:23], v[188:191], v[64:79]\n"
    "s_waitcnt lgkmcnt(2)\n"
    "v_mfma_f32_32x32x16_bf16 v[32:47], a[60:63], v[28:31], v[32:47]\n"
    "v_mfma_f32_32x32x16_bf16 v[48:63], a[192:195], v[8:11], v[48:63]\n"
    "v_xor_b32 v6, 0x20, v0\n"
    "ds_read_b128 v[16:19], v6 offset:32768\n"
    "ds_read_b128 v[28:31], v7 offset:49408\n"
    "v_mfma_f32_32x32x16_bf16 v[64:79], v[8:11], v[192:195], v[64:79]\n"
    "s_waitcnt lgkmcnt(2)\n"
    "v_mfma_f32_32x32x16_bf16 v[32:47], a[64:67], v[24:27], v[32:47]\n"
    "s_add_u32 m0, s67, 98304\n"
    "s_add_u32 s70, s70, 512\n"
    "buffer_load_dwordx4 v1, s[60:63], s70 offen offset:0 lds\n"
    "v_mfma_f32_32x32x16_bf16 v[48:63], a[224:227], v[12:15], v[48:63]\n"
    "ds_read_b128 v[20:23], v6 offset:33024\n"
    "v_xor_b32 v7, 0x20, v0\n"
    "ds_read_b128 v[24:27], v7 offset:49152\n"
    "buffer_load_dwordx4 v2, s[60:63], s70 offen offset:1024 lds\n"
    "v_mfma_f32_32x32x16_bf16 v[64:79], v[12:15], v[224:227], v[64:79]\n"
    "s_waitcnt lgkmcnt(2)\n"
    "v_mfma_f32_32x32x16_bf16 v[32:47], a[96:99], v[28:31], v[32:47]\n"
    "buffer_load_dwordx4 v3, s[60:63], s70 offen offset:2048 lds\n"
    "v_mfma_f32_32x32x16_bf16 v[48:63], a[196:199], v[16:19], v[48:63]\n"
    "v_xor_b32 v6, 0x40, v0\n"
    "ds_read_b128 v[8:11], v6 offset:32768\n"
    "ds_read_b128 v[28:31], v7 offset:49408\n"
    "buffer_load_dwordx4 v4, s[60:63], s70 offen offset:3072 lds\n"
    "v_mfma_f32_32x32x16_bf16 v[64:79], v[16:19], v[196:199], v[64:79]\n"
    "s_waitcnt lgkmcnt(2)\n"
    "v_mfma_f32_32x32x16_bf16 v[32:47], a[68:71], v[24:27], v[32:47]\n"
    "s_add_u32 m0, s67, 114688\n"
    "s_add_u32 s69, s69, 512\n"
    "buffer_load_dwordx4 v1, s[56:59], s69 offen offset:0 lds\n"
    "v_mfma_f32_32x32x16_bf16 v[48:63], a[228:231], v[20:23], v[48:63]\n"
    "ds_read_b128 v[12:15], v6 offset:33024\n"
    "v_xor_b32 v7, 0x40, v0\n"
    "ds_read_b128 v[24:27], v7 offset:49152\n"
    "buffer_load_dwordx4 v2, s[56:59], s69 offen offset:1024 lds\n"
    "v_mfma_f32_32x32x16_bf16 v[64:79], v[20:23], v[228:231], v[64:79]\n"
    "s_waitcnt lgkmcnt(2)\n"
    "v_mfma_f32_32x32x16_bf16 v[32:47], a[100:103], v[28:31], v[32:47]\n"
    "buffer_load_dwordx4 v3, s[56:59], s69 offen offset:2048 lds\n"
    "v_mfma_f32_32x32x16_bf16 v[48:63], a[200:203], v[8:11], v[48:63]\n"
    "v_xor_b32 v6, 0x60, v0\n"
    "ds_read_b128 v[16:19], v6 offset:32768\n"
    "ds_read_b128 v[28:31], v7 offset:49408\n"
    "buffer_load_dwordx4 v4, s[56:59], s69 offen offset:3072 lds\n"
    "v_mfma_f32_32x32x16_bf16 v[64:79], v[8:11], v[200:203], v[64:79]\n"
    "s_waitcnt lgkmcnt(2)\n"
    "v_mfma_f32_32x32x16_bf16 v[32:47], a[72:75], v[24:27], v[32:47]\n"
    "v_mfma_f32_32x32x16_bf16 v[48:63], a[232:235], v[12:15], v[48:63]\n"
    "ds_read_b128 v[20:23], v6 offset:33024\n"
    "v_xor_b32 v7, 0x60, v0\n"
    "ds_read_b128 v[24:27], v7 offset:49152\n"
    "v_mfma_f32_32x32x16_bf16 v[64:79], v[12:15], v[232:235], v[64:79]\n"
    "s_waitcnt lgkmcnt(2)\n"
    "v_mfma_f32_32x32x16_bf16 v[32:47], a[104:107], v[28:31], v[32:47]\n"
    "v_mfma_f32_32x32x16_bf16 v[48:63], a[204:207], v[16:19], v[48:63]\n"
    "v_xor_b32 v6, 0x80, v0\n"
    "ds_read_b128 v[8:11], v6 offset:32768\n"
    "ds_read_b128 v[28:31], v7 offset:49408\n"
    "v_mfma_f32_32x32x16_bf16 v[64:79], v[16:19], v[204:207], v[64:79]\n"
    "s_waitcnt lgkmcnt(2)\n"
    "v_mfma_f32_32x32x16_bf16 v[32:47], a[76:79], v[24:27], v[32:47]\n"
    "v_mfma_f32_32x32x16_bf16 v[48:63], a[236:239], v[20:23], v[48:63]\n"
    "ds_read_b128 v[12:15], v6 offset:33024\n"
    "v_xor_b32 v7, 0x80, v0\n"
    "ds_read_b128 v[24:27], v7 offset:49152\n"
    "v_mfma_f32_32x32x16_bf16 v[64:79], v[20:23], v[236:239], v[64:79]\n"
    "s_waitcnt lgkmcnt(2)\n"
    "v_mfma_f32_32x32x16_bf16 v[32:47], a[108:111], v[28:31], v[32:47]\n"
    "v_mfma_f32_32x32x16_bf16 v[48:63], a[208:211], v[8:11], v[48:63]\n"
    "v_xor_b32 v6, 0xa0, v0\n"
    "ds_read_b128 v[16:19], v6 offset:32768\n"
    "ds_read_b128 v[28:31], v7 offset:49408\n"
    "v_mfma_f32_32x32x16_bf16 v[64:79], v[8:11], v[208:211], v[64:79]\n"
    "s_waitcnt lgkmcnt(2)\n"
    "v_mfma_f32_32x32x16_bf16 v[32:47], a[80:83], v[24:27], v[32:47]\n"
    "v_mfma_f32_32x32x16_bf16 v[48:63], a[240:243], v[12:15], v[48:63]\n"
    "ds_read_b128 v[20:23], v6 offset:33024\n"
    "v_xor_b32 v7, 0xa0, v0\n"
    "ds_read_b128 v[24:27], v7 offset:49152\n"
    "v_mfma_f32_32x32x16_bf16 v[64:79], v[12:15], v[240:243], v[64:79]\n"
    "s_waitcnt lgkmcnt(2)\n"
    "v_mfma_f32_32x32x16_bf16 v[32:47], a[112:115], v[28:31], v[32:47]\n"
    "s_lshl_b32 s68, s37, 6\n"
    "s_add_u32 s72, s68, s75\n"
    "s_add_u32 s71, s68, s76\n"
    "s_lshl_b32 s72, s72, 13\n"
    "s_lshl_b32 s71, s71, 13\n"
    "v_mfma_f32_32x32x16_bf16 v[48:63], a[212:215], v[16:19], v[48:63]\n"
    "v_xor_b32 v6, 0xc0, v0\n"
    "ds_read_b128 v[8:11], v6 offset:32768\n"
    "ds_read_b128 v[28:31], v7 offset:49408\n"
    "v_mfma_f32_32x32x16_bf16 v[64:79], v[16:19], v[212:215], v[64:79]\n"
    "s_waitcnt lgkmcnt(2)\n"
    "v_mfma_f32_32x32x16_bf16 v[32:47], a[84:87], v[24:27], v[32:47]\n"
    "s_add_u32 s71, s71, 0x3800000\n"
    "s_add_u32 s72, s72, 0x4800000\n"
    "s_add_u32 s73, s72, 0x1000000\n"
    "s_mov_b32 s75, s79\n"
    "s_mov_b32 s76, s78\n"
    "s_add_u32 s74, s74, 1\n"
    "v_mfma_f32_32x32x16_bf16 v[48:63], a[244:247], v[20:23], v[48:63]\n"
    "ds_read_b128 v[12:15], v6 offset:33024\n"
    "v_xor_b32 v7, 0xc0, v0\n"
    "ds_read_b128 v[24:27], v7 offset:49152\n"
    "v_mfma_f32_32x32x16_bf16 v[64:79], v[20:23], v[244:247], v[64:79]\n"
    "s_waitcnt lgkmcnt(2)\n"
    "v_mfma_f32_32x32x16_bf16 v[32:47], a[116:119], v[28:31], v[32:47]\n"
    "v_mfma_f32_32x32x16_bf16 v[48:63], a[216:219], v[8:11], v[48:63]\n"
    "v_xor_b32 v6, 0xe0, v0\n"
    "ds_read_b128 v[16:19], v6 offset:32768\n"
    "ds_read_b128 v[28:31], v7 offset:49408\n"
    "v_mfma_f32_32x32x16_bf16 v[64:79], v[8:11], v[216:219], v[64:79]\n"
    "s_waitcnt lgkmcnt(2)\n"
    "v_mfma_f32_32x32x16_bf16 v[32:47], a[88:91], v[24:27], v[32:47]\n"
    "v_mfma_f32_32x32x16_bf16 v[48:63], a[248:251], v[12:15], v[48:63]\n"
    "ds_read_b128 v[20:23], v6 offset:33024\n"
    "v_xor_b32 v7, 0xe0, v0\n"
    "ds_read_b128 v[24:27], v7 offset:49152\n"
    "v_mfma_f32_32x32x16_bf16 v[64:79], v[12:15], v[248:251], v[64:79]\n"
    "s_waitcnt lgkmcnt(2)\n"
    "v_mfma_f32_32x32x16_bf16 v[32:47], a[120:123], v[28:31], v[32:47]\n"
    "s_waitcnt vmcnt(14)\n"
    "s_barrier\n"
    "v_mfma_f32_32x32x16_bf16 v[48:63], a[220:223], v[16:19], v[48:63]\n"
    "v_xor_b32 v6, 0x10000, v0\n"
    "ds_read_b128 v[8:11], v6 offset:0\n"
    "ds_read_b128 v[28:31], v7 offset:49408\n"
    "v_mfma_f32_32x32x16_bf16 v[64:79], v[16:19], v[220:223], v[64:79]\n"
    "s_waitcnt lgkmcnt(2)\n"
    "v_mfma_f32_32x32x16_bf16 v[32:47], a[92:95], v[24:27], v[32:47]\n"
    "v_mfma_f32_32x32x16_bf16 v[48:63], a[252:255], v[20:23], v[48:63]\n"
    "ds_read_b128 v[12:15], v6 offset:256\n"
    "v_xor_b32 v7, 0x10000, v0\n"
    "ds_read_b128 v[24:27], v7 offset:16384\n"
    "v_mfma_f32_32x32x16_bf16 v[64:79], v[20:23], v[252:255], v[64:79]\n"
    "s_waitcnt lgkmcnt(2)\n"
    "v_mfma_f32_32x32x16_bf16 v[32:47], a[124:127], v[28:31], v[32:47]\n"
    "v_mfma_f32_32x32x16_bf16 v[96:111], a[128:131], v[8:11], 0\n"
    "v_xor_b32 v6, 0x10020, v0\n"
    "ds_read_b128 v[16:19], v6 offset:0\n"
    "ds_read_b128 v[28:31], v7 offset:16640\n"
    "s_add_u32 s68, s74, 1\n"
    "s_min_u32 s68, s68, 15\n"
    "s_add_u32 s79, s68, s38\n"
    "s_add_u32 s78, s68, s39\n"
    "s_add_u32 s69, s68, s64\n"
    "s_sub_u32 s69, s69, 8\n"
    "s_cmp_lt_u32 s68, 8\n"
    "s_cselect_b32 s78, s78, s69\n"
    "s_lshl_b32 s70, s79, 15\n"
    "s_add_u32 s70, s70, s77\n"
    "s_lshl_b32 s69, s78, 15\n"
    "s_add_u32 s69, s69, s77\n"
    "v_mfma_f32_32x32x16_bf16 v[112:127], v[8:11], v[128:131], 0\n"
    "s_waitcnt lgkmcnt(2)\n"
    "v_mfma_f32_32x32x16_bf16 v[80:95], a[0:3], v[24:27], 0\n"
    "s_add_u32 m0, s67, 0\n"
    "s_nop 0\n"
    "buffer_load_dwordx4 v1, s[60:63], s70 offen offset:0 lds\n"
    "v_mfma_f32_32x32x16_bf16 v[96:111], a[160:163], v[12:15], v[96:111]\n"
    "ds_read_b128 v[20:23], v6 offset:256\n"
    "v_xor_b32 v7, 0x10020, v0\n"
    "ds_read_b128 v[24:27], v7 offset:16384\n"
    "buffer_load_dwordx4 v2, s[60:63], s70 offen offset:1024 lds\n"
    "v_mfma_f32_32x32x16_bf16 v[112:127], v[12:15], v[160:163], v[112:127]\n"
    "s_waitcnt lgkmcnt(2)\n"
    "v_mfma_f32_32x32x16_bf16 v[80:95], a[32:35], v[28:31], v[80:95]\n"
    "buffer_load_dwordx4 v3, s[60:63], s70 offen offset:2048 lds\n"
    "v_mfma_f32_32x32x16_bf16 v[96:111], a[132:135], v[16:19], v[96:111]\n"
    "v_xor_b32 v6, 0x10040, v0\n"
    "ds_read_b128 v[8:11], v6 offset:0\n"
    "ds_read_b128 v[28:31], v7 offset:16640\n"
    "buffer_load_dwordx4 v4, s[60:63], s70 offen offset:3072 lds\n"
    "v_mfma_f32_32x32x16_bf16 v[112:127], v[16:19], v[132:135], v[112:127]\n"
    "s_waitcnt lgkmcnt(2)\n"
    "v_mfma_f32_32x32x16_bf16 v[80:95], a[4:7], v[24:27], v[80:95]\n"
    "s_add_u32 m0, s67, 16384\n"
    "s_nop 0\n"
    "buffer_load_dwordx4 v1, s[56:59], s69 offen offset:0 lds\n"
    "v_mfma_f32_32x32x16_bf16 v[96:111], a[164:167], v[20:23], v[96:111]\n"
    "ds_read_b128 v[12:15], v6 offset:256\n"
    "v_xor_b32 v7, 0x10040, v0\n"
    "ds_read_b128 v[24:27], v7 offset:16384\n"
    "buffer_load_dwordx4 v2, s[56:59], s69 offen offset:1024 lds\n"
    "v_mfma_f32_32x32x16_bf16 v[112:127], v[20:23], v[164:167], v[112:127]\n"
    "s_waitcnt lgkmcnt(2)\n"
    "v_mfma_f32_32x32x16_bf16 v[80:95], a[36:39], v[28:31], v[80:95]\n"
    "buffer_load_dwordx4 v3, s[56:59], s69 offen offset:2048 lds\n"
    "v_mfma_f32_32x32x16_bf16 v[96:111], a[136:139], v[8:11], v[96:111]\n"
    "v_xor_b32 v6, 0x10060, v0\n"
    "ds_read_b128 v[16:19], v6 offset:0\n"
    "ds_read_b128 v[28:31], v7 offset:16640\n"
    "buffer_load_dwordx4 v4, s[56:59], s69 offen offset:3072 lds\n"
    "v_mfma_f32_32x32x16_bf16 v[112:127], v[8:11], v[136:139], v[112:127]\n"
    "s_waitcnt lgkmcnt(2)\n"
    "v_mfma_f32_32x32x16_bf16 v[80:95], a[8:11], v[24:27], v[80:95]\n"
    "v_mfma_f32_32x32x16_bf16 v[96:111], a[168:171], v[12:15], v[96:111]\n"
    "ds_read_b128 v[20:23], v6 offset:256\n"
    "v_xor_b32 v7, 0x10060, v0\n"
    "ds_read_b128 v[24:27], v7 offset:16384\n"
    "v_cvt_pk_bf16_f32 v48, v48, v49\n"
    "v_mfma_f32_32x32x16_bf16 v[112:127], v[12:15], v[168:171], v[112:127]\n"
    "v_cvt_pk_bf16_f32 v49, v50, v51\n"
    "v_cvt_pk_bf16_f32 v50, v52, v53\n"
    "v_cvt_pk_bf16_f32 v51, v54, v55\n"
    "s_waitcnt lgkmcnt(2)\n"
    "v_mfma_f32_32x32x16_bf16 v[80:95], a[40:43], v[28:31], v[80:95]\n"
    "buffer_store_dwordx4 v[48:51], v5, s[40:43], s72 offen offset:0 sc1\n"
    "v_cvt_pk_bf16_f32 v56, v56, v57\n"
    "v_cvt_pk_bf16_f32 v57, v58, v59\n"
    "v_mfma_f32_32x32x16_bf16 v[96:111], a[140:143], v[16:19], v[96:111]\n"
    "v_xor_b32 v6, 0x10080, v0\n"
    "ds_read_b128 v[8:11], v6 offset:0\n"
    "ds_read_b128 v[28:31], v7 offset:16640\n"
    "v_cvt_pk_bf16_f32 v58, v60, v61\n"
    "v_mfma_f32_32x32x16_bf16 v[112:127], v[16:19], v[140:143], v[112:127]\n"
    "v_cvt_pk_bf16_f32 v59, v62, v63\n"
    "buffer_store_dwordx4 v[56:59], v5, s[40:43], s72 offen offset:1024 sc1\n"
    "v_cvt_pk_bf16_f32 v64, v64, v65\n"
    "s_waitcnt lgkmcnt(2)\n"
    "v_mfma_f32_32x32x16_bf16 v[80:95], a[12:15], v[24:27], v[80:95]\n"
    "v_cvt_pk_bf16_f32 v65, v66, v67\n"
    "v_cvt_pk_bf16_f32 v66, v68, v69\n"
    "v_cvt_pk_bf16_f32 v67, v70, v71\n"
    "v_mfma_f32_32x32x16_bf16 v[96:111], a[172:175], v[20:23], v[96:111]\n"
    "ds_read_b128 v[12:15], v6 offset:256\n"
    "v_xor_b32 v7, 0x10080, v0\n"
    "ds_read_b128 v[24:27], v7 offset:16384\n"
    "buffer_store_dwordx4 v[64:67], v5, s[40:43], s73 offen offset:0 sc1\n"
    "v_mfma_f32_32x32x16_bf16 v[112:127], v[20:23], v[172:175], v[112:127]\n"
    "v_cvt_pk_bf16_f32 v72, v72, v73\n"
    "v_cvt_pk_bf16_f32 v73, v74, v75\n"
    "v_cvt_pk_bf16_f32 v74, v76, v77\n"
    "s_waitcnt lgkmcnt(2)\n"
    "v_mfma_f32_32x32x16_bf16 v[80:95], a[44:47], v[28:31], v[80:95]\n"
    "v_cvt_pk_bf16_f32 v75, v78, v79\n"
    "buffer_store_dwordx4 v[72:75], v5, s[40:43], s73 offen offset:1024 sc1\n"
    "v_mul_f32 v32, s82, v32\n"
    "v_mfma_f32_32x32x16_bf16 v[96:111], a[144:147], v[8:11], v[96:111]\n"
    "v_xor_b32 v6, 0x100a0, v0\n"
    "ds_read_b128 v[16:19], v6 offset:0\n"
    "ds_read_b128 v[28:31], v7 offset:16640\n"
    "v_mul_f32 v33, s82, v33\n"
    "v_mfma_f32_32x32x16_bf16 v[112:127], v[8:11], v[144:147], v[112:127]\n"
    "v_mul_f32 v34, s82, v34\n"
    "v_mul_f32 v35, s82, v35\n"
    "v_mul_f32 v36, s82, v36\n"
    "s_waitcnt lgkmcnt(2)\n"
    "v_mfma_f32_32x32x16_bf16 v[80:95], a[16:19], v[24:27], v[80:95]\n"
    "v_mul_f32 v37, s82, v37\n"
    "v_mul_f32 v38, s82, v38\n"
    "v_mul_f32 v39, s82, v39\n"
    "v_mfma_f32_32x32x16_bf16 v[96:111], a[176:179], v[12:15], v[96:111]\n"
    "ds_read_b128 v[20:23], v6 offset:256\n"
    "v_xor_b32 v7, 0x100a0, v0\n"
    "ds_read_b128 v[24:27], v7 offset:16384\n"
    "v_cvt_pk_bf16_f32 v32, v32, v33\n"
    "v_mfma_f32_32x32x16_bf16 v[112:127], v[12:15], v[176:179], v[112:127]\n"
    "v_cvt_pk_bf16_f32 v33, v34, v35\n"
    "v_cvt_pk_bf16_f32 v34, v36, v37\n"
    "v_cvt_pk_bf16_f32 v35, v38, v39\n"
    "s_waitcnt lgkmcnt(2)\n"
    "v_mfma_f32_32x32x16_bf16 v[80:95], a[48:51], v[28:31], v[80:95]\n"
    "buffer_store_dwordx4 v[32:35], v5, s[40:43], s71 offen offset:0 sc1\n"
    "v_mul_f32 v40, s82, v40\n"
    "v_mul_f32 v41, s82, v41\n"
    "v_mfma_f32_32x32x16_bf16 v[96:111], a[148:151], v[16:19], v[96:111]\n"
    "v_xor_b32 v6, 0x100c0, v0\n"
    "ds_read_b128 v[8:11], v6 offset:0\n"
    "ds_read_b128 v[28:31], v7 offset:16640\n"
    "v_mul_f32 v42, s82, v42\n"
    "v_mfma_f32_32x32x16_bf16 v[112:127], v[16:19], v[148:151], v[112:127]\n"
    "v_mul_f32 v43, s82, v43\n"
    "v_mul_f32 v44, s82, v44\n"
    "v_mul_f32 v45, s82, v45\n"
    "s_waitcnt lgkmcnt(2)\n"
    "v_mfma_f32_32x32x16_bf16 v[80:95], a[20:23], v[24:27], v[80:95]\n"
    "v_mul_f32 v46, s82, v46\n"
    "v_mul_f32 v47, s82, v47\n"
    "v_cvt_pk_bf16_f32 v40, v40, v41\n"
    "v_mfma_f32_32x32x16_bf16 v[96:111], a[180:183], v[20:23], v[96:111]\n"
    "ds_read_b128 v[12:15], v6 offset:256\n"
    "v_xor_b32 v7, 0x100c0, v0\n"
    "ds_read_b128 v[24:27], v7 offset:16384\n"
    "v_cvt_pk_bf16_f32 v41, v42, v43\n"
    "v_mfma_f32_32x32x16_bf16 v[112:127], v[20:23], v[180:183], v[112:127]\n"
    "v_cvt_pk_bf16_f32 v42, v44, v45\n"
    "v_cvt_pk_bf16_f32 v43, v46, v47\n"
    "buffer_store_dwordx4 v[40:43], v5, s[40:43], s71 offen offset:1024 sc1\n"
    "s_waitcnt lgkmcnt(2)\n"
    "v_mfma_f32_32x32x16_bf16 v[80:95], a[52:55], v[28:31], v[80:95]\n"
    "v_mfma_f32_32x32x16_bf16 v[96:111], a[152:155], v[8:11], v[96:111]\n"
    "v_xor_b32 v6, 0x100e0, v0\n"
    "ds_read_b128 v[16:19], v6 offset:0\n"
    "ds_read_b128 v[28:31], v7 offset:16640\n"
    "v_mfma_f32_32x32x16_bf16 v[112:127], v[8:11], v[152:155], v[112:127]\n"
    "s_waitcnt lgkmcnt(2)\n"
    "v_mfma_f32_32x32x16_bf16 v[80:95], a[24:27], v[24:27], v[80:95]\n"
    "v_mfma_f32_32x32x16_bf16 v[96:111], a[184:187], v[12:15], v[96:111]\n"
    "ds_read_b128 v[20:23], v6 offset:256\n"
    "v_xor_b32 v7, 0x100e0, v0\n"
    "ds_read_b128 v[24:27], v7 offset:16384\n"
    "v_mfma_f32_32x32x16_bf16 v[112:127], v[12:15], v[184:187], v[112:127]\n"
    "s_waitcnt lgkmcnt(2)\n"
    "v_mfma_f32_32x32x16_bf16 v[80:95], a[56:59], v[28:31], v[80:95]\n"
    "s_waitcnt vmcnt(14)\n"
    "s_barrier\n"
    "v_mfma_f32_32x32x16_bf16 v[96:111], a[156:159], v[16:19], v[96:111]\n"
    "v_xor_b32 v6, 0x10000, v0\n"
    "ds_read_b128 v[8:11], v6 offset:32768\n"
    "ds_read_b128 v[28:31], v7 offset:16640\n"
    "v_mfma_f32_32x32x16_bf16 v[112:127], v[16:19], v[156:159], v[112:127]\n"
    "s_waitcnt lgkmcnt(2)\n"
    "v_mfma_f32_32x32x16_bf16 v[80:95], a[28:31], v[24:27], v[80:95]\n"
    "v_mfma_f32_32x32x16_bf16 v[96:111], a[188:191], v[20:23], v[96:111]\n"
    "ds_read_b128 v[12:15], v6 offset:33024\n"
    "v_xor_b32 v7, 0x10000, v0\n"
    "ds_read_b128 v[24:27], v7 offset:49152\n"
    "v_mfma_f32_32x32x16_bf16 v[112:127], v[20:23], v[188:191], v[112:127]\n"
    "s_waitcnt lgkmcnt(2)\n"
    "v_mfma_f32_32x32x16_bf16 v[80:95], a[60:63], v[28:31], v[80:95]\n"
    "v_mfma_f32_32x32x16_bf16 v[96:111], a[192:195], v[8:11], v[96:111]\n"
    "v_xor_b32 v6, 0x10020, v0\n"
    "ds_read_b128 v[16:19], v6 offset:32768\n"
    "ds_read_b128 v[28:31], v7 offset:49408\n"
    "v_mfma_f32_32x32x16_bf16 v[112:127], v[8:11], v[192:195], v[112:127]\n"
    "s_waitcnt lgkmcnt(2)\n"
    "v_mfma_f32_32x32x16_bf16 v[80:95], a[64:67], v[24:27], v[80:95]\n"
    "s_add_u32 m0, s67, 32768\n"
    "s_add_u32 s70, s70, 512\n"
    "buffer_load_dwordx4 v1, s[60:63], s70 offen offset:0 lds\n"
    "v_mfma_f32_32x32x16_bf16 v[96:111], a[224:227], v[12:15], v[96:111]\n"
    "ds_read_b128 v[20:23], v6 offset:33024\n"
    "v_xor_b32 v7, 0x10020, v0\n"
    "ds_read_b128 v[24:27], v7 offset:49152\n"
    "buffer_load_dwordx4 v2, s[60:63], s70 offen offset:1024 lds\n"
    "v_mfma_f32_32x32x16_bf16 v[112:127], v[12:15], v[224:227], v[112:127]\n"
    "s_waitcnt lgkmcnt(2)\n"
    "v_mfma_f32_32x32x16_bf16 v[80:95], a[96:99], v[28:31], v[80:95]\n"
    "buffer_load_dwordx4 v3, s[60:63], s70 offen offset:2048 lds\n"
    "v_mfma_f32_32x32x16_bf16 v[96:111], a[196:199], v[16:19], v[96:111]\n"
    "v_xor_b32 v6, 0x10040, v0\n"
    "ds_read_b128 v[8:11], v6 offset:32768\n"
    "ds_read_b128 v[28:31], v7 offset:49408\n"
    "buffer_load_dwordx4 v4, s[60:63], s70 offen offset:3072 lds\n"
    "v_mfma_f32_32x32x16_bf16 v[112:127], v[16:19], v[196:199], v[112:127]\n"
    "s_waitcnt lgkmcnt(2)\n"
    "v_mfma_f32_32x32x16_bf16 v[80:95], a[68:71], v[24:27], v[80:95]\n"
    "s_add_u32 m0, s67, 49152\n"
    "s_add_u32 s69, s69, 512\n"
    "buffer_load_dwordx4 v1, s[56:59], s69 offen offset:0 lds\n"
    "v_mfma_f32_32x32x16_bf16 v[96:111], a[228:231], v[20:23], v[96:111]\n"
    "ds_read_b128 v[12:15], v6 offset:33024\n"
    "v_xor_b32 v7, 0x10040, v0\n"
    "ds_read_b128 v[24:27], v7 offset:49152\n"
    "buffer_load_dwordx4 v2, s[56:59], s69 offen offset:1024 lds\n"
    "v_mfma_f32_32x32x16_bf16 v[112:127], v[20:23], v[228:231], v[112:127]\n"
    "s_waitcnt lgkmcnt(2)\n"
    "v_mfma_f32_32x32x16_bf16 v[80:95], a[100:103], v[28:31], v[80:95]\n"
    "buffer_load_dwordx4 v3, s[56:59], s69 offen offset:2048 lds\n"
    "v_mfma_f32_32x32x16_bf16 v[96:111], a[200:203], v[8:11], v[96:111]\n"
    "v_xor_b32 v6, 0x10060, v0\n"
    "ds_read_b128 v[16:19], v6 offset:32768\n"
    "ds_read_b128 v[28:31], v7 offset:49408\n"
    "buffer_load_dwordx4 v4, s[56:59], s69 offen offset:3072 lds\n"
    "v_mfma_f32_32x32x16_bf16 v[112:127], v[8:11], v[200:203], v[112:127]\n"
    "s_waitcnt lgkmcnt(2)\n"
    "v_mfma_f32_32x32x16_bf16 v[80:95], a[72:75], v[24:27], v[80:95]\n"
    "v_mfma_f32_32x32x16_bf16 v[96:111], a[232:235], v[12:15], v[96:111]\n"
    "ds_read_b128 v[20:23], v6 offset:33024\n"
    "v_xor_b32 v7, 0x10060, v0\n"
    "ds_read_b128 v[24:27], v7 offset:49152\n"
    "v_mfma_f32_32x32x16_bf16 v[112:127], v[12:15], v[232:235], v[112:127]\n"
    "s_waitcnt lgkmcnt(2)\n"
    "v_mfma_f32_32x32x16_bf16 v[80:95], a[104:107], v[28:31], v[80:95]\n"
    "v_mfma_f32_32x32x16_bf16 v[96:111], a[204:207], v[16:19], v[96:111]\n"
    "v_xor_b32 v6, 0x10080, v0\n"
    "ds_read_b128 v[8:11], v6 offset:32768\n"
    "ds_read_b128 v[28:31], v7 offset:49408\n"
    "v_mfma_f32_32x32x16_bf16 v[112:127], v[16:19], v[204:207], v[112:127]\n"
    "s_waitcnt lgkmcnt(2)\n"
    "v_mfma_f32_32x32x16_bf16 v[80:95], a[76:79], v[24:27], v[80:95]\n"
    "v_mfma_f32_32x32x16_bf16 v[96:111], a[236:239], v[20:23], v[96:111]\n"
    "ds_read_b128 v[12:15], v6 offset:33024\n"
    "v_xor_b32 v7, 0x10080, v0\n"
    "ds_read_b128 v[24:27], v7 offset:49152\n"
    "v_mfma_f32_32x32x16_bf16 v[112:127], v[20:23], v[236:239], v[112:127]\n"
    "s_waitcnt lgkmcnt(2)\n"
    "v_mfma_f32_32x32x16_bf16 v[80:95], a[108:111], v[28:31], v[80:95]\n"
    "v_mfma_f32_32x32x16_bf16 v[96:111], a[208:211], v[8:11], v[96:111]\n"
    "v_xor_b32 v6, 0x100a0, v0\n"
    "ds_read_b128 v[16:19], v6 offset:32768\n"
    "ds_read_b128 v[28:31], v7 offset:49408\n"
    "v_mfma_f32_32x32x16_bf16 v[112:127], v[8:11], v[208:211], v[112:127]\n"
    "s_waitcnt lgkmcnt(2)\n"
    "v_mfma_f32_32x32x16_bf16 v[80:95], a[80:83], v[24:27], v[80:95]\n"
    "v_mfma_f32_32x32x16_bf16 v[96:111], a[240:243], v[12:15], v[96:111]\n"
    "ds_read_b128 v[20:23], v6 offset:33024\n"
    "v_xor_b32 v7, 0x100a0, v0\n"
    "ds_read_b128 v[24:27], v7 offset:49152\n"
    "v_mfma_f32_32x32x16_bf16 v[112:127], v[12:15], v[240:243], v[112:127]\n"
    "s_waitcnt lgkmcnt(2)\n"
    "v_mfma_f32_32x32x16_bf16 v[80:95], a[112:115], v[28:31], v[80:95]\n"
    "s_lshl_b32 s68, s37, 6\n"
    "s_add_u32 s72, s68, s75\n"
    "s_add_u32 s71, s68, s76\n"
    "s_lshl_b32 s72, s72, 13\n"
    "s_lshl_b32 s71, s71, 13\n"
    "v_mfma_f32_32x32x16_bf16 v[96:111], a[212:215], v[16:19], v[96:111]\n"
    "v_xor_b32 v6, 0x100c0, v0\n"
    "ds_read_b128 v[8:11], v6 offset:32768\n"
    "ds_read_b128 v[28:31], v7 offset:49408\n"
    "v_mfma_f32_32x32x16_bf16 v[112:127], v[16:19], v[212:215], v[112:127]\n"
    "s_waitcnt lgkmcnt(2)\n"
    "v_mfma_f32_32x32x16_bf16 v[80:95], a[84:87], v[24:27], v[80:95]\n"
    "s_add_u32 s71, s71, 0x3800000\n"
    "s_add_u32 s72, s72, 0x4800000\n"
    "s_add_u32 s73, s72, 0x1000000\n"
    "s_mov_b32 s75, s79\n"
    "s_mov_b32 s76, s78\n"
    "s_add_u32 s74, s74, 1\n"
    "v_mfma_f32_32x32x16_bf16 v[96:111], a[244:247], v[20:23], v[96:111]\n"
    "ds_read_b128 v[12:15], v6 offset:33024\n"
    "v_xor_b32 v7, 0x100c0, v0\n"
    "ds_read_b128 v[24:27], v7 offset:49152\n"
    "v_mfma_f32_32x32x16_bf16 v[112:127], v[20:23], v[244:247], v[112:127]\n"
    "s_waitcnt lgkmcnt(2)\n"
    "v_mfma_f32_32x32x16_bf16 v[80:95], a[116:119], v[28:31], v[80:95]\n"
    "v_mfma_f32_32x32x16_bf16 v[96:111], a[216:219], v[8:11], v[96:111]\n"
    "v_xor_b32 v6, 0x100e0, v0\n"
    "ds_read_b128 v[16:19], v6 offset:32768\n"
    "ds_read_b128 v[28:31], v7 offset:49408\n"
    "v_mfma_f32_32x32x16_bf16 v[112:127], v[8:11], v[216:219], v[112:127]\n"
    "s_waitcnt lgkmcnt(2)\n"
    "v_mfma_f32_32x32x16_bf16 v[80:95], a[88:91], v[24:27], v[80:95]\n"
    "v_mfma_f32_32x32x16_bf16 v[96:111], a[248:251], v[12:15], v[96:111]\n"
    "ds_read_b128 v[20:23], v6 offset:33024\n"
    "v_xor_b32 v7, 0x100e0, v0\n"
    "ds_read_b128 v[24:27], v7 offset:49152\n"
    "v_mfma_f32_32x32x16_bf16 v[112:127], v[12:15], v[248:251], v[112:127]\n"
    "s_waitcnt lgkmcnt(2)\n"
    "v_mfma_f32_32x32x16_bf16 v[80:95], a[120:123], v[28:31], v[80:95]\n"
    "s_waitcnt vmcnt(14)\n"
    "s_barrier\n"
    "v_mfma_f32_32x32x16_bf16 v[96:111], a[220:223], v[16:19], v[96:111]\n"
    "v_xor_b32 v6, 0x0, v0\n"
    "ds_read_b128 v[8:11], v6 offset:0\n"
    "ds_read_b128 v[28:31], v7 offset:49408\n"
    "v_mfma_f32_32x32x16_bf16 v[112:127], v[16:19], v[220:223], v[112:127]\n"
    "s_waitcnt lgkmcnt(2)\n"
    "v_mfma_f32_32x32x16_bf16 v[80:95], a[92:95], v[24:27], v[80:95]\n"
    "v_mfma_f32_32x32x16_bf16 v[96:111], a[252:255], v[20:23], v[96:111]\n"
    "ds_read_b128 v[12:15], v6 offset:256\n"
    "v_xor_b32 v7, 0x0, v0\n"
    "ds_read_b128 v[24:27], v7 offset:16384\n"
    "v_mfma_f32_32x32x16_bf16 v[112:127], v[20:23], v[252:255], v[112:127]\n"
    "s_waitcnt lgkmcnt(2)\n"
    "v_mfma_f32_32x32x16_bf16 v[80:95], a[124:127], v[28:31], v[80:95]\n"
    "s_cmp_lt_u32 s74, 16\n"
    "s_cbranch_scc1 .Lp2_loop_%=\n"
    "s_nop 11\n"
    "v_cvt_pk_bf16_f32 v96, v96, v97\n"
    "v_cvt_pk_bf16_f32 v97, v98, v99\n"
    "v_cvt_pk_bf16_f32 v98, v100, v101\n"
    "v_cvt_pk_bf16_f32 v99, v102, v103\n"
    "buffer_store_dwordx4 v[96:99], v5, s[40:43], s72 offen offset:0 sc1\n"
    "v_cvt_pk_bf16_f32 v104, v104, v105\n"
    "v_cvt_pk_bf16_f32 v105, v106, v107\n"
    "v_cvt_pk_bf16_f32 v106, v108, v109\n"
    "v_cvt_pk_bf16_f32 v107, v110, v111\n"
    "buffer_store_dwordx4 v[104:107], v5, s[40:43], s72 offen offset:1024 sc1\n"
    "v_cvt_pk_bf16_f32 v112, v112, v113\n"
    "v_cvt_pk_bf16_f32 v113, v114, v115\n"
    "v_cvt_pk_bf16_f32 v114, v116, v117\n"
    "v_cvt_pk_bf16_f32 v115, v118, v119\n"
    "buffer_store_dwordx4 v[112:115], v5, s[40:43], s73 offen offset:0 sc1\n"
    "v_cvt_pk_bf16_f32 v120, v120, v121\n"
    "v_cvt_pk_bf16_f32 v121, v122, v123\n"
    "v_cvt_pk_bf16_f32 v122, v124, v125\n"
    "v_cvt_pk_bf16_f32 v123, v126, v127\n"
    "buffer_store_dwordx4 v[120:123], v5, s[40:43], s73 offen offset:1024 sc1\n"
    "v_mul_f32 v80, s82, v80\n"
    "v_mul_f32 v81, s82, v81\n"
    "v_mul_f32 v82, s82, v82\n"
    "v_mul_f32 v83, s82, v83\n"
    "v_mul_f32 v84, s82, v84\n"
    "v_mul_f32 v85, s82, v85\n"
    "v_mul_f32 v86, s82, v86\n"
    "v_mul_f32 v87, s82, v87\n"
    "v_cvt_pk_bf16_f32 v80, v80, v81\n"
    "v_cvt_pk_bf16_f32 v81, v82, v83\n"
    "v_cvt_pk_bf16_f32 v82, v84, v85\n"
    "v_cvt_pk_bf16_f32 v83, v86, v87\n"
    "buffer_store_dwordx4 v[80:83], v5, s[40:43], s71 offen offset:0 sc1\n"
    "v_mul_f32 v88, s82, v88\n"
    "v_mul_f32 v89, s82, v89\n"
    "v_mul_f32 v90, s82, v90\n"
    "v_mul_f32 v91, s82, v91\n"
    "v_mul_f32 v92, s82, v92\n"
    "v_mul_f32 v93, s82, v93\n"
    "v_mul_f32 v94, s82, v94\n"
    "v_mul_f32 v95, s82, v95\n"
    "v_cvt_pk_bf16_f32 v88, v88, v89\n"
    "v_cvt_pk_bf16_f32 v89, v90, v91\n"
    "v_cvt_pk_bf16_f32 v90, v92, v93\n"
    "v_cvt_pk_bf16_f32 v91, v94, v95\n"
    "buffer_store_dwordx4 v[88:91], v5, s[40:43], s71 offen offset:1024 sc1\n"
    "s_waitcnt vmcnt(0) lgkmcnt(0)\n"
    PROJ_OPS);
}

template <bool STREAM>
DI void oproj(rsrc_t rw, rsrc_t ws, rsrc_t ro, u32 wsO, int cg, u32 ocol, int u0, int nu, int ustep, int wid, int lane) {
    const u32 scr = 16384 + wid * SCR_BYTES;
    {
        const u32 vo = (wid * 1024 + (lane >> 2)) * 1024 + cg * 64 + (lane & 3) * 16, wad = scr + lane * 16, rad = scr + tr_base<2>(lane);
        auto issue = [&](auto c) { sfor<4>([&](auto i) { gld_a<16 * CI(c) + 4 * CI(i)>(vo, rw, (CI(c) * 64 + 16 * CI(i)) * 1024); }); };
        sfor<15>(issue);
        sfor<16>([&](auto c) {
            constexpr int C = CI(c);
            vmwait<4 * (C == 0 ? 14 : 15 - C)>();
            chunk_a<2, 16 * C>(wad, rad);
            if constexpr (C == 0) issue(std::integral_constant<int, 15>{});
        });
        lgkm0();
    }
    const u32 vo = wsO + wid * 32768 + lane * 16;
    const u32 radd = ((lane >> 2) & 1) * 1024 + ((lane & 3) * 16 + (wid & 1) * 8 + (lane >> 3)) * 16;
    const u32 oo = wid < 2 ? ((wid & 1) * 8 + (lane >> 3)) * 2048 + ocol + cg * 64 + (lane & 7) * 8 : OOB;
    u32x4 x[32];
    if constexpr (!STREAM) {
        wait_words(ws, lane < 32 ? W_FBA + (u0 >> 4) * 512 + wid * 128 + lane * 4 : OOB, 1, lane);
        sfor<32>([&](auto k) { x[CI(k)] = gld128(ws, vo + (CI(k) & 3) * 1024, u0 * 131072 + (CI(k) >> 2) * 4096); });
    }
    for (int i = 0; i < nu; ++i) {
        const int u = u0 + i * ustep;
        if constexpr (STREAM) {
            wait_words(ws, lane < 8 ? W_FF1 + ((i & 1) * 32 + wid * 8 + lane) * 128 : OOB, i + 1, lane);
            sfor<32>([&](auto k) { x[CI(k)] = gld128(ws, vo + (CI(k) & 3) * 1024, u * 131072 + (CI(k) >> 2) * 4096); });
        }
        const int un = i + 1 < nu ? u + ustep : u;
        f32x4 d0, d1;
        sfor<32>([&](auto k) {
            constexpr int K = CI(k);
            m16_av<8 * K, K == 0>(d0, x[K]); m16_av<8 * K + 4, K == 0>(d1, x[K]);
            if constexpr (!STREAM) x[K] = gld128(ws, vo + (K & 3) * 1024, un * 131072 + (K >> 2) * 4096);
            SB();
        });
        nop<7>(d0); pin(d1);
        const u32 lb = (i & 1) * 8192;
        ldsw128(lb + wid * 2048 + lane * 16, __builtin_bit_cast(u32x4, d0)); ldsw128(lb + wid * 2048 + 1024 + lane * 16, __builtin_bit_cast(u32x4, d1));
        bar();
        if (wid < 2) {
            f32x4 s = __builtin_bit_cast(f32x4, ldsr128(lb + radd));
            sfor<3>([&](auto j) { s += __builtin_bit_cast(f32x4, ldsr128(lb + radd + (CI(j) + 1) * 2048)); });
            __builtin_amdgcn_raw_buffer_store_b64(u32x2{cvtpk(s[0], s[1]), cvtpk(s[2], s[3])}, ro, oo, u * 32768, 0);
        }
    }
}

#define F1_EARLY 0
DI void role_f1(rsrc_t ws, int id, int lane) {
    const int h = id & 31, par = id >> 5, c = lane & 31, hi = lane >> 5;
    const u32 vo = lane * 16, fH = lane < 4 ? W_FH + h * 128 + lane * 4 : OOB, fC = lane < 4 ? W_FC + h * 128 + lane * 4 : OOB;
    const u32 fo = lane == 0 ? W_FF1 + id * 128 : OOB;
    const u32 vkm = c < 8 ? WS_KM + hi * 128 + c * 16 : OOB, vvm = WS_VM + c * 16 + hi * 8;
    const u32 voo = WS_OA + (c >> 4) * 131072 + h * 4096 + hi * 256 + (c & 15) * 16;
    const float NEG = -__builtin_inff();
    for (int s = par; s < 64; s += 2) {
        wait_words(ws, fH, s + 1, lane);
        if (s < F1_EARLY) wait_words(ws, fC, s + 1, lane);
        const u32 so = (h * 64 + s) * 8192;
        u32x4 q[8], k[8], v[8];
        sfor<8>([&](auto i) { q[CI(i)] = gld128(ws, vo + (CI(i) & 3) * 1024, WS_QA + so + (CI(i) >> 2) * 4096); });
        sfor<8>([&](auto i) { k[CI(i)] = gld128(ws, vo + (CI(i) & 3) * 1024, WS_KA + so + (CI(i) >> 2) * 4096); });
        sfor<8>([&](auto i) { v[CI(i)] = gld128(ws, vo + (CI(i) & 3) * 1024, WS_VA + so + (CI(i) >> 2) * 4096); });
        f32x16 sx, sm;
        sfor<8>([&](auto i) { m32_vv<CI(i) == 0>(sx, k[CI(i)], q[CI(i)]); });
        nop<11>(sx);
        sfor<16>([&](auto r) { constexpr int R = CI(r); if ((R & 3) + 8 * (R >> 2) + 4 * hi > c) sx[R] = NEG; });
        float mx = sx[0];
        sfor<15>([&](auto r) { mx = fmaxf(mx, sx[CI(r) + 1]); });
        wait_words(ws, fC, s + 1, lane);
        const u32 sm_ = (s * 32 + h) * 2048;
        u32x2 vm[4];
        sfor<8>([&](auto i) { k[CI(i)] = gld128(ws, vkm + CI(i) * 256, sm_); });
        sfor<4>([&](auto i) { vm[CI(i)] = __builtin_amdgcn_raw_buffer_load_b64(ws, vvm + CI(i) * 512, sm_, SC1); });
        sfor<8>([&](auto i) { m32_vv<CI(i) == 0>(sm, k[CI(i)], q[CI(i)]); });
        nop<11>(sm);
        mx = fmaxf(fmaxf(mx, sm[0]), fmaxf(fmaxf(sm[1], sm[2]), sm[3]));
        { float a = mx, b = mx; swap32(a, b); mx = fmaxf(a, b); }
        float l = 0.f;
        sfor<16>([&](auto r) { sx[CI(r)] = ex2(sx[CI(r)] - mx); l += sx[CI(r)]; });
        sfor<4>([&](auto r) { sm[CI(r)] = ex2(sm[CI(r)] - mx); l += sm[CI(r)]; });
        { float a = l, b = l; swap32(a, b); l = a + b; }
        const float inv = 1.0f / l;
        u32x4 p0 = pack8(sx, 0), p1 = pack8(sx, 8), pm = u32x4{cvtpk(sm[0], sm[1]), cvtpk(sm[2], sm[3]), 0u, 0u};
        sfor<4>([&](auto w) {
            constexpr int W = CI(w);
            f32x16 o; u32x4 am = u32x4{vm[W][0], vm[W][1], 0u, 0u};
            nop4<1>(p0, p1, pm, am);
            m32_vv<true>(o, v[2 * W], p0); m32_vv<false>(o, v[2 * W + 1], p1); m32_vv<false>(o, am, pm);
            nop<11>(o);
            gst128(pack8(o, 0, inv), ws, voo + W * 1024, s * 262144); gst128(pack8(o, 8, inv), ws, voo + W * 1024 + 512, s * 262144);
        });
        vmwait<0>(); gst32(s / 2 * 2 + par + 1, ws, fo, 0);
    }
}

DI void attention(KP& kp, int h, int cq, int wid) {
    const u32 lane = lane_id(), c = lane & 31, hi = lane >> 5;
    u32x4 c0 = {lane * 16, c - 4 * hi, (c >> 4) * 131072 + h * 4096 + hi * 256 + (c & 15) * 16, lane};
    const u32x4 sb = {(u32)wid, (u32)h, (u32)cq, WS_PROF + (blockIdx.x * 4 + wid) * 1024};
    asm volatile(
    "s_lshl_b32 s67, s36, 12\n"
    "s_add_u32 s68, s67, 0x10000\n"
    "s_lshl_b32 s64, s37, 19\n"
    "s_add_u32 s64, s64, s67\n"
    "s_add_u32 s64, s64, 0x4800000\n"
    "s_lshl_b32 s61, s38, 2\n"
    "s_sub_u32 s61, 32, s61\n"
    "v_add_u32 v3, 0x10000, v0\n"
    "s_mov_b32 s56, 0\n"
    "s_mov_b32 s62, s64\n"
    "s_add_u32 m0, s67, 0\n"
    "s_nop 0\n"
    "buffer_load_dwordx4 v0, s[40:43], s62 offen offset:0 lds\n"
    "buffer_load_dwordx4 v0, s[40:43], s62 offen offset:1024 lds\n"
    "buffer_load_dwordx4 v0, s[40:43], s62 offen offset:2048 lds\n"
    "buffer_load_dwordx4 v0, s[40:43], s62 offen offset:3072 lds\n"
    "s_sub_u32 s60, 7, s38\n"
    "s_lshl_b32 s65, s60, 3\n"
    "s_lshl_b32 s66, s36, 1\n"
    "s_add_u32 s65, s65, s66\n"
    "s_lshl_b32 s65, s65, 13\n"
    "s_lshl_b32 s66, s37, 19\n"
    "s_add_u32 s65, s65, s66\n"
    "s_add_u32 s65, s65, 0x3800000\n"
    "buffer_load_dwordx4 a[128:131], v0, s[40:43], s65 offen offset:0\n"
    "buffer_load_dwordx4 a[132:135], v0, s[40:43], s65 offen offset:1024\n"
    "buffer_load_dwordx4 a[136:139], v0, s[40:43], s65 offen offset:2048\n"
    "buffer_load_dwordx4 a[140:143], v0, s[40:43], s65 offen offset:3072\n"
    "s_add_u32 s65, s65, 0x1000\n"
    "buffer_load_dwordx4 a[144:147], v0, s[40:43], s65 offen offset:0\n"
    "buffer_load_dwordx4 a[148:151], v0, s[40:43], s65 offen offset:1024\n"
    "buffer_load_dwordx4 a[152:155], v0, s[40:43], s65 offen offset:2048\n"
    "buffer_load_dwordx4 a[156:159], v0, s[40:43], s65 offen offset:3072\n"
    "s_add_u32 s65, s65, 0x1000\n"
    "buffer_load_dwordx4 a[160:163], v0, s[40:43], s65 offen offset:0\n"
    "buffer_load_dwordx4 a[164:167], v0, s[40:43], s65 offen offset:1024\n"
    "buffer_load_dwordx4 a[168:171], v0, s[40:43], s65 offen offset:2048\n"
    "buffer_load_dwordx4 a[172:175], v0, s[40:43], s65 offen offset:3072\n"
    "s_add_u32 s65, s65, 0x1000\n"
    "buffer_load_dwordx4 a[176:179], v0, s[40:43], s65 offen offset:0\n"
    "buffer_load_dwordx4 a[180:183], v0, s[40:43], s65 offen offset:1024\n"
    "buffer_load_dwordx4 a[184:187], v0, s[40:43], s65 offen offset:2048\n"
    "buffer_load_dwordx4 a[188:191], v0, s[40:43], s65 offen offset:3072\n"
    "s_add_u32 s63, s62, 0x1000000\n"
    "s_add_u32 s56, s56, 1\n"
    "s_add_u32 s62, s62, 0x4000\n"
    "s_cmp_eq_u32 s56, s61\n"
    "s_cselect_b32 s62, s64, s62\n"
    "s_add_u32 m0, s68, 0\n"
    "s_nop 0\n"
    "buffer_load_dwordx4 v0, s[40:43], s63 offen offset:0 lds\n"
    "buffer_load_dwordx4 v0, s[40:43], s63 offen offset:1024 lds\n"
    "buffer_load_dwordx4 v0, s[40:43], s63 offen offset:2048 lds\n"
    "buffer_load_dwordx4 v0, s[40:43], s63 offen offset:3072 lds\n"
    "s_add_u32 m0, s67, 16384\n"
    "s_nop 0\n"
    "buffer_load_dwordx4 v0, s[40:43], s62 offen offset:0 lds\n"
    "buffer_load_dwordx4 v0, s[40:43], s62 offen offset:1024 lds\n"
    "buffer_load_dwordx4 v0, s[40:43], s62 offen offset:2048 lds\n"
    "buffer_load_dwordx4 v0, s[40:43], s62 offen offset:3072 lds\n"
    "s_add_u32 s63, s62, 0x1000000\n"
    "s_add_u32 s56, s56, 1\n"
    "s_add_u32 s62, s62, 0x4000\n"
    "s_cmp_eq_u32 s56, s61\n"
    "s_cselect_b32 s62, s64, s62\n"
    "s_add_u32 m0, s68, 16384\n"
    "s_nop 0\n"
    "buffer_load_dwordx4 v0, s[40:43], s63 offen offset:0 lds\n"
    "buffer_load_dwordx4 v0, s[40:43], s63 offen offset:1024 lds\n"
    "buffer_load_dwordx4 v0, s[40:43], s63 offen offset:2048 lds\n"
    "buffer_load_dwordx4 v0, s[40:43], s63 offen offset:3072 lds\n"
    "s_add_u32 m0, s67, 32768\n"
    "s_nop 0\n"
    "buffer_load_dwordx4 v0, s[40:43], s62 offen offset:0 lds\n"
    "buffer_load_dwordx4 v0, s[40:43], s62 offen offset:1024 lds\n"
    "buffer_load_dwordx4 v0, s[40:43], s62 offen offset:2048 lds\n"
    "buffer_load_dwordx4 v0, s[40:43], s62 offen offset:3072 lds\n"
    "s_mov_b32 s69, 1\n"
    "s_waitcnt vmcnt(16)\n"
    "s_barrier\n"
    ".Lat_blk_%=:\n"
    "ds_read_b128 a[192:195], v0 offset:0\n"
    "ds_read_b128 a[196:199], v0 offset:1024\n"
    "ds_read_b128 a[200:203], v0 offset:2048\n"
    "ds_read_b128 a[204:207], v0 offset:3072\n"
    "ds_read_b128 a[208:211], v0 offset:4096\n"
    "ds_read_b128 a[212:215], v0 offset:5120\n"
    "ds_read_b128 a[216:219], v0 offset:6144\n"
    "ds_read_b128 a[220:223], v0 offset:7168\n"
    "ds_read_b128 a[224:227], v0 offset:8192\n"
    "ds_read_b128 a[228:231], v0 offset:9216\n"
    "ds_read_b128 a[232:235], v0 offset:10240\n"
    "ds_read_b128 a[236:239], v0 offset:11264\n"
    "ds_read_b128 a[240:243], v0 offset:12288\n"
    "ds_read_b128 a[244:247], v0 offset:13312\n"
    "ds_read_b128 a[248:251], v0 offset:14336\n"
    "ds_read_b128 a[252:255], v0 offset:15360\n"
    "v_mov_b64 v[80:81], 0\n"
    "v_mov_b64 v[82:83], 0\n"
    "v_mov_b64 v[84:85], 0\n"
    "v_mov_b64 v[86:87], 0\n"
    "v_mov_b64 v[88:89], 0\n"
    "v_mov_b64 v[90:91], 0\n"
    "v_mov_b64 v[92:93], 0\n"
    "v_mov_b64 v[94:95], 0\n"
    "v_mov_b64 v[96:97], 0\n"
    "v_mov_b64 v[98:99], 0\n"
    "v_mov_b64 v[100:101], 0\n"
    "v_mov_b64 v[102:103], 0\n"
    "v_mov_b64 v[104:105], 0\n"
    "v_mov_b64 v[106:107], 0\n"
    "v_mov_b64 v[108:109], 0\n"
    "v_mov_b64 v[110:111], 0\n"
    "v_mov_b64 v[8:9], 0\n"
    "v_mov_b64 v[10:11], 0\n"
    "s_lshl_b32 s59, s60, 2\n"
    "s_add_u32 s58, s59, 4\n"
    "s_add_u32 s59, s59, s36\n"
    "s_mov_b32 s57, 0\n"
    "s_waitcnt lgkmcnt(8)\n"
    "v_mfma_f32_32x32x16_bf16 v[16:31], a[192:195], a[128:131], v[80:95]\n"
    "v_accvgpr_write_b32 a64, 0\n"
    "v_accvgpr_write_b32 a65, 0\n"
    "v_accvgpr_write_b32 a66, 0\n"
    "v_accvgpr_write_b32 a67, 0\n"
    "v_mfma_f32_32x32x16_bf16 v[16:31], a[196:199], a[132:135], v[16:31]\n"
    "v_accvgpr_write_b32 a68, 0\n"
    "v_accvgpr_write_b32 a69, 0\n"
    "v_accvgpr_write_b32 a70, 0\n"
    "v_accvgpr_write_b32 a71, 0\n"
    "v_mfma_f32_32x32x16_bf16 v[16:31], a[200:203], a[136:139], v[16:31]\n"
    "v_accvgpr_write_b32 a72, 0\n"
    "v_accvgpr_write_b32 a73, 0\n"
    "v_accvgpr_write_b32 a74, 0\n"
    "v_accvgpr_write_b32 a75, 0\n"
    "v_mfma_f32_32x32x16_bf16 v[16:31], a[204:207], a[140:143], v[16:31]\n"
    "v_accvgpr_write_b32 a76, 0\n"
    "v_accvgpr_write_b32 a77, 0\n"
    "v_accvgpr_write_b32 a78, 0\n"
    "v_accvgpr_write_b32 a79, 0\n"
    "v_mfma_f32_32x32x16_bf16 v[16:31], a[208:211], a[144:147], v[16:31]\n"
    "v_accvgpr_write_b32 a80, 0\n"
    "v_accvgpr_write_b32 a81, 0\n"
    "v_accvgpr_write_b32 a82, 0\n"
    "v_accvgpr_write_b32 a83, 0\n"
    "v_mfma_f32_32x32x16_bf16 v[16:31], a[212:215], a[148:151], v[16:31]\n"
    "v_accvgpr_write_b32 a84, 0\n"
    "v_accvgpr_write_b32 a85, 0\n"
    "v_accvgpr_write_b32 a86, 0\n"
    "v_accvgpr_write_b32 a87, 0\n"
    "v_mfma_f32_32x32x16_bf16 v[16:31], a[216:219], a[152:155], v[16:31]\n"
    "v_accvgpr_write_b32 a88, 0\n"
    "v_accvgpr_write_b32 a89, 0\n"
    "v_accvgpr_write_b32 a90, 0\n"
    "v_accvgpr_write_b32 a91, 0\n"
    "v_mfma_f32_32x32x16_bf16 v[16:31], a[220:223], a[156:159], v[16:31]\n"
    "v_accvgpr_write_b32 a92, 0\n"
    "v_accvgpr_write_b32 a93, 0\n"
    "v_accvgpr_write_b32 a94, 0\n"
    "v_accvgpr_write_b32 a95, 0\n"
    "s_waitcnt lgkmcnt(0)\n"
    "v_mfma_f32_32x32x16_bf16 v[32:47], a[224:227], a[128:131], v[80:95]\n"
    "v_accvgpr_write_b32 a96, 0\n"
    "v_accvgpr_write_b32 a97, 0\n"
    "v_accvgpr_write_b32 a98, 0\n"
    "v_accvgpr_write_b32 a99, 0\n"
    "v_mfma_f32_32x32x16_bf16 v[32:47], a[228:231], a[132:135], v[32:47]\n"
    "v_accvgpr_write_b32 a100, 0\n"
    "v_accvgpr_write_b32 a101, 0\n"
    "v_accvgpr_write_b32 a102, 0\n"
    "v_accvgpr_write_b32 a103, 0\n"
    "v_mfma_f32_32x32x16_bf16 v[32:47], a[232:235], a[136:139], v[32:47]\n"
    "v_accvgpr_write_b32 a104, 0\n"
    "v_accvgpr_write_b32 a105, 0\n"
    "v_accvgpr_write_b32 a106, 0\n"
    "v_accvgpr_write_b32 a107, 0\n"
    "v_mfma_f32_32x32x16_bf16 v[32:47], a[236:239], a[140:143], v[32:47]\n"
    "v_accvgpr_write_b32 a108, 0\n"
    "v_accvgpr_write_b32 a109, 0\n"
    "v_accvgpr_write_b32 a110, 0\n"
    "v_accvgpr_write_b32 a111, 0\n"
    "v_mfma_f32_32x32x16_bf16 v[32:47], a[240:243], a[144:147], v[32:47]\n"
    "v_accvgpr_write_b32 a112, 0\n"
    "v_accvgpr_write_b32 a113, 0\n"
    "v_accvgpr_write_b32 a114, 0\n"
    "v_accvgpr_write_b32 a115, 0\n"
    "s_add_u32 s63, s62, 0x1000000\n"
    "v_mfma_f32_32x32x16_bf16 v[32:47], a[244:247], a[148:151], v[32:47]\n"
    "v_accvgpr_write_b32 a116, 0\n"
    "v_accvgpr_write_b32 a117, 0\n"
    "v_accvgpr_write_b32 a118, 0\n"
    "v_accvgpr_write_b32 a119, 0\n"
    "s_add_u32 s56, s56, 1\n"
    "v_mfma_f32_32x32x16_bf16 v[32:47], a[248:251], a[152:155], v[32:47]\n"
    "v_accvgpr_write_b32 a120, 0\n"
    "v_accvgpr_write_b32 a121, 0\n"
    "v_accvgpr_write_b32 a122, 0\n"
    "v_accvgpr_write_b32 a123, 0\n"
    "s_add_u32 s62, s62, 0x4000\n"
    "v_mfma_f32_32x32x16_bf16 v[32:47], a[252:255], a[156:159], v[32:47]\n"
    "v_accvgpr_write_b32 a124, 0\n"
    "v_accvgpr_write_b32 a125, 0\n"
    "v_accvgpr_write_b32 a126, 0\n"
    "v_accvgpr_write_b32 a127, 0\n"
    "s_cmp_eq_u32 s56, s61\n"
    "s_cselect_b32 s62, s64, s62\n"
    "s_nop 11\n"
    "s_cmp_eq_u32 s57, s59\n"
    "s_cbranch_scc1 .Lat_dg1_%=\n"
    ".Lat_dgr1_%=:\n"
    "v_max3_f32 v12, v16, v17, v18\n"
    "v_max3_f32 v13, v19, v20, v21\n"
    "v_max3_f32 v12, v12, v22, v23\n"
    "v_max3_f32 v13, v13, v24, v25\n"
    "v_max3_f32 v12, v12, v26, v27\n"
    "v_max3_f32 v13, v13, v28, v29\n"
    "v_max3_f32 v12, v12, v30, v31\n"
    "v_max3_f32 v13, v13, v32, v33\n"
    "v_max3_f32 v12, v12, v34, v35\n"
    "v_max3_f32 v13, v13, v36, v37\n"
    "v_max3_f32 v12, v12, v38, v39\n"
    "v_max3_f32 v13, v13, v40, v41\n"
    "v_max3_f32 v12, v12, v42, v43\n"
    "v_max3_f32 v13, v13, v44, v45\n"
    "v_max3_f32 v12, v12, v46, v47\n"
    "v_max_f32 v12, v12, v13\n"
    "v_mov_b32 v13, v12\n"
    "s_nop 1\n"
    "v_permlane32_swap_b32 v12, v13\n"
    "v_max_f32 v12, v12, v13\n"
    "v_sub_f32 v16, v16, v12\n"
    "v_sub_f32 v32, v32, v12\n"
    "v_sub_f32 v80, v80, v12\n"
    "v_sub_f32 v17, v17, v12\n"
    "v_sub_f32 v33, v33, v12\n"
    "v_sub_f32 v81, v81, v12\n"
    "v_sub_f32 v18, v18, v12\n"
    "v_sub_f32 v34, v34, v12\n"
    "v_sub_f32 v82, v82, v12\n"
    "v_sub_f32 v19, v19, v12\n"
    "v_sub_f32 v35, v35, v12\n"
    "v_sub_f32 v83, v83, v12\n"
    "v_sub_f32 v20, v20, v12\n"
    "v_sub_f32 v36, v36, v12\n"
    "v_sub_f32 v84, v84, v12\n"
    "v_sub_f32 v21, v21, v12\n"
    "v_sub_f32 v37, v37, v12\n"
    "v_sub_f32 v85, v85, v12\n"
    "v_sub_f32 v22, v22, v12\n"
    "v_sub_f32 v38, v38, v12\n"
    "v_sub_f32 v86, v86, v12\n"
    "v_sub_f32 v23, v23, v12\n"
    "v_sub_f32 v39, v39, v12\n"
    "v_sub_f32 v87, v87, v12\n"
    "v_sub_f32 v24, v24, v12\n"
    "v_sub_f32 v40, v40, v12\n"
    "v_sub_f32 v88, v88, v12\n"
    "v_sub_f32 v25, v25, v12\n"
    "v_sub_f32 v41, v41, v12\n"
    "v_sub_f32 v89, v89, v12\n"
    "v_sub_f32 v26, v26, v12\n"
    "v_sub_f32 v42, v42, v12\n"
    "v_sub_f32 v90, v90, v12\n"
    "v_sub_f32 v27, v27, v12\n"
    "v_sub_f32 v43, v43, v12\n"
    "v_sub_f32 v91, v91, v12\n"
    "v_sub_f32 v28, v28, v12\n"
    "v_sub_f32 v44, v44, v12\n"
    "v_sub_f32 v92, v92, v12\n"
    "v_sub_f32 v29, v29, v12\n"
    "v_sub_f32 v45, v45, v12\n"
    "v_sub_f32 v93, v93, v12\n"
    "v_sub_f32 v30, v30, v12\n"
    "v_sub_f32 v46, v46, v12\n"
    "v_sub_f32 v94, v94, v12\n"
    "v_sub_f32 v31, v31, v12\n"
    "v_sub_f32 v47, v47, v12\n"
    "v_sub_f32 v95, v95, v12\n"
    "v_exp_f32 v16, v16\n"
    "v_exp_f32 v17, v17\n"
    "v_exp_f32 v18, v18\n"
    "v_exp_f32 v19, v19\n"
    "v_add_f32 v8, v8, v16\n"
    "v_add_f32 v9, v9, v17\n"
    "v_cvt_pk_bf16_f32 v112, v16, v17\n"
    "v_exp_f32 v20, v20\n"
    "v_exp_f32 v21, v21\n"
    "v_add_f32 v8, v8, v18\n"
    "v_add_f32 v9, v9, v19\n"
    "v_cvt_pk_bf16_f32 v113, v18, v19\n"
    "v_exp_f32 v22, v22\n"
    "v_exp_f32 v23, v23\n"
    "v_add_f32 v8, v8, v20\n"
    "v_add_f32 v9, v9, v21\n"
    "v_cvt_pk_bf16_f32 v114, v20, v21\n"
    "v_exp_f32 v24, v24\n"
    "v_exp_f32 v25, v25\n"
    "v_add_f32 v8, v8, v22\n"
    "v_add_f32 v9, v9, v23\n"
    "v_cvt_pk_bf16_f32 v115, v22, v23\n"
    "v_exp_f32 v26, v26\n"
    "v_exp_f32 v27, v27\n"
    "v_add_f32 v8, v8, v24\n"
    "v_add_f32 v9, v9, v25\n"
    "v_cvt_pk_bf16_f32 v116, v24, v25\n"
    "v_exp_f32 v28, v28\n"
    "v_exp_f32 v29, v29\n"
    "v_add_f32 v8, v8, v26\n"
    "v_add_f32 v9, v9, v27\n"
    "v_cvt_pk_bf16_f32 v117, v26, v27\n"
    "v_exp_f32 v30, v30\n"
    "v_exp_f32 v31, v31\n"
    "v_add_f32 v8, v8, v28\n"
    "v_add_f32 v9, v9, v29\n"
    "v_cvt_pk_bf16_f32 v118, v28, v29\n"
    "v_exp_f32 v32, v32\n"
    "v_exp_f32 v33, v33\n"
    "v_add_f32 v8, v8, v30\n"
    "v_add_f32 v9, v9, v31\n"
    "v_cvt_pk_bf16_f32 v119, v30, v31\n"
    "s_waitcnt vmcnt(8)\n"
    "s_barrier\n"
    "v_mfma_f32_32x32x16_bf16 v[48:63], a[192:195], a[160:163], v[96:111]\n"
    "ds_read_b128 v[176:179], v3 offset:0\n"
    "v_exp_f32 v34, v34\n"
    "v_exp_f32 v35, v35\n"
    "v_add_f32 v8, v8, v32\n"
    "v_mfma_f32_32x32x16_bf16 v[48:63], a[196:199], a[164:167], v[48:63]\n"
    "ds_read_b128 v[180:183], v3 offset:2048\n"
    "s_add_u32 m0, s67, 49152\n"
    "s_nop 0\n"
    "buffer_load_dwordx4 v0, s[40:43], s62 offen offset:0 lds\n"
    "v_add_f32 v9, v9, v33\n"
    "v_cvt_pk_bf16_f32 v120, v32, v33\n"
    "v_mfma_f32_32x32x16_bf16 v[48:63], a[200:203], a[168:171], v[48:63]\n"
    "ds_read_b128 v[184:187], v3 offset:4096\n"
    "v_exp_f32 v36, v36\n"
    "v_exp_f32 v37, v37\n"
    "v_add_f32 v8, v8, v34\n"
    "v_mfma_f32_32x32x16_bf16 v[48:63], a[204:207], a[172:175], v[48:63]\n"
    "ds_read_b128 v[188:191], v3 offset:6144\n"
    "buffer_load_dwordx4 v0, s[40:43], s62 offen offset:1024 lds\n"
    "v_add_f32 v9, v9, v35\n"
    "v_cvt_pk_bf16_f32 v121, v34, v35\n"
    "v_exp_f32 v38, v38\n"
    "v_mfma_f32_32x32x16_bf16 v[48:63], a[208:211], a[176:179], v[48:63]\n"
    "ds_read_b128 v[192:195], v3 offset:1024\n"
    "v_exp_f32 v39, v39\n"
    "v_add_f32 v8, v8, v36\n"
    "v_add_f32 v9, v9, v37\n"
    "v_cvt_pk_bf16_f32 v122, v36, v37\n"
    "v_mfma_f32_32x32x16_bf16 v[48:63], a[212:215], a[180:183], v[48:63]\n"
    "ds_read_b128 v[196:199], v3 offset:3072\n"
    "buffer_load_dwordx4 v0, s[40:43], s62 offen offset:2048 lds\n"
    "v_exp_f32 v40, v40\n"
    "v_exp_f32 v41, v41\n"
    "v_mfma_f32_32x32x16_bf16 v[48:63], a[216:219], a[184:187], v[48:63]\n"
    "ds_read_b128 v[200:203], v3 offset:5120\n"
    "v_add_f32 v8, v8, v38\n"
    "v_add_f32 v9, v9, v39\n"
    "v_cvt_pk_bf16_f32 v123, v38, v39\n"
    "v_exp_f32 v42, v42\n"
    "v_mfma_f32_32x32x16_bf16 v[48:63], a[220:223], a[188:191], v[48:63]\n"
    "ds_read_b128 v[204:207], v3 offset:7168\n"
    "buffer_load_dwordx4 v0, s[40:43], s62 offen offset:3072 lds\n"
    "v_exp_f32 v43, v43\n"
    "v_add_f32 v8, v8, v40\n"
    "v_add_f32 v9, v9, v41\n"
    "v_mfma_f32_32x32x16_bf16 v[64:79], a[224:227], a[160:163], v[96:111]\n"
    "ds_read_b128 v[208:211], v3 offset:8192\n"
    "v_cvt_pk_bf16_f32 v124, v40, v41\n"
    "v_exp_f32 v44, v44\n"
    "v_exp_f32 v45, v45\n"
    "v_mfma_f32_32x32x16_bf16 v[64:79], a[228:231], a[164:167], v[64:79]\n"
    "ds_read_b128 v[212:215], v3 offset:10240\n"
    "s_add_u32 m0, s68, 32768\n"
    "s_nop 0\n"
    "buffer_load_dwordx4 v0, s[40:43], s63 offen offset:0 lds\n"
    "v_add_f32 v8, v8, v42\n"
    "v_add_f32 v9, v9, v43\n"
    "v_mfma_f32_32x32x16_bf16 v[64:79], a[232:235], a[168:171], v[64:79]\n"
    "ds_read_b128 v[216:219], v3 offset:12288\n"
    "v_cvt_pk_bf16_f32 v125, v42, v43\n"
    "v_exp_f32 v46, v46\n"
    "v_exp_f32 v47, v47\n"
    "v_mfma_f32_32x32x16_bf16 v[64:79], a[236:239], a[172:175], v[64:79]\n"
    "ds_read_b128 v[220:223], v3 offset:14336\n"
    "buffer_load_dwordx4 v0, s[40:43], s63 offen offset:1024 lds\n"
    "v_add_f32 v8, v8, v44\n"
    "v_add_f32 v9, v9, v45\n"
    "v_cvt_pk_bf16_f32 v126, v44, v45\n"
    "v_add_f32 v8, v8, v46\n"
    "v_mfma_f32_32x32x16_bf16 v[64:79], a[240:243], a[176:179], v[64:79]\n"
    "ds_read_b128 v[224:227], v3 offset:9216\n"
    "v_add_f32 v9, v9, v47\n"
    "v_cvt_pk_bf16_f32 v127, v46, v47\n"
    "v_mfma_f32_32x32x16_bf16 v[64:79], a[244:247], a[180:183], v[64:79]\n"
    "ds_read_b128 v[228:231], v3 offset:11264\n"
    "buffer_load_dwordx4 v0, s[40:43], s63 offen offset:2048 lds\n"
    "v_mfma_f32_32x32x16_bf16 v[64:79], a[248:251], a[184:187], v[64:79]\n"
    "ds_read_b128 v[232:235], v3 offset:13312\n"
    "v_mfma_f32_32x32x16_bf16 v[64:79], a[252:255], a[188:191], v[64:79]\n"
    "ds_read_b128 v[236:239], v3 offset:15360\n"
    "buffer_load_dwordx4 v0, s[40:43], s63 offen offset:3072 lds\n"
    "s_waitcnt lgkmcnt(8)\n"
    "v_mfma_f32_32x32x16_bf16 a[0:15], v[176:179], v[112:115], 0\n"
    "ds_read_b128 a[192:195], v0 offset:16384\n"
    "s_cmp_eq_u32 s57, s59\n"
    "s_cbranch_scc1 .Lat_dg2_%=\n"
    ".Lat_dgr2_%=:\n"
    "v_max3_f32 v14, v48, v49, v50\n"
    "v_max3_f32 v15, v51, v52, v53\n"
    "v_max3_f32 v14, v14, v54, v55\n"
    "v_mfma_f32_32x32x16_bf16 a[16:31], v[180:183], v[112:115], 0\n"
    "ds_read_b128 a[196:199], v0 offset:17408\n"
    "v_max3_f32 v15, v15, v56, v57\n"
    "v_max3_f32 v14, v14, v58, v59\n"
    "v_max3_f32 v15, v15, v60, v61\n"
    "v_max3_f32 v14, v14, v62, v63\n"
    "v_max3_f32 v15, v15, v64, v65\n"
    "v_mfma_f32_32x32x16_bf16 a[32:47], v[184:187], v[112:115], 0\n"
    "ds_read_b128 a[200:203], v0 offset:18432\n"
    "v_max3_f32 v14, v14, v66, v67\n"
    "v_max3_f32 v15, v15, v68, v69\n"
    "v_max3_f32 v14, v14, v70, v71\n"
    "v_max3_f32 v15, v15, v72, v73\n"
    "v_max3_f32 v14, v14, v74, v75\n"
    "v_mfma_f32_32x32x16_bf16 a[48:63], v[188:191], v[112:115], 0\n"
    "ds_read_b128 a[204:207], v0 offset:19456\n"
    "v_max3_f32 v15, v15, v76, v77\n"
    "v_max3_f32 v14, v14, v78, v79\n"
    "v_max_f32 v14, v14, v15\n"
    "v_mov_b32 v15, v14\n"
    "s_nop 1\n"
    "v_mfma_f32_32x32x16_bf16 a[0:15], v[192:195], v[116:119], a[0:15]\n"
    "ds_read_b128 a[208:211], v0 offset:20480\n"
    "v_permlane32_swap_b32 v14, v15\n"
    "v_max_f32 v14, v14, v15\n"
    "v_sub_f32 v48, v48, v14\n"
    "v_sub_f32 v64, v64, v14\n"
    "v_sub_f32 v96, v96, v14\n"
    "v_sub_f32 v49, v49, v14\n"
    "v_sub_f32 v65, v65, v14\n"
    "v_sub_f32 v97, v97, v14\n"
    "v_sub_f32 v50, v50, v14\n"
    "v_sub_f32 v66, v66, v14\n"
    "v_sub_f32 v98, v98, v14\n"
    "v_sub_f32 v51, v51, v14\n"
    "v_sub_f32 v67, v67, v14\n"
    "v_sub_f32 v99, v99, v14\n"
    "v_sub_f32 v52, v52, v14\n"
    "v_sub_f32 v68, v68, v14\n"
    "v_sub_f32 v100, v100, v14\n"
    "v_sub_f32 v53, v53, v14\n"
    "v_sub_f32 v69, v69, v14\n"
    "v_sub_f32 v101, v101, v14\n"
    "v_sub_f32 v54, v54, v14\n"
    "v_sub_f32 v70, v70, v14\n"
    "v_sub_f32 v102, v102, v14\n"
    "v_sub_f32 v55, v55, v14\n"
    "v_sub_f32 v71, v71, v14\n"
    "v_sub_f32 v103, v103, v14\n"
    "v_sub_f32 v56, v56, v14\n"
    "v_sub_f32 v72, v72, v14\n"
    "v_sub_f32 v104, v104, v14\n"
    "v_sub_f32 v57, v57, v14\n"
    "v_sub_f32 v73, v73, v14\n"
    "v_sub_f32 v105, v105, v14\n"
    "v_sub_f32 v58, v58, v14\n"
    "v_sub_f32 v74, v74, v14\n"
    "v_sub_f32 v106, v106, v14\n"
    "v_sub_f32 v59, v59, v14\n"
    "v_sub_f32 v75, v75, v14\n"
    "v_sub_f32 v107, v107, v14\n"
    "v_sub_f32 v60, v60, v14\n"
    "v_sub_f32 v76, v76, v14\n"
    "v_sub_f32 v108, v108, v14\n"
    "v_sub_f32 v61, v61, v14\n"
    "v_sub_f32 v77, v77, v14\n"
    "v_sub_f32 v109, v109, v14\n"
    "v_sub_f32 v62, v62, v14\n"
    "v_sub_f32 v78, v78, v14\n"
    "v_sub_f32 v110, v110, v14\n"
    "v_sub_f32 v63, v63, v14\n"
    "v_sub_f32 v79, v79, v14\n"
    "v_sub_f32 v111, v111, v14\n"
    "v_mfma_f32_32x32x16_bf16 a[16:31], v[196:199], v[116:119], a[16:31]\n"
    "ds_read_b128 a[212:215], v0 offset:21504\n"
    "v_exp_f32 v48, v48\n"
    "v_exp_f32 v49, v49\n"
    "v_mfma_f32_32x32x16_bf16 a[32:47], v[200:203], v[116:119], a[32:47]\n"
    "ds_read_b128 a[216:219], v0 offset:22528\n"
    "v_exp_f32 v50, v50\n"
    "v_exp_f32 v51, v51\n"
    "v_add_f32 v10, v10, v48\n"
    "v_mfma_f32_32x32x16_bf16 a[48:63], v[204:207], v[116:119], a[48:63]\n"
    "ds_read_b128 a[220:223], v0 offset:23552\n"
    "v_add_f32 v11, v11, v49\n"
    "v_cvt_pk_bf16_f32 v128, v48, v49\n"
    "v_exp_f32 v52, v52\n"
    "s_waitcnt lgkmcnt(8)\n"
    "v_mfma_f32_32x32x16_bf16 a[0:15], v[208:211], v[120:123], a[0:15]\n"
    "ds_read_b128 a[224:227], v0 offset:24576\n"
    "v_exp_f32 v53, v53\n"
    "v_add_f32 v10, v10, v50\n"
    "v_add_f32 v11, v11, v51\n"
    "v_cvt_pk_bf16_f32 v129, v50, v51\n"
    "v_mfma_f32_32x32x16_bf16 a[16:31], v[212:215], v[120:123], a[16:31]\n"
    "ds_read_b128 a[228:231], v0 offset:25600\n"
    "v_exp_f32 v54, v54\n"
    "v_exp_f32 v55, v55\n"
    "v_add_f32 v10, v10, v52\n"
    "v_mfma_f32_32x32x16_bf16 a[32:47], v[216:219], v[120:123], a[32:47]\n"
    "ds_read_b128 a[232:235], v0 offset:26624\n"
    "v_add_f32 v11, v11, v53\n"
    "v_cvt_pk_bf16_f32 v130, v52, v53\n"
    "v_exp_f32 v56, v56\n"
    "v_mfma_f32_32x32x16_bf16 a[48:63], v[220:223], v[120:123], a[48:63]\n"
    "ds_read_b128 a[236:239], v0 offset:27648\n"
    "v_exp_f32 v57, v57\n"
    "v_add_f32 v10, v10, v54\n"
    "v_add_f32 v11, v11, v55\n"
    "v_cvt_pk_bf16_f32 v131, v54, v55\n"
    "v_mfma_f32_32x32x16_bf16 a[0:15], v[224:227], v[124:127], a[0:15]\n"
    "ds_read_b128 a[240:243], v0 offset:28672\n"
    "v_exp_f32 v58, v58\n"
    "v_exp_f32 v59, v59\n"
    "v_add_f32 v10, v10, v56\n"
    "v_mfma_f32_32x32x16_bf16 a[16:31], v[228:231], v[124:127], a[16:31]\n"
    "ds_read_b128 a[244:247], v0 offset:29696\n"
    "v_add_f32 v11, v11, v57\n"
    "v_cvt_pk_bf16_f32 v132, v56, v57\n"
    "v_exp_f32 v60, v60\n"
    "v_mfma_f32_32x32x16_bf16 a[32:47], v[232:235], v[124:127], a[32:47]\n"
    "ds_read_b128 a[248:251], v0 offset:30720\n"
    "v_exp_f32 v61, v61\n"
    "v_add_f32 v10, v10, v58\n"
    "v_add_f32 v11, v11, v59\n"
    "v_cvt_pk_bf16_f32 v133, v58, v59\n"
    "v_mfma_f32_32x32x16_bf16 a[48:63], v[236:239], v[124:127], a[48:63]\n"
    "ds_read_b128 a[252:255], v0 offset:31744\n"
    "v_exp_f32 v62, v62\n"
    "v_exp_f32 v63, v63\n"
    "v_add_f32 v10, v10, v60\n"
    ".Lat_loop_%=:\n"
    "s_add_u32 s57, s57, 1\n"
    "s_cmp_gt_u32 s57, s59\n"
    "s_cbranch_scc1 .Lat_drain_%=\n"
    "s_waitcnt lgkmcnt(8)\n"
    "v_mfma_f32_32x32x16_bf16 v[16:31], a[192:195], a[128:131], v[80:95]\n"
    "v_add_f32 v11, v11, v61\n"
    "v_cvt_pk_bf16_f32 v134, v60, v61\n"
    "v_exp_f32 v64, v64\n"
    "v_exp_f32 v65, v65\n"
    "v_mfma_f32_32x32x16_bf16 v[16:31], a[196:199], a[132:135], v[16:31]\n"
    "v_add_f32 v10, v10, v62\n"
    "v_add_f32 v11, v11, v63\n"
    "v_cvt_pk_bf16_f32 v135, v62, v63\n"
    "v_exp_f32 v66, v66\n"
    "v_mfma_f32_32x32x16_bf16 v[16:31], a[200:203], a[136:139], v[16:31]\n"
    "v_exp_f32 v67, v67\n"
    "v_add_f32 v10, v10, v64\n"
    "v_add_f32 v11, v11, v65\n"
    "v_cvt_pk_bf16_f32 v136, v64, v65\n"
    "v_mfma_f32_32x32x16_bf16 v[16:31], a[204:207], a[140:143], v[16:31]\n"
    "v_exp_f32 v68, v68\n"
    "v_exp_f32 v69, v69\n"
    "v_add_f32 v10, v10, v66\n"
    "v_add_f32 v11, v11, v67\n"
    "v_mfma_f32_32x32x16_bf16 v[16:31], a[208:211], a[144:147], v[16:31]\n"
    "v_cvt_pk_bf16_f32 v137, v66, v67\n"
    "v_exp_f32 v70, v70\n"
    "v_exp_f32 v71, v71\n"
    "v_add_f32 v10, v10, v68\n"
    "v_mfma_f32_32x32x16_bf16 v[16:31], a[212:215], a[148:151], v[16:31]\n"
    "v_add_f32 v11, v11, v69\n"
    "v_cvt_pk_bf16_f32 v138, v68, v69\n"
    "v_exp_f32 v72, v72\n"
    "v_exp_f32 v73, v73\n"
    "v_mfma_f32_32x32x16_bf16 v[16:31], a[216:219], a[152:155], v[16:31]\n"
    "v_add_f32 v10, v10, v70\n"
    "v_add_f32 v11, v11, v71\n"
    "v_cvt_pk_bf16_f32 v139, v70, v71\n"
    "v_exp_f32 v74, v74\n"
    "v_mfma_f32_32x32x16_bf16 v[16:31], a[220:223], a[156:159], v[16:31]\n"
    "v_exp_f32 v75, v75\n"
    "v_add_f32 v10, v10, v72\n"
    "v_add_f32 v11, v11, v73\n"
    "v_cvt_pk_bf16_f32 v140, v72, v73\n"
    "s_waitcnt lgkmcnt(0)\n"
    "v_mfma_f32_32x32x16_bf16 v[32:47], a[224:227], a[128:131], v[80:95]\n"
    "v_exp_f32 v76, v76\n"
    "v_exp_f32 v77, v77\n"
    "v_add_f32 v10, v10, v74\n"
    "v_add_f32 v11, v11, v75\n"
    "v_mfma_f32_32x32x16_bf16 v[32:47], a[228:231], a[132:135], v[32:47]\n"
    "v_cvt_pk_bf16_f32 v141, v74, v75\n"
    "v_exp_f32 v78, v78\n"
    "v_exp_f32 v79, v79\n"
    "v_add_f32 v10, v10, v76\n"
    "v_mfma_f32_32x32x16_bf16 v[32:47], a[232:235], a[136:139], v[32:47]\n"
    "v_add_f32 v11, v11, v77\n"
    "v_cvt_pk_bf16_f32 v142, v76, v77\n"
    "v_add_f32 v10, v10, v78\n"
    "v_add_f32 v11, v11, v79\n"
    "v_cvt_pk_bf16_f32 v143, v78, v79\n"
    "v_mfma_f32_32x32x16_bf16 v[32:47], a[236:239], a[140:143], v[32:47]\n"
    "v_mfma_f32_32x32x16_bf16 v[32:47], a[240:243], a[144:147], v[32:47]\n"
    "s_add_u32 s63, s62, 0x1000000\n"
    "v_mfma_f32_32x32x16_bf16 v[32:47], a[244:247], a[148:151], v[32:47]\n"
    "s_add_u32 s56, s56, 1\n"
    "v_mfma_f32_32x32x16_bf16 v[32:47], a[248:251], a[152:155], v[32:47]\n"
    "s_add_u32 s62, s62, 0x4000\n"
    "v_mfma_f32_32x32x16_bf16 v[32:47], a[252:255], a[156:159], v[32:47]\n"
    "s_cmp_eq_u32 s56, s61\n"
    "s_cselect_b32 s62, s64, s62\n"
    "v_mfma_f32_32x32x16_bf16 a[64:79], v[176:179], v[128:131], a[64:79]\n"
    "s_cmp_eq_u32 s57, s59\n"
    "s_cbranch_scc1 .Lat_dg3_%=\n"
    ".Lat_dgr3_%=:\n"
    "v_max3_f32 v12, v16, v17, v18\n"
    "v_max3_f32 v13, v19, v20, v21\n"
    "v_max3_f32 v12, v12, v22, v23\n"
    "v_mfma_f32_32x32x16_bf16 a[80:95], v[180:183], v[128:131], a[80:95]\n"
    "v_max3_f32 v13, v13, v24, v25\n"
    "v_max3_f32 v12, v12, v26, v27\n"
    "v_max3_f32 v13, v13, v28, v29\n"
    "v_max3_f32 v12, v12, v30, v31\n"
    "v_max3_f32 v13, v13, v32, v33\n"
    "v_mfma_f32_32x32x16_bf16 a[96:111], v[184:187], v[128:131], a[96:111]\n"
    "v_max3_f32 v12, v12, v34, v35\n"
    "v_max3_f32 v13, v13, v36, v37\n"
    "v_max3_f32 v12, v12, v38, v39\n"
    "v_max3_f32 v13, v13, v40, v41\n"
    "v_max3_f32 v12, v12, v42, v43\n"
    "v_mfma_f32_32x32x16_bf16 a[112:127], v[188:191], v[128:131], a[112:127]\n"
    "v_max3_f32 v13, v13, v44, v45\n"
    "v_max3_f32 v12, v12, v46, v47\n"
    "v_max_f32 v12, v12, v13\n"
    "v_mov_b32 v13, v12\n"
    "s_nop 0\n"
    "v_mfma_f32_32x32x16_bf16 a[64:79], v[192:195], v[132:135], a[64:79]\n"
    "v_permlane32_swap_b32 v12, v13\n"
    "v_max_f32 v12, v12, v13\n"
    "v_cmp_lt_f32 vcc, 0x41f00000, v12\n"
    "v_mfma_f32_32x32x16_bf16 a[80:95], v[196:199], v[132:135], a[80:95]\n"
    "s_cbranch_vccnz .Lat_rs3_%=\n"
    ".Lat_rsr3_%=:\n"
    "v_exp_f32 v16, v16\n"
    "v_exp_f32 v17, v17\n"
    "v_mfma_f32_32x32x16_bf16 a[96:111], v[200:203], v[132:135], a[96:111]\n"
    "v_exp_f32 v18, v18\n"
    "v_exp_f32 v19, v19\n"
    "v_add_f32 v8, v8, v16\n"
    "v_add_f32 v9, v9, v17\n"
    "v_mfma_f32_32x32x16_bf16 a[112:127], v[204:207], v[132:135], a[112:127]\n"
    "v_cvt_pk_bf16_f32 v112, v16, v17\n"
    "v_exp_f32 v20, v20\n"
    "v_exp_f32 v21, v21\n"
    "v_add_f32 v8, v8, v18\n"
    "v_mfma_f32_32x32x16_bf16 a[64:79], v[208:211], v[136:139], a[64:79]\n"
    "v_add_f32 v9, v9, v19\n"
    "v_cvt_pk_bf16_f32 v113, v18, v19\n"
    "v_exp_f32 v22, v22\n"
    "v_exp_f32 v23, v23\n"
    "v_mfma_f32_32x32x16_bf16 a[80:95], v[212:215], v[136:139], a[80:95]\n"
    "v_add_f32 v8, v8, v20\n"
    "v_add_f32 v9, v9, v21\n"
    "v_cvt_pk_bf16_f32 v114, v20, v21\n"
    "v_exp_f32 v24, v24\n"
    "v_mfma_f32_32x32x16_bf16 a[96:111], v[216:219], v[136:139], a[96:111]\n"
    "v_exp_f32 v25, v25\n"
    "v_add_f32 v8, v8, v22\n"
    "v_add_f32 v9, v9, v23\n"
    "v_cvt_pk_bf16_f32 v115, v22, v23\n"
    "v_mfma_f32_32x32x16_bf16 a[112:127], v[220:223], v[136:139], a[112:127]\n"
    "v_exp_f32 v26, v26\n"
    "v_exp_f32 v27, v27\n"
    "v_add_f32 v8, v8, v24\n"
    "v_add_f32 v9, v9, v25\n"
    "v_mfma_f32_32x32x16_bf16 a[64:79], v[224:227], v[140:143], a[64:79]\n"
    "v_cvt_pk_bf16_f32 v116, v24, v25\n"
    "v_exp_f32 v28, v28\n"
    "v_exp_f32 v29, v29\n"
    "v_add_f32 v8, v8, v26\n"
    "v_mfma_f32_32x32x16_bf16 a[80:95], v[228:231], v[140:143], a[80:95]\n"
    "v_add_f32 v9, v9, v27\n"
    "v_cvt_pk_bf16_f32 v117, v26, v27\n"
    "v_exp_f32 v30, v30\n"
    "v_exp_f32 v31, v31\n"
    "v_mfma_f32_32x32x16_bf16 a[96:111], v[232:235], v[140:143], a[96:111]\n"
    "v_add_f32 v8, v8, v28\n"
    "v_add_f32 v9, v9, v29\n"
    "v_cvt_pk_bf16_f32 v118, v28, v29\n"
    "v_exp_f32 v32, v32\n"
    "v_mfma_f32_32x32x16_bf16 a[112:127], v[236:239], v[140:143], a[112:127]\n"
    "v_exp_f32 v33, v33\n"
    "v_add_f32 v8, v8, v30\n"
    "v_add_f32 v9, v9, v31\n"
    "v_cvt_pk_bf16_f32 v119, v30, v31\n"
    "s_waitcnt vmcnt(8)\n"
    "s_barrier\n"
    "v_mfma_f32_32x32x16_bf16 v[48:63], a[192:195], a[160:163], v[96:111]\n"
    "ds_read_b128 v[176:179], v3 offset:16384\n"
    "v_exp_f32 v34, v34\n"
    "v_exp_f32 v35, v35\n"
    "v_add_f32 v8, v8, v32\n"
    "v_mfma_f32_32x32x16_bf16 v[48:63], a[196:199], a[164:167], v[48:63]\n"
    "ds_read_b128 v[180:183], v3 offset:18432\n"
    "s_add_u32 m0, s67, 0\n"
    "s_nop 0\n"
    "buffer_load_dwordx4 v0, s[40:43], s62 offen offset:0 lds\n"
    "v_add_f32 v9, v9, v33\n"
    "v_cvt_pk_bf16_f32 v120, v32, v33\n"
    "v_mfma_f32_32x32x16_bf16 v[48:63], a[200:203], a[168:171], v[48:63]\n"
    "ds_read_b128 v[184:187], v3 offset:20480\n"
    "v_exp_f32 v36, v36\n"
    "v_exp_f32 v37, v37\n"
    "v_add_f32 v8, v8, v34\n"
    "v_mfma_f32_32x32x16_bf16 v[48:63], a[204:207], a[172:175], v[48:63]\n"
    "ds_read_b128 v[188:191], v3 offset:22528\n"
    "buffer_load_dwordx4 v0, s[40:43], s62 offen offset:1024 lds\n"
    "v_add_f32 v9, v9, v35\n"
    "v_cvt_pk_bf16_f32 v121, v34, v35\n"
    "v_exp_f32 v38, v38\n"
    "v_mfma_f32_32x32x16_bf16 v[48:63], a[208:211], a[176:179], v[48:63]\n"
    "ds_read_b128 v[192:195], v3 offset:17408\n"
    "v_exp_f32 v39, v39\n"
    "v_add_f32 v8, v8, v36\n"
    "v_add_f32 v9, v9, v37\n"
    "v_cvt_pk_bf16_f32 v122, v36, v37\n"
    "v_mfma_f32_32x32x16_bf16 v[48:63], a[212:215], a[180:183], v[48:63]\n"
    "ds_read_b128 v[196:199], v3 offset:19456\n"
    "buffer_load_dwordx4 v0, s[40:43], s62 offen offset:2048 lds\n"
    "v_exp_f32 v40, v40\n"
    "v_exp_f32 v41, v41\n"
    "v_mfma_f32_32x32x16_bf16 v[48:63], a[216:219], a[184:187], v[48:63]\n"
    "ds_read_b128 v[200:203], v3 offset:21504\n"
    "v_add_f32 v8, v8, v38\n"
    "v_add_f32 v9, v9, v39\n"
    "v_cvt_pk_bf16_f32 v123, v38, v39\n"
    "v_exp_f32 v42, v42\n"
    "v_mfma_f32_32x32x16_bf16 v[48:63], a[220:223], a[188:191], v[48:63]\n"
    "ds_read_b128 v[204:207], v3 offset:23552\n"
    "buffer_load_dwordx4 v0, s[40:43], s62 offen offset:3072 lds\n"
    "v_exp_f32 v43, v43\n"
    "v_add_f32 v8, v8, v40\n"
    "v_add_f32 v9, v9, v41\n"
    "v_mfma_f32_32x32x16_bf16 v[64:79], a[224:227], a[160:163], v[96:111]\n"
    "ds_read_b128 v[208:211], v3 offset:24576\n"
    "v_cvt_pk_bf16_f32 v124, v40, v41\n"
    "v_exp_f32 v44, v44\n"
    "v_exp_f32 v45, v45\n"
    "v_mfma_f32_32x32x16_bf16 v[64:79], a[228:231], a[164:167], v[64:79]\n"
    "ds_read_b128 v[212:215], v3 offset:26624\n"
    "s_add_u32 m0, s68, 49152\n"
    "s_nop 0\n"
    "buffer_load_dwordx4 v0, s[40:43], s63 offen offset:0 lds\n"
    "v_add_f32 v8, v8, v42\n"
    "v_add_f32 v9, v9, v43\n"
    "v_mfma_f32_32x32x16_bf16 v[64:79], a[232:235], a[168:171], v[64:79]\n"
    "ds_read_b128 v[216:219], v3 offset:28672\n"
    "v_cvt_pk_bf16_f32 v125, v42, v43\n"
    "v_exp_f32 v46, v46\n"
    "v_exp_f32 v47, v47\n"
    "v_mfma_f32_32x32x16_bf16 v[64:79], a[236:239], a[172:175], v[64:79]\n"
    "ds_read_b128 v[220:223], v3 offset:30720\n"
    "buffer_load_dwordx4 v0, s[40:43], s63 offen offset:1024 lds\n"
    "v_add_f32 v8, v8, v44\n"
    "v_add_f32 v9, v9, v45\n"
    "v_cvt_pk_bf16_f32 v126, v44, v45\n"
    "v_add_f32 v8, v8, v46\n"
    "v_mfma_f32_32x32x16_bf16 v[64:79], a[240:243], a[176:179], v[64:79]\n"
    "ds_read_b128 v[224:227], v3 offset:25600\n"
    "v_add_f32 v9, v9, v47\n"
    "v_cvt_pk_bf16_f32 v127, v46, v47\n"
    "v_mfma_f32_32x32x16_bf16 v[64:79], a[244:247], a[180:183], v[64:79]\n"
    "ds_read_b128 v[228:231], v3 offset:27648\n"
    "buffer_load_dwordx4 v0, s[40:43], s63 offen offset:2048 lds\n"
    "v_mfma_f32_32x32x16_bf16 v[64:79], a[248:251], a[184:187], v[64:79]\n"
    "ds_read_b128 v[232:235], v3 offset:29696\n"
    "v_mfma_f32_32x32x16_bf16 v[64:79], a[252:255], a[188:191], v[64:79]\n"
    "ds_read_b128 v[236:239], v3 offset:31744\n"
    "buffer_load_dwordx4 v0, s[40:43], s63 offen offset:3072 lds\n"
    "s_waitcnt lgkmcnt(8)\n"
    "v_mfma_f32_32x32x16_bf16 a[0:15], v[176:179], v[112:115], a[0:15]\n"
    "ds_read_b128 a[192:195], v0 offset:32768\n"
    "s_cmp_eq_u32 s57, s59\n"
    "s_cbranch_scc1 .Lat_dg4_%=\n"
    ".Lat_dgr4_%=:\n"
    "v_max3_f32 v14, v48, v49, v50\n"
    "v_max3_f32 v15, v51, v52, v53\n"
    "v_max3_f32 v14, v14, v54, v55\n"
    "v_mfma_f32_32x32x16_bf16 a[16:31], v[180:183], v[112:115], a[16:31]\n"
    "ds_read_b128 a[196:199], v0 offset:33792\n"
    "v_max3_f32 v15, v15, v56, v57\n"
    "v_max3_f32 v14, v14, v58, v59\n"
    "v_max3_f32 v15, v15, v60, v61\n"
    "v_max3_f32 v14, v14, v62, v63\n"
    "v_max3_f32 v15, v15, v64, v65\n"
    "v_mfma_f32_32x32x16_bf16 a[32:47], v[184:187], v[112:115], a[32:47]\n"
    "ds_read_b128 a[200:203], v0 offset:34816\n"
    "v_max3_f32 v14, v14, v66, v67\n"
    "v_max3_f32 v15, v15, v68, v69\n"
    "v_max3_f32 v14, v14, v70, v71\n"
    "v_max3_f32 v15, v15, v72, v73\n"
    "v_max3_f32 v14, v14, v74, v75\n"
    "v_mfma_f32_32x32x16_bf16 a[48:63], v[188:191], v[112:115], a[48:63]\n"
    "ds_read_b128 a[204:207], v0 offset:35840\n"
    "v_max3_f32 v15, v15, v76, v77\n"
    "v_max3_f32 v14, v14, v78, v79\n"
    "v_max_f32 v14, v14, v15\n"
    "v_mov_b32 v15, v14\n"
    "s_nop 0\n"
    "v_mfma_f32_32x32x16_bf16 a[0:15], v[192:195], v[116:119], a[0:15]\n"
    "ds_read_b128 a[208:211], v0 offset:36864\n"
    "v_permlane32_swap_b32 v14, v15\n"
    "v_max_f32 v14, v14, v15\n"
    "v_cmp_lt_f32 vcc, 0x41f00000, v14\n"
    "v_mfma_f32_32x32x16_bf16 a[16:31], v[196:199], v[116:119], a[16:31]\n"
    "ds_read_b128 a[212:215], v0 offset:37888\n"
    "s_cbranch_vccnz .Lat_rs4_%=\n"
    ".Lat_rsr4_%=:\n"
    "v_exp_f32 v48, v48\n"
    "v_exp_f32 v49, v49\n"
    "v_mfma_f32_32x32x16_bf16 a[32:47], v[200:203], v[116:119], a[32:47]\n"
    "ds_read_b128 a[216:219], v0 offset:38912\n"
    "v_exp_f32 v50, v50\n"
    "v_exp_f32 v51, v51\n"
    "v_add_f32 v10, v10, v48\n"
    "v_mfma_f32_32x32x16_bf16 a[48:63], v[204:207], v[116:119], a[48:63]\n"
    "ds_read_b128 a[220:223], v0 offset:39936\n"
    "v_add_f32 v11, v11, v49\n"
    "v_cvt_pk_bf16_f32 v128, v48, v49\n"
    "v_exp_f32 v52, v52\n"
    "s_waitcnt lgkmcnt(8)\n"
    "v_mfma_f32_32x32x16_bf16 a[0:15], v[208:211], v[120:123], a[0:15]\n"
    "ds_read_b128 a[224:227], v0 offset:40960\n"
    "v_exp_f32 v53, v53\n"
    "v_add_f32 v10, v10, v50\n"
    "v_add_f32 v11, v11, v51\n"
    "v_cvt_pk_bf16_f32 v129, v50, v51\n"
    "v_mfma_f32_32x32x16_bf16 a[16:31], v[212:215], v[120:123], a[16:31]\n"
    "ds_read_b128 a[228:231], v0 offset:41984\n"
    "v_exp_f32 v54, v54\n"
    "v_exp_f32 v55, v55\n"
    "v_add_f32 v10, v10, v52\n"
    "v_mfma_f32_32x32x16_bf16 a[32:47], v[216:219], v[120:123], a[32:47]\n"
    "ds_read_b128 a[232:235], v0 offset:43008\n"
    "v_add_f32 v11, v11, v53\n"
    "v_cvt_pk_bf16_f32 v130, v52, v53\n"
    "v_exp_f32 v56, v56\n"
    "v_mfma_f32_32x32x16_bf16 a[48:63], v[220:223], v[120:123], a[48:63]\n"
    "ds_read_b128 a[236:239], v0 offset:44032\n"
    "v_exp_f32 v57, v57\n"
    "v_add_f32 v10, v10, v54\n"
    "v_add_f32 v11, v11, v55\n"
    "v_cvt_pk_bf16_f32 v131, v54, v55\n"
    "v_mfma_f32_32x32x16_bf16 a[0:15], v[224:227], v[124:127], a[0:15]\n"
    "ds_read_b128 a[240:243], v0 offset:45056\n"
    "v_exp_f32 v58, v58\n"
    "v_exp_f32 v59, v59\n"
    "v_add_f32 v10, v10, v56\n"
    "v_mfma_f32_32x32x16_bf16 a[16:31], v[228:231], v[124:127], a[16:31]\n"
    "ds_read_b128 a[244:247], v0 offset:46080\n"
    "v_add_f32 v11, v11, v57\n"
    "v_cvt_pk_bf16_f32 v132, v56, v57\n"
    "v_exp_f32 v60, v60\n"
    "v_mfma_f32_32x32x16_bf16 a[32:47], v[232:235], v[124:127], a[32:47]\n"
    "ds_read_b128 a[248:251], v0 offset:47104\n"
    "v_exp_f32 v61, v61\n"
    "v_add_f32 v10, v10, v58\n"
    "v_add_f32 v11, v11, v59\n"
    "v_cvt_pk_bf16_f32 v133, v58, v59\n"
    "v_mfma_f32_32x32x16_bf16 a[48:63], v[236:239], v[124:127], a[48:63]\n"
    "ds_read_b128 a[252:255], v0 offset:48128\n"
    "v_exp_f32 v62, v62\n"
    "v_exp_f32 v63, v63\n"
    "v_add_f32 v10, v10, v60\n"
    "s_add_u32 s57, s57, 1\n"
    "s_cmp_gt_u32 s57, s59\n"
    "s_cbranch_scc1 .Lat_drain_%=\n"
    "s_waitcnt lgkmcnt(8)\n"
    "v_mfma_f32_32x32x16_bf16 v[16:31], a[192:195], a[128:131], v[80:95]\n"
    "v_add_f32 v11, v11, v61\n"
    "v_cvt_pk_bf16_f32 v134, v60, v61\n"
    "v_exp_f32 v64, v64\n"
    "v_exp_f32 v65, v65\n"
    "v_mfma_f32_32x32x16_bf16 v[16:31], a[196:199], a[132:135], v[16:31]\n"
    "v_add_f32 v10, v10, v62\n"
    "v_add_f32 v11, v11, v63\n"
    "v_cvt_pk_bf16_f32 v135, v62, v63\n"
    "v_exp_f32 v66, v66\n"
    "v_mfma_f32_32x32x16_bf16 v[16:31], a[200:203], a[136:139], v[16:31]\n"
    "v_exp_f32 v67, v67\n"
    "v_add_f32 v10, v10, v64\n"
    "v_add_f32 v11, v11, v65\n"
    "v_cvt_pk_bf16_f32 v136, v64, v65\n"
    "v_mfma_f32_32x32x16_bf16 v[16:31], a[204:207], a[140:143], v[16:31]\n"
    "v_exp_f32 v68, v68\n"
    "v_exp_f32 v69, v69\n"
    "v_add_f32 v10, v10, v66\n"
    "v_add_f32 v11, v11, v67\n"
    "v_mfma_f32_32x32x16_bf16 v[16:31], a[208:211], a[144:147], v[16:31]\n"
    "v_cvt_pk_bf16_f32 v137, v66, v67\n"
    "v_exp_f32 v70, v70\n"
    "v_exp_f32 v71, v71\n"
    "v_add_f32 v10, v10, v68\n"
    "v_mfma_f32_32x32x16_bf16 v[16:31], a[212:215], a[148:151], v[16:31]\n"
    "v_add_f32 v11, v11, v69\n"
    "v_cvt_pk_bf16_f32 v138, v68, v69\n"
    "v_exp_f32 v72, v72\n"
    "v_exp_f32 v73, v73\n"
    "v_mfma_f32_32x32x16_bf16 v[16:31], a[216:219], a[152:155], v[16:31]\n"
    "v_add_f32 v10, v10, v70\n"
    "v_add_f32 v11, v11, v71\n"
    "v_cvt_pk_bf16_f32 v139, v70, v71\n"
    "v_exp_f32 v74, v74\n"
    "v_mfma_f32_32x32x16_bf16 v[16:31], a[220:223], a[156:159], v[16:31]\n"
    "v_exp_f32 v75, v75\n"
    "v_add_f32 v10, v10, v72\n"
    "v_add_f32 v11, v11, v73\n"
    "v_cvt_pk_bf16_f32 v140, v72, v73\n"
    "s_waitcnt lgkmcnt(0)\n"
    "v_mfma_f32_32x32x16_bf16 v[32:47], a[224:227], a[128:131], v[80:95]\n"
    "v_exp_f32 v76, v76\n"
    "v_exp_f32 v77, v77\n"
    "v_add_f32 v10, v10, v74\n"
    "v_add_f32 v11, v11, v75\n"
    "v_mfma_f32_32x32x16_bf16 v[32:47], a[228:231], a[132:135], v[32:47]\n"
    "v_cvt_pk_bf16_f32 v141, v74, v75\n"
    "v_exp_f32 v78, v78\n"
    "v_exp_f32 v79, v79\n"
    "v_add_f32 v10, v10, v76\n"
    "v_mfma_f32_32x32x16_bf16 v[32:47], a[232:235], a[136:139], v[32:47]\n"
    "v_add_f32 v11, v11, v77\n"
    "v_cvt_pk_bf16_f32 v142, v76, v77\n"
    "v_add_f32 v10, v10, v78\n"
    "v_add_f32 v11, v11, v79\n"
    "v_cvt_pk_bf16_f32 v143, v78, v79\n"
    "v_mfma_f32_32x32x16_bf16 v[32:47], a[236:239], a[140:143], v[32:47]\n"
    "v_mfma_f32_32x32x16_bf16 v[32:47], a[240:243], a[144:147], v[32:47]\n"
    "s_add_u32 s63, s62, 0x1000000\n"
    "v_mfma_f32_32x32x16_bf16 v[32:47], a[244:247], a[148:151], v[32:47]\n"
    "s_add_u32 s56, s56, 1\n"
    "v_mfma_f32_32x32x16_bf16 v[32:47], a[248:251], a[152:155], v[32:47]\n"
    "s_add_u32 s62, s62, 0x4000\n"
    "v_mfma_f32_32x32x16_bf16 v[32:47], a[252:255], a[156:159], v[32:47]\n"
    "s_cmp_eq_u32 s56, s61\n"
    "s_cselect_b32 s62, s64, s62\n"
    "v_mfma_f32_32x32x16_bf16 a[64:79], v[176:179], v[128:131], a[64:79]\n"
    "s_cmp_eq_u32 s57, s59\n"
    "s_cbranch_scc1 .Lat_dg5_%=\n"
    ".Lat_dgr5_%=:\n"
    "v_max3_f32 v12, v16, v17, v18\n"
    "v_max3_f32 v13, v19, v20, v21\n"
    "v_max3_f32 v12, v12, v22, v23\n"
    "v_mfma_f32_32x32x16_bf16 a[80:95], v[180:183], v[128:131], a[80:95]\n"
    "v_max3_f32 v13, v13, v24, v25\n"
    "v_max3_f32 v12, v12, v26, v27\n"
    "v_max3_f32 v13, v13, v28, v29\n"
    "v_max3_f32 v12, v12, v30, v31\n"
    "v_max3_f32 v13, v13, v32, v33\n"
    "v_mfma_f32_32x32x16_bf16 a[96:111], v[184:187], v[128:131], a[96:111]\n"
    "v_max3_f32 v12, v12, v34, v35\n"
    "v_max3_f32 v13, v13, v36, v37\n"
    "v_max3_f32 v12, v12, v38, v39\n"
    "v_max3_f32 v13, v13, v40, v41\n"
    "v_max3_f32 v12, v12, v42, v43\n"
    "v_mfma_f32_32x32x16_bf16 a[112:127], v[188:191], v[128:131], a[112:127]\n"
    "v_max3_f32 v13, v13, v44, v45\n"
    "v_max3_f32 v12, v12, v46, v47\n"
    "v_max_f32 v12, v12, v13\n"
    "v_mov_b32 v13, v12\n"
    "s_nop 0\n"
    "v_mfma_f32_32x32x16_bf16 a[64:79], v[192:195], v[132:135], a[64:79]\n"
    "v_permlane32_swap_b32 v12, v13\n"
    "v_max_f32 v12, v12, v13\n"
    "v_cmp_lt_f32 vcc, 0x41f00000, v12\n"
    "v_mfma_f32_32x32x16_bf16 a[80:95], v[196:199], v[132:135], a[80:95]\n"
    "s_cbranch_vccnz .Lat_rs5_%=\n"
    ".Lat_rsr5_%=:\n"
    "v_exp_f32 v16, v16\n"
    "v_exp_f32 v17, v17\n"
    "v_mfma_f32_32x32x16_bf16 a[96:111], v[200:203], v[132:135], a[96:111]\n"
    "v_exp_f32 v18, v18\n"
    "v_exp_f32 v19, v19\n"
    "v_add_f32 v8, v8, v16\n"
    "v_add_f32 v9, v9, v17\n"
    "v_mfma_f32_32x32x16_bf16 a[112:127], v[204:207], v[132:135], a[112:127]\n"
    "v_cvt_pk_bf16_f32 v112, v16, v17\n"
    "v_exp_f32 v20, v20\n"
    "v_exp_f32 v21, v21\n"
    "v_add_f32 v8, v8, v18\n"
    "v_mfma_f32_32x32x16_bf16 a[64:79], v[208:211], v[136:139], a[64:79]\n"
    "v_add_f32 v9, v9, v19\n"
    "v_cvt_pk_bf16_f32 v113, v18, v19\n"
    "v_exp_f32 v22, v22\n"
    "v_exp_f32 v23, v23\n"
    "v_mfma_f32_32x32x16_bf16 a[80:95], v[212:215], v[136:139], a[80:95]\n"
    "v_add_f32 v8, v8, v20\n"
    "v_add_f32 v9, v9, v21\n"
    "v_cvt_pk_bf16_f32 v114, v20, v21\n"
    "v_exp_f32 v24, v24\n"
    "v_mfma_f32_32x32x16_bf16 a[96:111], v[216:219], v[136:139], a[96:111]\n"
    "v_exp_f32 v25, v25\n"
    "v_add_f32 v8, v8, v22\n"
    "v_add_f32 v9, v9, v23\n"
    "v_cvt_pk_bf16_f32 v115, v22, v23\n"
    "v_mfma_f32_32x32x16_bf16 a[112:127], v[220:223], v[136:139], a[112:127]\n"
    "v_exp_f32 v26, v26\n"
    "v_exp_f32 v27, v27\n"
    "v_add_f32 v8, v8, v24\n"
    "v_add_f32 v9, v9, v25\n"
    "v_mfma_f32_32x32x16_bf16 a[64:79], v[224:227], v[140:143], a[64:79]\n"
    "v_cvt_pk_bf16_f32 v116, v24, v25\n"
    "v_exp_f32 v28, v28\n"
    "v_exp_f32 v29, v29\n"
    "v_add_f32 v8, v8, v26\n"
    "v_mfma_f32_32x32x16_bf16 a[80:95], v[228:231], v[140:143], a[80:95]\n"
    "v_add_f32 v9, v9, v27\n"
    "v_cvt_pk_bf16_f32 v117, v26, v27\n"
    "v_exp_f32 v30, v30\n"
    "v_exp_f32 v31, v31\n"
    "v_mfma_f32_32x32x16_bf16 a[96:111], v[232:235], v[140:143], a[96:111]\n"
    "v_add_f32 v8, v8, v28\n"
    "v_add_f32 v9, v9, v29\n"
    "v_cvt_pk_bf16_f32 v118, v28, v29\n"
    "v_exp_f32 v32, v32\n"
    "v_mfma_f32_32x32x16_bf16 a[112:127], v[236:239], v[140:143], a[112:127]\n"
    "v_exp_f32 v33, v33\n"
    "v_add_f32 v8, v8, v30\n"
    "v_add_f32 v9, v9, v31\n"
    "v_cvt_pk_bf16_f32 v119, v30, v31\n"
    "s_waitcnt vmcnt(8)\n"
    "s_barrier\n"
    "v_mfma_f32_32x32x16_bf16 v[48:63], a[192:195], a[160:163], v[96:111]\n"
    "ds_read_b128 v[176:179], v3 offset:32768\n"
    "v_exp_f32 v34, v34\n"
    "v_exp_f32 v35, v35\n"
    "v_add_f32 v8, v8, v32\n"
    "v_mfma_f32_32x32x16_bf16 v[48:63], a[196:199], a[164:167], v[48:63]\n"
    "ds_read_b128 v[180:183], v3 offset:34816\n"
    "s_add_u32 m0, s67, 16384\n"
    "s_nop 0\n"
    "buffer_load_dwordx4 v0, s[40:43], s62 offen offset:0 lds\n"
    "v_add_f32 v9, v9, v33\n"
    "v_cvt_pk_bf16_f32 v120, v32, v33\n"
    "v_mfma_f32_32x32x16_bf16 v[48:63], a[200:203], a[168:171], v[48:63]\n"
    "ds_read_b128 v[184:187], v3 offset:36864\n"
    "v_exp_f32 v36, v36\n"
    "v_exp_f32 v37, v37\n"
    "v_add_f32 v8, v8, v34\n"
    "v_mfma_f32_32x32x16_bf16 v[48:63], a[204:207], a[172:175], v[48:63]\n"
    "ds_read_b128 v[188:191], v3 offset:38912\n"
    "buffer_load_dwordx4 v0, s[40:43], s62 offen offset:1024 lds\n"
    "v_add_f32 v9, v9, v35\n"
    "v_cvt_pk_bf16_f32 v121, v34, v35\n"
    "v_exp_f32 v38, v38\n"
    "v_mfma_f32_32x32x16_bf16 v[48:63], a[208:211], a[176:179], v[48:63]\n"
    "ds_read_b128 v[192:195], v3 offset:33792\n"
    "v_exp_f32 v39, v39\n"
    "v_add_f32 v8, v8, v36\n"
    "v_add_f32 v9, v9, v37\n"
    "v_cvt_pk_bf16_f32 v122, v36, v37\n"
    "v_mfma_f32_32x32x16_bf16 v[48:63], a[212:215], a[180:183], v[48:63]\n"
    "ds_read_b128 v[196:199], v3 offset:35840\n"
    "buffer_load_dwordx4 v0, s[40:43], s62 offen offset:2048 lds\n"
    "v_exp_f32 v40, v40\n"
    "v_exp_f32 v41, v41\n"
    "v_mfma_f32_32x32x16_bf16 v[48:63], a[216:219], a[184:187], v[48:63]\n"
    "ds_read_b128 v[200:203], v3 offset:37888\n"
    "v_add_f32 v8, v8, v38\n"
    "v_add_f32 v9, v9, v39\n"
    "v_cvt_pk_bf16_f32 v123, v38, v39\n"
    "v_exp_f32 v42, v42\n"
    "v_mfma_f32_32x32x16_bf16 v[48:63], a[220:223], a[188:191], v[48:63]\n"
    "ds_read_b128 v[204:207], v3 offset:39936\n"
    "buffer_load_dwordx4 v0, s[40:43], s62 offen offset:3072 lds\n"
    "v_exp_f32 v43, v43\n"
    "v_add_f32 v8, v8, v40\n"
    "v_add_f32 v9, v9, v41\n"
    "v_mfma_f32_32x32x16_bf16 v[64:79], a[224:227], a[160:163], v[96:111]\n"
    "ds_read_b128 v[208:211], v3 offset:40960\n"
    "v_cvt_pk_bf16_f32 v124, v40, v41\n"
    "v_exp_f32 v44, v44\n"
    "v_exp_f32 v45, v45\n"
    "v_mfma_f32_32x32x16_bf16 v[64:79], a[228:231], a[164:167], v[64:79]\n"
    "ds_read_b128 v[212:215], v3 offset:43008\n"
    "s_add_u32 m0, s68, 0\n"
    "s_nop 0\n"
    "buffer_load_dwordx4 v0, s[40:43], s63 offen offset:0 lds\n"
    "v_add_f32 v8, v8, v42\n"
    "v_add_f32 v9, v9, v43\n"
    "v_mfma_f32_32x32x16_bf16 v[64:79], a[232:235], a[168:171], v[64:79]\n"
    "ds_read_b128 v[216:219], v3 offset:45056\n"
    "v_cvt_pk_bf16_f32 v125, v42, v43\n"
    "v_exp_f32 v46, v46\n"
    "v_exp_f32 v47, v47\n"
    "v_mfma_f32_32x32x16_bf16 v[64:79], a[236:239], a[172:175], v[64:79]\n"
    "ds_read_b128 v[220:223], v3 offset:47104\n"
    "buffer_load_dwordx4 v0, s[40:43], s63 offen offset:1024 lds\n"
    "v_add_f32 v8, v8, v44\n"
    "v_add_f32 v9, v9, v45\n"
    "v_cvt_pk_bf16_f32 v126, v44, v45\n"
    "v_add_f32 v8, v8, v46\n"
    "v_mfma_f32_32x32x16_bf16 v[64:79], a[240:243], a[176:179], v[64:79]\n"
    "ds_read_b128 v[224:227], v3 offset:41984\n"
    "v_add_f32 v9, v9, v47\n"
    "v_cvt_pk_bf16_f32 v127, v46, v47\n"
    "v_mfma_f32_32x32x16_bf16 v[64:79], a[244:247], a[180:183], v[64:79]\n"
    "ds_read_b128 v[228:231], v3 offset:44032\n"
    "buffer_load_dwordx4 v0, s[40:43], s63 offen offset:2048 lds\n"
    "v_mfma_f32_32x32x16_bf16 v[64:79], a[248:251], a[184:187], v[64:79]\n"
    "ds_read_b128 v[232:235], v3 offset:46080\n"
    "v_mfma_f32_32x32x16_bf16 v[64:79], a[252:255], a[188:191], v[64:79]\n"
    "ds_read_b128 v[236:239], v3 offset:48128\n"
    "buffer_load_dwordx4 v0, s[40:43], s63 offen offset:3072 lds\n"
    "s_waitcnt lgkmcnt(8)\n"
    "v_mfma_f32_32x32x16_bf16 a[0:15], v[176:179], v[112:115], a[0:15]\n"
    "ds_read_b128 a[192:195], v0 offset:49152\n"
    "s_cmp_eq_u32 s57, s59\n"
    "s_cbranch_scc1 .Lat_dg6_%=\n"
    ".Lat_dgr6_%=:\n"
    "v_max3_f32 v14, v48, v49, v50\n"
    "v_max3_f32 v15, v51, v52, v53\n"
    "v_max3_f32 v14, v14, v54, v55\n"
    "v_mfma_f32_32x32x16_bf16 a[16:31], v[180:183], v[112:115], a[16:31]\n"
    "ds_read_b128 a[196:199], v0 offset:50176\n"
    "v_max3_f32 v15, v15, v56, v57\n"
    "v_max3_f32 v14, v14, v58, v59\n"
    "v_max3_f32 v15, v15, v60, v61\n"
    "v_max3_f32 v14, v14, v62, v63\n"
    "v_max3_f32 v15, v15, v64, v65\n"
    "v_mfma_f32_32x32x16_bf16 a[32:47], v[184:187], v[112:115], a[32:47]\n"
    "ds_read_b128 a[200:203], v0 offset:51200\n"
    "v_max3_f32 v14, v14, v66, v67\n"
    "v_max3_f32 v15, v15, v68, v69\n"
    "v_max3_f32 v14, v14, v70, v71\n"
    "v_max3_f32 v15, v15, v72, v73\n"
    "v_max3_f32 v14, v14, v74, v75\n"
    "v_mfma_f32_32x32x16_bf16 a[48:63], v[188:191], v[112:115], a[48:63]\n"
    "ds_read_b128 a[204:207], v0 offset:52224\n"
    "v_max3_f32 v15, v15, v76, v77\n"
    "v_max3_f32 v14, v14, v78, v79\n"
    "v_max_f32 v14, v14, v15\n"
    "v_mov_b32 v15, v14\n"
    "s_nop 0\n"
    "v_mfma_f32_32x32x16_bf16 a[0:15], v[192:195], v[116:119], a[0:15]\n"
    "ds_read_b128 a[208:211], v0 offset:53248\n"
    "v_permlane32_swap_b32 v14, v15\n"
    "v_max_f32 v14, v14, v15\n"
    "v_cmp_lt_f32 vcc, 0x41f00000, v14\n"
    "v_mfma_f32_32x32x16_bf16 a[16:31], v[196:199], v[116:119], a[16:31]\n"
    "ds_read_b128 a[212:215], v0 offset:54272\n"
    "s_cbranch_vccnz .Lat_rs6_%=\n"
    ".Lat_rsr6_%=:\n"
    "v_exp_f32 v48, v48\n"
    "v_exp_f32 v49, v49\n"
    "v_mfma_f32_32x32x16_bf16 a[32:47], v[200:203], v[116:119], a[32:47]\n"
    "ds_read_b128 a[216:219], v0 offset:55296\n"
    "v_exp_f32 v50, v50\n"
    "v_exp_f32 v51, v51\n"
    "v_add_f32 v10, v10, v48\n"
    "v_mfma_f32_32x32x16_bf16 a[48:63], v[204:207], v[116:119], a[48:63]\n"
    "ds_read_b128 a[220:223], v0 offset:56320\n"
    "v_add_f32 v11, v11, v49\n"
    "v_cvt_pk_bf16_f32 v128, v48, v49\n"
    "v_exp_f32 v52, v52\n"
    "s_waitcnt lgkmcnt(8)\n"
    "v_mfma_f32_32x32x16_bf16 a[0:15], v[208:211], v[120:123], a[0:15]\n"
    "ds_read_b128 a[224:227], v0 offset:57344\n"
    "v_exp_f32 v53, v53\n"
    "v_add_f32 v10, v10, v50\n"
    "v_add_f32 v11, v11, v51\n"
    "v_cvt_pk_bf16_f32 v129, v50, v51\n"
    "v_mfma_f32_32x32x16_bf16 a[16:31], v[212:215], v[120:123], a[16:31]\n"
    "ds_read_b128 a[228:231], v0 offset:58368\n"
    "v_exp_f32 v54, v54\n"
    "v_exp_f32 v55, v55\n"
    "v_add_f32 v10, v10, v52\n"
    "v_mfma_f32_32x32x16_bf16 a[32:47], v[216:219], v[120:123], a[32:47]\n"
    "ds_read_b128 a[232:235], v0 offset:59392\n"
    "v_add_f32 v11, v11, v53\n"
    "v_cvt_pk_bf16_f32 v130, v52, v53\n"
    "v_exp_f32 v56, v56\n"
    "v_mfma_f32_32x32x16_bf16 a[48:63], v[220:223], v[120:123], a[48:63]\n"
    "ds_read_b128 a[236:239], v0 offset:60416\n"
    "v_exp_f32 v57, v57\n"
    "v_add_f32 v10, v10, v54\n"
    "v_add_f32 v11, v11, v55\n"
    "v_cvt_pk_bf16_f32 v131, v54, v55\n"
    "v_mfma_f32_32x32x16_bf16 a[0:15], v[224:227], v[124:127], a[0:15]\n"
    "ds_read_b128 a[240:243], v0 offset:61440\n"
    "v_exp_f32 v58, v58\n"
    "v_exp_f32 v59, v59\n"
    "v_add_f32 v10, v10, v56\n"
    "v_mfma_f32_32x32x16_bf16 a[16:31], v[228:231], v[124:127], a[16:31]\n"
    "ds_read_b128 a[244:247], v0 offset:62464\n"
    "v_add_f32 v11, v11, v57\n"
    "v_cvt_pk_bf16_f32 v132, v56, v57\n"
    "v_exp_f32 v60, v60\n"
    "v_mfma_f32_32x32x16_bf16 a[32:47], v[232:235], v[124:127], a[32:47]\n"
    "ds_read_b128 a[248:251], v0 offset:63488\n"
    "v_exp_f32 v61, v61\n"
    "v_add_f32 v10, v10, v58\n"
    "v_add_f32 v11, v11, v59\n"
    "v_cvt_pk_bf16_f32 v133, v58, v59\n"
    "v_mfma_f32_32x32x16_bf16 a[48:63], v[236:239], v[124:127], a[48:63]\n"
    "ds_read_b128 a[252:255], v0 offset:64512\n"
    "v_exp_f32 v62, v62\n"
    "v_exp_f32 v63, v63\n"
    "v_add_f32 v10, v10, v60\n"
    "s_add_u32 s57, s57, 1\n"
    "s_cmp_gt_u32 s57, s59\n"
    "s_cbranch_scc1 .Lat_drain_%=\n"
    "s_waitcnt lgkmcnt(8)\n"
    "v_mfma_f32_32x32x16_bf16 v[16:31], a[192:195], a[128:131], v[80:95]\n"
    "v_add_f32 v11, v11, v61\n"
    "v_cvt_pk_bf16_f32 v134, v60, v61\n"
    "v_exp_f32 v64, v64\n"
    "v_exp_f32 v65, v65\n"
    "v_mfma_f32_32x32x16_bf16 v[16:31], a[196:199], a[132:135], v[16:31]\n"
    "v_add_f32 v10, v10, v62\n"
    "v_add_f32 v11, v11, v63\n"
    "v_cvt_pk_bf16_f32 v135, v62, v63\n"
    "v_exp_f32 v66, v66\n"
    "v_mfma_f32_32x32x16_bf16 v[16:31], a[200:203], a[136:139], v[16:31]\n"
    "v_exp_f32 v67, v67\n"
    "v_add_f32 v10, v10, v64\n"
    "v_add_f32 v11, v11, v65\n"
    "v_cvt_pk_bf16_f32 v136, v64, v65\n"
    "v_mfma_f32_32x32x16_bf16 v[16:31], a[204:207], a[140:143], v[16:31]\n"
    "v_exp_f32 v68, v68\n"
    "v_exp_f32 v69, v69\n"
    "v_add_f32 v10, v10, v66\n"
    "v_add_f32 v11, v11, v67\n"
    "v_mfma_f32_32x32x16_bf16 v[16:31], a[208:211], a[144:147], v[16:31]\n"
    "v_cvt_pk_bf16_f32 v137, v66, v67\n"
    "v_exp_f32 v70, v70\n"
    "v_exp_f32 v71, v71\n"
    "v_add_f32 v10, v10, v68\n"
    "v_mfma_f32_32x32x16_bf16 v[16:31], a[212:215], a[148:151], v[16:31]\n"
    "v_add_f32 v11, v11, v69\n"
    "v_cvt_pk_bf16_f32 v138, v68, v69\n"
    "v_exp_f32 v72, v72\n"
    "v_exp_f32 v73, v73\n"
    "v_mfma_f32_32x32x16_bf16 v[16:31], a[216:219], a[152:155], v[16:31]\n"
    "v_add_f32 v10, v10, v70\n"
    "v_add_f32 v11, v11, v71\n"
    "v_cvt_pk_bf16_f32 v139, v70, v71\n"
    "v_exp_f32 v74, v74\n"
    "v_mfma_f32_32x32x16_bf16 v[16:31], a[220:223], a[156:159], v[16:31]\n"
    "v_exp_f32 v75, v75\n"
    "v_add_f32 v10, v10, v72\n"
    "v_add_f32 v11, v11, v73\n"
    "v_cvt_pk_bf16_f32 v140, v72, v73\n"
    "s_waitcnt lgkmcnt(0)\n"
    "v_mfma_f32_32x32x16_bf16 v[32:47], a[224:227], a[128:131], v[80:95]\n"
    "v_exp_f32 v76, v76\n"
    "v_exp_f32 v77, v77\n"
    "v_add_f32 v10, v10, v74\n"
    "v_add_f32 v11, v11, v75\n"
    "v_mfma_f32_32x32x16_bf16 v[32:47], a[228:231], a[132:135], v[32:47]\n"
    "v_cvt_pk_bf16_f32 v141, v74, v75\n"
    "v_exp_f32 v78, v78\n"
    "v_exp_f32 v79, v79\n"
    "v_add_f32 v10, v10, v76\n"
    "v_mfma_f32_32x32x16_bf16 v[32:47], a[232:235], a[136:139], v[32:47]\n"
    "v_add_f32 v11, v11, v77\n"
    "v_cvt_pk_bf16_f32 v142, v76, v77\n"
    "v_add_f32 v10, v10, v78\n"
    "v_add_f32 v11, v11, v79\n"
    "v_cvt_pk_bf16_f32 v143, v78, v79\n"
    "v_mfma_f32_32x32x16_bf16 v[32:47], a[236:239], a[140:143], v[32:47]\n"
    "v_mfma_f32_32x32x16_bf16 v[32:47], a[240:243], a[144:147], v[32:47]\n"
    "s_add_u32 s63, s62, 0x1000000\n"
    "v_mfma_f32_32x32x16_bf16 v[32:47], a[244:247], a[148:151], v[32:47]\n"
    "s_add_u32 s56, s56, 1\n"
    "v_mfma_f32_32x32x16_bf16 v[32:47], a[248:251], a[152:155], v[32:47]\n"
    "s_add_u32 s62, s62, 0x4000\n"
    "v_mfma_f32_32x32x16_bf16 v[32:47], a[252:255], a[156:159], v[32:47]\n"
    "s_cmp_eq_u32 s56, s61\n"
    "s_cselect_b32 s62, s64, s62\n"
    "v_mfma_f32_32x32x16_bf16 a[64:79], v[176:179], v[128:131], a[64:79]\n"
    "s_cmp_eq_u32 s57, s59\n"
    "s_cbranch_scc1 .Lat_dg7_%=\n"
    ".Lat_dgr7_%=:\n"
    "v_max3_f32 v12, v16, v17, v18\n"
    "v_max3_f32 v13, v19, v20, v21\n"
    "v_max3_f32 v12, v12, v22, v23\n"
    "v_mfma_f32_32x32x16_bf16 a[80:95], v[180:183], v[128:131], a[80:95]\n"
    "v_max3_f32 v13, v13, v24, v25\n"
    "v_max3_f32 v12, v12, v26, v27\n"
    "v_max3_f32 v13, v13, v28, v29\n"
    "v_max3_f32 v12, v12, v30, v31\n"
    "v_max3_f32 v13, v13, v32, v33\n"
    "v_mfma_f32_32x32x16_bf16 a[96:111], v[184:187], v[128:131], a[96:111]\n"
    "v_max3_f32 v12, v12, v34, v35\n"
    "v_max3_f32 v13, v13, v36, v37\n"
    "v_max3_f32 v12, v12, v38, v39\n"
    "v_max3_f32 v13, v13, v40, v41\n"
    "v_max3_f32 v12, v12, v42, v43\n"
    "v_mfma_f32_32x32x16_bf16 a[112:127], v[188:191], v[128:131], a[112:127]\n"
    "v_max3_f32 v13, v13, v44, v45\n"
    "v_max3_f32 v12, v12, v46, v47\n"
    "v_max_f32 v12, v12, v13\n"
    "v_mov_b32 v13, v12\n"
    "s_nop 0\n"
    "v_mfma_f32_32x32x16_bf16 a[64:79], v[192:195], v[132:135], a[64:79]\n"
    "v_permlane32_swap_b32 v12, v13\n"
    "v_max_f32 v12, v12, v13\n"
    "v_cmp_lt_f32 vcc, 0x41f00000, v12\n"
    "v_mfma_f32_32x32x16_bf16 a[80:95], v[196:199], v[132:135], a[80:95]\n"
    "s_cbranch_vccnz .Lat_rs7_%=\n"
    ".Lat_rsr7_%=:\n"
    "v_exp_f32 v16, v16\n"
    "v_exp_f32 v17, v17\n"
    "v_mfma_f32_32x32x16_bf16 a[96:111], v[200:203], v[132:135], a[96:111]\n"
    "v_exp_f32 v18, v18\n"
    "v_exp_f32 v19, v19\n"
    "v_add_f32 v8, v8, v16\n"
    "v_add_f32 v9, v9, v17\n"
    "v_mfma_f32_32x32x16_bf16 a[112:127], v[204:207], v[132:135], a[112:127]\n"
    "v_cvt_pk_bf16_f32 v112, v16, v17\n"
    "v_exp_f32 v20, v20\n"
    "v_exp_f32 v21, v21\n"
    "v_add_f32 v8, v8, v18\n"
    "v_mfma_f32_32x32x16_bf16 a[64:79], v[208:211], v[136:139], a[64:79]\n"
    "v_add_f32 v9, v9, v19\n"
    "v_cvt_pk_bf16_f32 v113, v18, v19\n"
    "v_exp_f32 v22, v22\n"
    "v_exp_f32 v23, v23\n"
    "v_mfma_f32_32x32x16_bf16 a[80:95], v[212:215], v[136:139], a[80:95]\n"
    "v_add_f32 v8, v8, v20\n"
    "v_add_f32 v9, v9, v21\n"
    "v_cvt_pk_bf16_f32 v114, v20, v21\n"
    "v_exp_f32 v24, v24\n"
    "v_mfma_f32_32x32x16_bf16 a[96:111], v[216:219], v[136:139], a[96:111]\n"
    "v_exp_f32 v25, v25\n"
    "v_add_f32 v8, v8, v22\n"
    "v_add_f32 v9, v9, v23\n"
    "v_cvt_pk_bf16_f32 v115, v22, v23\n"
    "v_mfma_f32_32x32x16_bf16 a[112:127], v[220:223], v[136:139], a[112:127]\n"
    "v_exp_f32 v26, v26\n"
    "v_exp_f32 v27, v27\n"
    "v_add_f32 v8, v8, v24\n"
    "v_add_f32 v9, v9, v25\n"
    "v_mfma_f32_32x32x16_bf16 a[64:79], v[224:227], v[140:143], a[64:79]\n"
    "v_cvt_pk_bf16_f32 v116, v24, v25\n"
    "v_exp_f32 v28, v28\n"
    "v_exp_f32 v29, v29\n"
    "v_add_f32 v8, v8, v26\n"
    "v_mfma_f32_32x32x16_bf16 a[80:95], v[228:231], v[140:143], a[80:95]\n"
    "v_add_f32 v9, v9, v27\n"
    "v_cvt_pk_bf16_f32 v117, v26, v27\n"
    "v_exp_f32 v30, v30\n"
    "v_exp_f32 v31, v31\n"
    "v_mfma_f32_32x32x16_bf16 a[96:111], v[232:235], v[140:143], a[96:111]\n"
    "v_add_f32 v8, v8, v28\n"
    "v_add_f32 v9, v9, v29\n"
    "v_cvt_pk_bf16_f32 v118, v28, v29\n"
    "v_exp_f32 v32, v32\n"
    "v_mfma_f32_32x32x16_bf16 a[112:127], v[236:239], v[140:143], a[112:127]\n"
    "v_exp_f32 v33, v33\n"
    "v_add_f32 v8, v8, v30\n"
    "v_add_f32 v9, v9, v31\n"
    "v_cvt_pk_bf16_f32 v119, v30, v31\n"
    "s_waitcnt vmcnt(8)\n"
    "s_barrier\n"
    "v_mfma_f32_32x32x16_bf16 v[48:63], a[192:195], a[160:163], v[96:111]\n"
    "ds_read_b128 v[176:179], v3 offset:49152\n"
    "v_exp_f32 v34, v34\n"
    "v_exp_f32 v35, v35\n"
    "v_add_f32 v8, v8, v32\n"
    "v_mfma_f32_32x32x16_bf16 v[48:63], a[196:199], a[164:167], v[48:63]\n"
    "ds_read_b128 v[180:183], v3 offset:51200\n"
    "s_add_u32 m0, s67, 32768\n"
    "s_nop 0\n"
    "buffer_load_dwordx4 v0, s[40:43], s62 offen offset:0 lds\n"
    "v_add_f32 v9, v9, v33\n"
    "v_cvt_pk_bf16_f32 v120, v32, v33\n"
    "v_mfma_f32_32x32x16_bf16 v[48:63], a[200:203], a[168:171], v[48:63]\n"
    "ds_read_b128 v[184:187], v3 offset:53248\n"
    "v_exp_f32 v36, v36\n"
    "v_exp_f32 v37, v37\n"
    "v_add_f32 v8, v8, v34\n"
    "v_mfma_f32_32x32x16_bf16 v[48:63], a[204:207], a[172:175], v[48:63]\n"
    "ds_read_b128 v[188:191], v3 offset:55296\n"
    "buffer_load_dwordx4 v0, s[40:43], s62 offen offset:1024 lds\n"
    "v_add_f32 v9, v9, v35\n"
    "v_cvt_pk_bf16_f32 v121, v34, v35\n"
    "v_exp_f32 v38, v38\n"
    "v_mfma_f32_32x32x16_bf16 v[48:63], a[208:211], a[176:179], v[48:63]\n"
    "ds_read_b128 v[192:195], v3 offset:50176\n"
    "v_exp_f32 v39, v39\n"
    "v_add_f32 v8, v8, v36\n"
    "v_add_f32 v9, v9, v37\n"
    "v_cvt_pk_bf16_f32 v122, v36, v37\n"
    "v_mfma_f32_32x32x16_bf16 v[48:63], a[212:215], a[180:183], v[48:63]\n"
    "ds_read_b128 v[196:199], v3 offset:52224\n"
    "buffer_load_dwordx4 v0, s[40:43], s62 offen offset:2048 lds\n"
    "v_exp_f32 v40, v40\n"
    "v_exp_f32 v41, v41\n"
    "v_mfma_f32_32x32x16_bf16 v[48:63], a[216:219], a[184:187], v[48:63]\n"
    "ds_read_b128 v[200:203], v3 offset:54272\n"
    "v_add_f32 v8, v8, v38\n"
    "v_add_f32 v9, v9, v39\n"
    "v_cvt_pk_bf16_f32 v123, v38, v39\n"
    "v_exp_f32 v42, v42\n"
    "v_mfma_f32_32x32x16_bf16 v[48:63], a[220:223], a[188:191], v[48:63]\n"
    "ds_read_b128 v[204:207], v3 offset:56320\n"
    "buffer_load_dwordx4 v0, s[40:43], s62 offen offset:3072 lds\n"
    "v_exp_f32 v43, v43\n"
    "v_add_f32 v8, v8, v40\n"
    "v_add_f32 v9, v9, v41\n"
    "v_mfma_f32_32x32x16_bf16 v[64:79], a[224:227], a[160:163], v[96:111]\n"
    "ds_read_b128 v[208:211], v3 offset:57344\n"
    "v_cvt_pk_bf16_f32 v124, v40, v41\n"
    "v_exp_f32 v44, v44\n"
    "v_exp_f32 v45, v45\n"
    "v_mfma_f32_32x32x16_bf16 v[64:79], a[228:231], a[164:167], v[64:79]\n"
    "ds_read_b128 v[212:215], v3 offset:59392\n"
    "s_add_u32 m0, s68, 16384\n"
    "s_nop 0\n"
    "buffer_load_dwordx4 v0, s[40:43], s63 offen offset:0 lds\n"
    "v_add_f32 v8, v8, v42\n"
    "v_add_f32 v9, v9, v43\n"
    "v_mfma_f32_32x32x16_bf16 v[64:79], a[232:235], a[168:171], v[64:79]\n"
    "ds_read_b128 v[216:219], v3 offset:61440\n"
    "v_cvt_pk_bf16_f32 v125, v42, v43\n"
    "v_exp_f32 v46, v46\n"
    "v_exp_f32 v47, v47\n"
    "v_mfma_f32_32x32x16_bf16 v[64:79], a[236:239], a[172:175], v[64:79]\n"
    "ds_read_b128 v[220:223], v3 offset:63488\n"
    "buffer_load_dwordx4 v0, s[40:43], s63 offen offset:1024 lds\n"
    "v_add_f32 v8, v8, v44\n"
    "v_add_f32 v9, v9, v45\n"
    "v_cvt_pk_bf16_f32 v126, v44, v45\n"
    "v_add_f32 v8, v8, v46\n"
    "v_mfma_f32_32x32x16_bf16 v[64:79], a[240:243], a[176:179], v[64:79]\n"
    "ds_read_b128 v[224:227], v3 offset:58368\n"
    "v_add_f32 v9, v9, v47\n"
    "v_cvt_pk_bf16_f32 v127, v46, v47\n"
    "v_mfma_f32_32x32x16_bf16 v[64:79], a[244:247], a[180:183], v[64:79]\n"
    "ds_read_b128 v[228:231], v3 offset:60416\n"
    "buffer_load_dwordx4 v0, s[40:43], s63 offen offset:2048 lds\n"
    "v_mfma_f32_32x32x16_bf16 v[64:79], a[248:251], a[184:187], v[64:79]\n"
    "ds_read_b128 v[232:235], v3 offset:62464\n"
    "v_mfma_f32_32x32x16_bf16 v[64:79], a[252:255], a[188:191], v[64:79]\n"
    "ds_read_b128 v[236:239], v3 offset:64512\n"
    "buffer_load_dwordx4 v0, s[40:43], s63 offen offset:3072 lds\n"
    "s_waitcnt lgkmcnt(8)\n"
    "v_mfma_f32_32x32x16_bf16 a[0:15], v[176:179], v[112:115], a[0:15]\n"
    "ds_read_b128 a[192:195], v0 offset:0\n"
    "s_cmp_eq_u32 s57, s59\n"
    "s_cbranch_scc1 .Lat_dg8_%=\n"
    ".Lat_dgr8_%=:\n"
    "v_max3_f32 v14, v48, v49, v50\n"
    "v_max3_f32 v15, v51, v52, v53\n"
    "v_max3_f32 v14, v14, v54, v55\n"
    "v_mfma_f32_32x32x16_bf16 a[16:31], v[180:183], v[112:115], a[16:31]\n"
    "ds_read_b128 a[196:199], v0 offset:1024\n"
    "v_max3_f32 v15, v15, v56, v57\n"
    "v_max3_f32 v14, v14, v58, v59\n"
    "v_max3_f32 v15, v15, v60, v61\n"
    "v_max3_f32 v14, v14, v62, v63\n"
    "v_max3_f32 v15, v15, v64, v65\n"
    "v_mfma_f32_32x32x16_bf16 a[32:47], v[184:187], v[112:115], a[32:47]\n"
    "ds_read_b128 a[200:203], v0 offset:2048\n"
    "v_max3_f32 v14, v14, v66, v67\n"
    "v_max3_f32 v15, v15, v68, v69\n"
    "v_max3_f32 v14, v14, v70, v71\n"
    "v_max3_f32 v15, v15, v72, v73\n"
    "v_max3_f32 v14, v14, v74, v75\n"
    "v_mfma_f32_32x32x16_bf16 a[48:63], v[188:191], v[112:115], a[48:63]\n"
    "ds_read_b128 a[204:207], v0 offset:3072\n"
    "v_max3_f32 v15, v15, v76, v77\n"
    "v_max3_f32 v14, v14, v78, v79\n"
    "v_max_f32 v14, v14, v15\n"
    "v_mov_b32 v15, v14\n"
    "s_nop 0\n"
    "v_mfma_f32_32x32x16_bf16 a[0:15], v[192:195], v[116:119], a[0:15]\n"
    "ds_read_b128 a[208:211], v0 offset:4096\n"
    "v_permlane32_swap_b32 v14, v15\n"
    "v_max_f32 v14, v14, v15\n"
    "v_cmp_lt_f32 vcc, 0x41f00000, v14\n"
    "v_mfma_f32_32x32x16_bf16 a[16:31], v[196:199], v[116:119], a[16:31]\n"
    "ds_read_b128 a[212:215], v0 offset:5120\n"
    "s_cbranch_vccnz .Lat_rs8_%=\n"
    ".Lat_rsr8_%=:\n"
    "v_exp_f32 v48, v48\n"
    "v_exp_f32 v49, v49\n"
    "v_mfma_f32_32x32x16_bf16 a[32:47], v[200:203], v[116:119], a[32:47]\n"
    "ds_read_b128 a[216:219], v0 offset:6144\n"
    "v_exp_f32 v50, v50\n"
    "v_exp_f32 v51, v51\n"
    "v_add_f32 v10, v10, v48\n"
    "v_mfma_f32_32x32x16_bf16 a[48:63], v[204:207], v[116:119], a[48:63]\n"
    "ds_read_b128 a[220:223], v0 offset:7168\n"
    "v_add_f32 v11, v11, v49\n"
    "v_cvt_pk_bf16_f32 v128, v48, v49\n"
    "v_exp_f32 v52, v52\n"
    "s_waitcnt lgkmcnt(8)\n"
    "v_mfma_f32_32x32x16_bf16 a[0:15], v[208:211], v[120:123], a[0:15]\n"
    "ds_read_b128 a[224:227], v0 offset:8192\n"
    "v_exp_f32 v53, v53\n"
    "v_add_f32 v10, v10, v50\n"
    "v_add_f32 v11, v11, v51\n"
    "v_cvt_pk_bf16_f32 v129, v50, v51\n"
    "v_mfma_f32_32x32x16_bf16 a[16:31], v[212:215], v[120:123], a[16:31]\n"
    "ds_read_b128 a[228:231], v0 offset:9216\n"
    "v_exp_f32 v54, v54\n"
    "v_exp_f32 v55, v55\n"
    "v_add_f32 v10, v10, v52\n"
    "v_mfma_f32_32x32x16_bf16 a[32:47], v[216:219], v[120:123], a[32:47]\n"
    "ds_read_b128 a[232:235], v0 offset:10240\n"
    "v_add_f32 v11, v11, v53\n"
    "v_cvt_pk_bf16_f32 v130, v52, v53\n"
    "v_exp_f32 v56, v56\n"
    "v_mfma_f32_32x32x16_bf16 a[48:63], v[220:223], v[120:123], a[48:63]\n"
    "ds_read_b128 a[236:239], v0 offset:11264\n"
    "v_exp_f32 v57, v57\n"
    "v_add_f32 v10, v10, v54\n"
    "v_add_f32 v11, v11, v55\n"
    "v_cvt_pk_bf16_f32 v131, v54, v55\n"
    "v_mfma_f32_32x32x16_bf16 a[0:15], v[224:227], v[124:127], a[0:15]\n"
    "ds_read_b128 a[240:243], v0 offset:12288\n"
    "v_exp_f32 v58, v58\n"
    "v_exp_f32 v59, v59\n"
    "v_add_f32 v10, v10, v56\n"
    "v_mfma_f32_32x32x16_bf16 a[16:31], v[228:231], v[124:127], a[16:31]\n"
    "ds_read_b128 a[244:247], v0 offset:13312\n"
    "v_add_f32 v11, v11, v57\n"
    "v_cvt_pk_bf16_f32 v132, v56, v57\n"
    "v_exp_f32 v60, v60\n"
    "v_mfma_f32_32x32x16_bf16 a[32:47], v[232:235], v[124:127], a[32:47]\n"
    "ds_read_b128 a[248:251], v0 offset:14336\n"
    "v_exp_f32 v61, v61\n"
    "v_add_f32 v10, v10, v58\n"
    "v_add_f32 v11, v11, v59\n"
    "v_cvt_pk_bf16_f32 v133, v58, v59\n"
    "v_mfma_f32_32x32x16_bf16 a[48:63], v[236:239], v[124:127], a[48:63]\n"
    "ds_read_b128 a[252:255], v0 offset:15360\n"
    "v_exp_f32 v62, v62\n"
    "v_exp_f32 v63, v63\n"
    "v_add_f32 v10, v10, v60\n"
    "s_add_u32 s57, s57, 1\n"
    "s_cmp_gt_u32 s57, s59\n"
    "s_cbranch_scc1 .Lat_drain_%=\n"
    "s_waitcnt lgkmcnt(8)\n"
    "v_mfma_f32_32x32x16_bf16 v[16:31], a[192:195], a[128:131], v[80:95]\n"
    "v_add_f32 v11, v11, v61\n"
    "v_cvt_pk_bf16_f32 v134, v60, v61\n"
    "v_exp_f32 v64, v64\n"
    "v_exp_f32 v65, v65\n"
    "v_mfma_f32_32x32x16_bf16 v[16:31], a[196:199], a[132:135], v[16:31]\n"
    "v_add_f32 v10, v10, v62\n"
    "v_add_f32 v11, v11, v63\n"
    "v_cvt_pk_bf16_f32 v135, v62, v63\n"
    "v_exp_f32 v66, v66\n"
    "v_mfma_f32_32x32x16_bf16 v[16:31], a[200:203], a[136:139], v[16:31]\n"
    "v_exp_f32 v67, v67\n"
    "v_add_f32 v10, v10, v64\n"
    "v_add_f32 v11, v11, v65\n"
    "v_cvt_pk_bf16_f32 v136, v64, v65\n"
    "v_mfma_f32_32x32x16_bf16 v[16:31], a[204:207], a[140:143], v[16:31]\n"
    "v_exp_f32 v68, v68\n"
    "v_exp_f32 v69, v69\n"
    "v_add_f32 v10, v10, v66\n"
    "v_add_f32 v11, v11, v67\n"
    "v_mfma_f32_32x32x16_bf16 v[16:31], a[208:211], a[144:147], v[16:31]\n"
    "v_cvt_pk_bf16_f32 v137, v66, v67\n"
    "v_exp_f32 v70, v70\n"
    "v_exp_f32 v71, v71\n"
    "v_add_f32 v10, v10, v68\n"
    "v_mfma_f32_32x32x16_bf16 v[16:31], a[212:215], a[148:151], v[16:31]\n"
    "v_add_f32 v11, v11, v69\n"
    "v_cvt_pk_bf16_f32 v138, v68, v69\n"
    "v_exp_f32 v72, v72\n"
    "v_exp_f32 v73, v73\n"
    "v_mfma_f32_32x32x16_bf16 v[16:31], a[216:219], a[152:155], v[16:31]\n"
    "v_add_f32 v10, v10, v70\n"
    "v_add_f32 v11, v11, v71\n"
    "v_cvt_pk_bf16_f32 v139, v70, v71\n"
    "v_exp_f32 v74, v74\n"
    "v_mfma_f32_32x32x16_bf16 v[16:31], a[220:223], a[156:159], v[16:31]\n"
    "v_exp_f32 v75, v75\n"
    "v_add_f32 v10, v10, v72\n"
    "v_add_f32 v11, v11, v73\n"
    "v_cvt_pk_bf16_f32 v140, v72, v73\n"
    "s_waitcnt lgkmcnt(0)\n"
    "v_mfma_f32_32x32x16_bf16 v[32:47], a[224:227], a[128:131], v[80:95]\n"
    "v_exp_f32 v76, v76\n"
    "v_exp_f32 v77, v77\n"
    "v_add_f32 v10, v10, v74\n"
    "v_add_f32 v11, v11, v75\n"
    "v_mfma_f32_32x32x16_bf16 v[32:47], a[228:231], a[132:135], v[32:47]\n"
    "v_cvt_pk_bf16_f32 v141, v74, v75\n"
    "v_exp_f32 v78, v78\n"
    "v_exp_f32 v79, v79\n"
    "v_add_f32 v10, v10, v76\n"
    "v_mfma_f32_32x32x16_bf16 v[32:47], a[232:235], a[136:139], v[32:47]\n"
    "v_add_f32 v11, v11, v77\n"
    "v_cvt_pk_bf16_f32 v142, v76, v77\n"
    "v_add_f32 v10, v10, v78\n"
    "v_add_f32 v11, v11, v79\n"
    "v_cvt_pk_bf16_f32 v143, v78, v79\n"
    "v_mfma_f32_32x32x16_bf16 v[32:47], a[236:239], a[140:143], v[32:47]\n"
    "v_mfma_f32_32x32x16_bf16 v[32:47], a[240:243], a[144:147], v[32:47]\n"
    "s_add_u32 s63, s62, 0x1000000\n"
    "v_mfma_f32_32x32x16_bf16 v[32:47], a[244:247], a[148:151], v[32:47]\n"
    "s_add_u32 s56, s56, 1\n"
    "v_mfma_f32_32x32x16_bf16 v[32:47], a[248:251], a[152:155], v[32:47]\n"
    "s_add_u32 s62, s62, 0x4000\n"
    "v_mfma_f32_32x32x16_bf16 v[32:47], a[252:255], a[156:159], v[32:47]\n"
    "s_cmp_eq_u32 s56, s61\n"
    "s_cselect_b32 s62, s64, s62\n"
    "v_mfma_f32_32x32x16_bf16 a[64:79], v[176:179], v[128:131], a[64:79]\n"
    "s_cmp_eq_u32 s57, s59\n"
    "s_cbranch_scc1 .Lat_dg9_%=\n"
    ".Lat_dgr9_%=:\n"
    "v_max3_f32 v12, v16, v17, v18\n"
    "v_max3_f32 v13, v19, v20, v21\n"
    "v_max3_f32 v12, v12, v22, v23\n"
    "v_mfma_f32_32x32x16_bf16 a[80:95], v[180:183], v[128:131], a[80:95]\n"
    "v_max3_f32 v13, v13, v24, v25\n"
    "v_max3_f32 v12, v12, v26, v27\n"
    "v_max3_f32 v13, v13, v28, v29\n"
    "v_max3_f32 v12, v12, v30, v31\n"
    "v_max3_f32 v13, v13, v32, v33\n"
    "v_mfma_f32_32x32x16_bf16 a[96:111], v[184:187], v[128:131], a[96:111]\n"
    "v_max3_f32 v12, v12, v34, v35\n"
    "v_max3_f32 v13, v13, v36, v37\n"
    "v_max3_f32 v12, v12, v38, v39\n"
    "v_max3_f32 v13, v13, v40, v41\n"
    "v_max3_f32 v12, v12, v42, v43\n"
    "v_mfma_f32_32x32x16_bf16 a[112:127], v[188:191], v[128:131], a[112:127]\n"
    "v_max3_f32 v13, v13, v44, v45\n"
    "v_max3_f32 v12, v12, v46, v47\n"
    "v_max_f32 v12, v12, v13\n"
    "v_mov_b32 v13, v12\n"
    "s_nop 0\n"
    "v_mfma_f32_32x32x16_bf16 a[64:79], v[192:195], v[132:135], a[64:79]\n"
    "v_permlane32_swap_b32 v12, v13\n"
    "v_max_f32 v12, v12, v13\n"
    "v_cmp_lt_f32 vcc, 0x41f00000, v12\n"
    "v_mfma_f32_32x32x16_bf16 a[80:95], v[196:199], v[132:135], a[80:95]\n"
    "s_cbranch_vccnz .Lat_rs9_%=\n"
    ".Lat_rsr9_%=:\n"
    "v_exp_f32 v16, v16\n"
    "v_exp_f32 v17, v17\n"
    "v_mfma_f32_32x32x16_bf16 a[96:111], v[200:203], v[132:135], a[96:111]\n"
    "v_exp_f32 v18, v18\n"
    "v_exp_f32 v19, v19\n"
    "v_add_f32 v8, v8, v16\n"
    "v_add_f32 v9, v9, v17\n"
    "v_mfma_f32_32x32x16_bf16 a[112:127], v[204:207], v[132:135], a[112:127]\n"
    "v_cvt_pk_bf16_f32 v112, v16, v17\n"
    "v_exp_f32 v20, v20\n"
    "v_exp_f32 v21, v21\n"
    "v_add_f32 v8, v8, v18\n"
    "v_mfma_f32_32x32x16_bf16 a[64:79], v[208:211], v[136:139], a[64:79]\n"
    "v_add_f32 v9, v9, v19\n"
    "v_cvt_pk_bf16_f32 v113, v18, v19\n"
    "v_exp_f32 v22, v22\n"
    "v_exp_f32 v23, v23\n"
    "v_mfma_f32_32x32x16_bf16 a[80:95], v[212:215], v[136:139], a[80:95]\n"
    "v_add_f32 v8, v8, v20\n"
    "v_add_f32 v9, v9, v21\n"
    "v_cvt_pk_bf16_f32 v114, v20, v21\n"
    "v_exp_f32 v24, v24\n"
    "v_mfma_f32_32x32x16_bf16 a[96:111], v[216:219], v[136:139], a[96:111]\n"
    "v_exp_f32 v25, v25\n"
    "v_add_f32 v8, v8, v22\n"
    "v_add_f32 v9, v9, v23\n"
    "v_cvt_pk_bf16_f32 v115, v22, v23\n"
    "v_mfma_f32_32x32x16_bf16 a[112:127], v[220:223], v[136:139], a[112:127]\n"
    "v_exp_f32 v26, v26\n"
    "v_exp_f32 v27, v27\n"
    "v_add_f32 v8, v8, v24\n"
    "v_add_f32 v9, v9, v25\n"
    "v_mfma_f32_32x32x16_bf16 a[64:79], v[224:227], v[140:143], a[64:79]\n"
    "v_cvt_pk_bf16_f32 v116, v24, v25\n"
    "v_exp_f32 v28, v28\n"
    "v_exp_f32 v29, v29\n"
    "v_add_f32 v8, v8, v26\n"
    "v_mfma_f32_32x32x16_bf16 a[80:95], v[228:231], v[140:143], a[80:95]\n"
    "v_add_f32 v9, v9, v27\n"
    "v_cvt_pk_bf16_f32 v117, v26, v27\n"
    "v_exp_f32 v30, v30\n"
    "v_exp_f32 v31, v31\n"
    "v_mfma_f32_32x32x16_bf16 a[96:111], v[232:235], v[140:143], a[96:111]\n"
    "v_add_f32 v8, v8, v28\n"
    "v_add_f32 v9, v9, v29\n"
    "v_cvt_pk_bf16_f32 v118, v28, v29\n"
    "v_exp_f32 v32, v32\n"
    "v_mfma_f32_32x32x16_bf16 a[112:127], v[236:239], v[140:143], a[112:127]\n"
    "v_exp_f32 v33, v33\n"
    "v_add_f32 v8, v8, v30\n"
    "v_add_f32 v9, v9, v31\n"
    "v_cvt_pk_bf16_f32 v119, v30, v31\n"
    "s_waitcnt vmcnt(8)\n"
    "s_barrier\n"
    "v_mfma_f32_32x32x16_bf16 v[48:63], a[192:195], a[160:163], v[96:111]\n"
    "ds_read_b128 v[176:179], v3 offset:0\n"
    "v_exp_f32 v34, v34\n"
    "v_exp_f32 v35, v35\n"
    "v_add_f32 v8, v8, v32\n"
    "v_mfma_f32_32x32x16_bf16 v[48:63], a[196:199], a[164:167], v[48:63]\n"
    "ds_read_b128 v[180:183], v3 offset:2048\n"
    "s_add_u32 m0, s67, 49152\n"
    "s_nop 0\n"
    "buffer_load_dwordx4 v0, s[40:43], s62 offen offset:0 lds\n"
    "v_add_f32 v9, v9, v33\n"
    "v_cvt_pk_bf16_f32 v120, v32, v33\n"
    "v_mfma_f32_32x32x16_bf16 v[48:63], a[200:203], a[168:171], v[48:63]\n"
    "ds_read_b128 v[184:187], v3 offset:4096\n"
    "v_exp_f32 v36, v36\n"
    "v_exp_f32 v37, v37\n"
    "v_add_f32 v8, v8, v34\n"
    "v_mfma_f32_32x32x16_bf16 v[48:63], a[204:207], a[172:175], v[48:63]\n"
    "ds_read_b128 v[188:191], v3 offset:6144\n"
    "buffer_load_dwordx4 v0, s[40:43], s62 offen offset:1024 lds\n"
    "v_add_f32 v9, v9, v35\n"
    "v_cvt_pk_bf16_f32 v121, v34, v35\n"
    "v_exp_f32 v38, v38\n"
    "v_mfma_f32_32x32x16_bf16 v[48:63], a[208:211], a[176:179], v[48:63]\n"
    "ds_read_b128 v[192:195], v3 offset:1024\n"
    "v_exp_f32 v39, v39\n"
    "v_add_f32 v8, v8, v36\n"
    "v_add_f32 v9, v9, v37\n"
    "v_cvt_pk_bf16_f32 v122, v36, v37\n"
    "v_mfma_f32_32x32x16_bf16 v[48:63], a[212:215], a[180:183], v[48:63]\n"
    "ds_read_b128 v[196:199], v3 offset:3072\n"
    "buffer_load_dwordx4 v0, s[40:43], s62 offen offset:2048 lds\n"
    "v_exp_f32 v40, v40\n"
    "v_exp_f32 v41, v41\n"
    "v_mfma_f32_32x32x16_bf16 v[48:63], a[216:219], a[184:187], v[48:63]\n"
    "ds_read_b128 v[200:203], v3 offset:5120\n"
    "v_add_f32 v8, v8, v38\n"
    "v_add_f32 v9, v9, v39\n"
    "v_cvt_pk_bf16_f32 v123, v38, v39\n"
    "v_exp_f32 v42, v42\n"
    "v_mfma_f32_32x32x16_bf16 v[48:63], a[220:223], a[188:191], v[48:63]\n"
    "ds_read_b128 v[204:207], v3 offset:7168\n"
    "buffer_load_dwordx4 v0, s[40:43], s62 offen offset:3072 lds\n"
    "v_exp_f32 v43, v43\n"
    "v_add_f32 v8, v8, v40\n"
    "v_add_f32 v9, v9, v41\n"
    "v_mfma_f32_32x32x16_bf16 v[64:79], a[224:227], a[160:163], v[96:111]\n"
    "ds_read_b128 v[208:211], v3 offset:8192\n"
    "v_cvt_pk_bf16_f32 v124, v40, v41\n"
    "v_exp_f32 v44, v44\n"
    "v_exp_f32 v45, v45\n"
    "v_mfma_f32_32x32x16_bf16 v[64:79], a[228:231], a[164:167], v[64:79]\n"
    "ds_read_b128 v[212:215], v3 offset:10240\n"
    "s_add_u32 m0, s68, 32768\n"
    "s_nop 0\n"
    "buffer_load_dwordx4 v0, s[40:43], s63 offen offset:0 lds\n"
    "v_add_f32 v8, v8, v42\n"
    "v_add_f32 v9, v9, v43\n"
    "v_mfma_f32_32x32x16_bf16 v[64:79], a[232:235], a[168:171], v[64:79]\n"
    "ds_read_b128 v[216:219], v3 offset:12288\n"
    "v_cvt_pk_bf16_f32 v125, v42, v43\n"
    "v_exp_f32 v46, v46\n"
    "v_exp_f32 v47, v47\n"
    "v_mfma_f32_32x32x16_bf16 v[64:79], a[236:239], a[172:175], v[64:79]\n"
    "ds_read_b128 v[220:223], v3 offset:14336\n"
    "buffer_load_dwordx4 v0, s[40:43], s63 offen offset:1024 lds\n"
    "v_add_f32 v8, v8, v44\n"
    "v_add_f32 v9, v9, v45\n"
    "v_cvt_pk_bf16_f32 v126, v44, v45\n"
    "v_add_f32 v8, v8, v46\n"
    "v_mfma_f32_32x32x16_bf16 v[64:79], a[240:243], a[176:179], v[64:79]\n"
    "ds_read_b128 v[224:227], v3 offset:9216\n"
    "v_add_f32 v9, v9, v47\n"
    "v_cvt_pk_bf16_f32 v127, v46, v47\n"
    "v_mfma_f32_32x32x16_bf16 v[64:79], a[244:247], a[180:183], v[64:79]\n"
    "ds_read_b128 v[228:231], v3 offset:11264\n"
    "buffer_load_dwordx4 v0, s[40:43], s63 offen offset:2048 lds\n"
    "v_mfma_f32_32x32x16_bf16 v[64:79], a[248:251], a[184:187], v[64:79]\n"
    "ds_read_b128 v[232:235], v3 offset:13312\n"
    "v_mfma_f32_32x32x16_bf16 v[64:79], a[252:255], a[188:191], v[64:79]\n"
    "ds_read_b128 v[236:239], v3 offset:15360\n"
    "buffer_load_dwordx4 v0, s[40:43], s63 offen offset:3072 lds\n"
    "s_waitcnt lgkmcnt(8)\n"
    "v_mfma_f32_32x32x16_bf16 a[0:15], v[176:179], v[112:115], a[0:15]\n"
    "ds_read_b128 a[192:195], v0 offset:16384\n"
    "s_cmp_eq_u32 s57, s59\n"
    "s_cbranch_scc1 .Lat_dg10_%=\n"
    ".Lat_dgr10_%=:\n"
    "v_max3_f32 v14, v48, v49, v50\n"
    "v_max3_f32 v15, v51, v52, v53\n"
    "v_max3_f32 v14, v14, v54, v55\n"
    "v_mfma_f32_32x32x16_bf16 a[16:31], v[180:183], v[112:115], a[16:31]\n"
    "ds_read_b128 a[196:199], v0 offset:17408\n"
    "v_max3_f32 v15, v15, v56, v57\n"
    "v_max3_f32 v14, v14, v58, v59\n"
    "v_max3_f32 v15, v15, v60, v61\n"
    "v_max3_f32 v14, v14, v62, v63\n"
    "v_max3_f32 v15, v15, v64, v65\n"
    "v_mfma_f32_32x32x16_bf16 a[32:47], v[184:187], v[112:115], a[32:47]\n"
    "ds_read_b128 a[200:203], v0 offset:18432\n"
    "v_max3_f32 v14, v14, v66, v67\n"
    "v_max3_f32 v15, v15, v68, v69\n"
    "v_max3_f32 v14, v14, v70, v71\n"
    "v_max3_f32 v15, v15, v72, v73\n"
    "v_max3_f32 v14, v14, v74, v75\n"
    "v_mfma_f32_32x32x16_bf16 a[48:63], v[188:191], v[112:115], a[48:63]\n"
    "ds_read_b128 a[204:207], v0 offset:19456\n"
    "v_max3_f32 v15, v15, v76, v77\n"
    "v_max3_f32 v14, v14, v78, v79\n"
    "v_max_f32 v14, v14, v15\n"
    "v_mov_b32 v15, v14\n"
    "s_nop 0\n"
    "v_mfma_f32_32x32x16_bf16 a[0:15], v[192:195], v[116:119], a[0:15]\n"
    "ds_read_b128 a[208:211], v0 offset:20480\n"
    "v_permlane32_swap_b32 v14, v15\n"
    "v_max_f32 v14, v14, v15\n"
    "v_cmp_lt_f32 vcc, 0x41f00000, v14\n"
    "v_mfma_f32_32x32x16_bf16 a[16:31], v[196:199], v[116:119], a[16:31]\n"
    "ds_read_b128 a[212:215], v0 offset:21504\n"
    "s_cbranch_vccnz .Lat_rs10_%=\n"
    ".Lat_rsr10_%=:\n"
    "v_exp_f32 v48, v48\n"
    "v_exp_f32 v49, v49\n"
    "v_mfma_f32_32x32x16_bf16 a[32:47], v[200:203], v[116:119], a[32:47]\n"
    "ds_read_b128 a[216:219], v0 offset:22528\n"
    "v_exp_f32 v50, v50\n"
    "v_exp_f32 v51, v51\n"
    "v_add_f32 v10, v10, v48\n"
    "v_mfma_f32_32x32x16_bf16 a[48:63], v[204:207], v[116:119], a[48:63]\n"
    "ds_read_b128 a[220:223], v0 offset:23552\n"
    "v_add_f32 v11, v11, v49\n"
    "v_cvt_pk_bf16_f32 v128, v48, v49\n"
    "v_exp_f32 v52, v52\n"
    "s_waitcnt lgkmcnt(8)\n"
    "v_mfma_f32_32x32x16_bf16 a[0:15], v[208:211], v[120:123], a[0:15]\n"
    "ds_read_b128 a[224:227], v0 offset:24576\n"
    "v_exp_f32 v53, v53\n"
    "v_add_f32 v10, v10, v50\n"
    "v_add_f32 v11, v11, v51\n"
    "v_cvt_pk_bf16_f32 v129, v50, v51\n"
    "v_mfma_f32_32x32x16_bf16 a[16:31], v[212:215], v[120:123], a[16:31]\n"
    "ds_read_b128 a[228:231], v0 offset:25600\n"
    "v_exp_f32 v54, v54\n"
    "v_exp_f32 v55, v55\n"
    "v_add_f32 v10, v10, v52\n"
    "v_mfma_f32_32x32x16_bf16 a[32:47], v[216:219], v[120:123], a[32:47]\n"
    "ds_read_b128 a[232:235], v0 offset:26624\n"
    "v_add_f32 v11, v11, v53\n"
    "v_cvt_pk_bf16_f32 v130, v52, v53\n"
    "v_exp_f32 v56, v56\n"
    "v_mfma_f32_32x32x16_bf16 a[48:63], v[220:223], v[120:123], a[48:63]\n"
    "ds_read_b128 a[236:239], v0 offset:27648\n"
    "v_exp_f32 v57, v57\n"
    "v_add_f32 v10, v10, v54\n"
    "v_add_f32 v11, v11, v55\n"
    "v_cvt_pk_bf16_f32 v131, v54, v55\n"
    "v_mfma_f32_32x32x16_bf16 a[0:15], v[224:227], v[124:127], a[0:15]\n"
    "ds_read_b128 a[240:243], v0 offset:28672\n"
    "v_exp_f32 v58, v58\n"
    "v_exp_f32 v59, v59\n"
    "v_add_f32 v10, v10, v56\n"
    "v_mfma_f32_32x32x16_bf16 a[16:31], v[228:231], v[124:127], a[16:31]\n"
    "ds_read_b128 a[244:247], v0 offset:29696\n"
    "v_add_f32 v11, v11, v57\n"
    "v_cvt_pk_bf16_f32 v132, v56, v57\n"
    "v_exp_f32 v60, v60\n"
    "v_mfma_f32_32x32x16_bf16 a[32:47], v[232:235], v[124:127], a[32:47]\n"
    "ds_read_b128 a[248:251], v0 offset:30720\n"
    "v_exp_f32 v61, v61\n"
    "v_add_f32 v10, v10, v58\n"
    "v_add_f32 v11, v11, v59\n"
    "v_cvt_pk_bf16_f32 v133, v58, v59\n"
    "v_mfma_f32_32x32x16_bf16 a[48:63], v[236:239], v[124:127], a[48:63]\n"
    "ds_read_b128 a[252:255], v0 offset:31744\n"
    "v_exp_f32 v62, v62\n"
    "v_exp_f32 v63, v63\n"
    "v_add_f32 v10, v10, v60\n"
    "s_branch .Lat_loop_%=\n"
    ".Lat_drain_%=:\n"
    "v_add_f32 v11, v11, v61\n"
    "v_cvt_pk_bf16_f32 v134, v60, v61\n"
    "v_exp_f32 v64, v64\n"
    "v_exp_f32 v65, v65\n"
    "v_add_f32 v10, v10, v62\n"
    "v_add_f32 v11, v11, v63\n"
    "v_cvt_pk_bf16_f32 v135, v62, v63\n"
    "v_exp_f32 v66, v66\n"
    "v_exp_f32 v67, v67\n"
    "v_add_f32 v10, v10, v64\n"
    "v_add_f32 v11, v11, v65\n"
    "v_cvt_pk_bf16_f32 v136, v64, v65\n"
    "v_exp_f32 v68, v68\n"
    "v_exp_f32 v69, v69\n"
    "v_add_f32 v10, v10, v66\n"
    "v_add_f32 v11, v11, v67\n"
    "v_cvt_pk_bf16_f32 v137, v66, v67\n"
    "v_exp_f32 v70, v70\n"
    "v_exp_f32 v71, v71\n"
    "v_add_f32 v10, v10, v68\n"
    "v_add_f32 v11, v11, v69\n"
    "v_cvt_pk_bf16_f32 v138, v68, v69\n"
    "v_exp_f32 v72, v72\n"
    "v_exp_f32 v73, v73\n"
    "v_add_f32 v10, v10, v70\n"
    "v_add_f32 v11, v11, v71\n"
    "v_cvt_pk_bf16_f32 v139, v70, v71\n"
    "v_exp_f32 v74, v74\n"
    "v_exp_f32 v75, v75\n"
    "v_add_f32 v10, v10, v72\n"
    "v_add_f32 v11, v11, v73\n"
    "v_cvt_pk_bf16_f32 v140, v72, v73\n"
    "v_exp_f32 v76, v76\n"
    "v_exp_f32 v77, v77\n"
    "v_add_f32 v10, v10, v74\n"
    "v_add_f32 v11, v11, v75\n"
    "v_cvt_pk_bf16_f32 v141, v74, v75\n"
    "v_exp_f32 v78, v78\n"
    "v_exp_f32 v79, v79\n"
    "v_add_f32 v10, v10, v76\n"
    "v_add_f32 v11, v11, v77\n"
    "v_cvt_pk_bf16_f32 v142, v76, v77\n"
    "v_add_f32 v10, v10, v78\n"
    "v_add_f32 v11, v11, v79\n"
    "v_cvt_pk_bf16_f32 v143, v78, v79\n"
    "s_nop 1\n"
    "v_mfma_f32_32x32x16_bf16 a[64:79], v[176:179], v[128:131], a[64:79]\n"
    "v_mfma_f32_32x32x16_bf16 a[80:95], v[180:183], v[128:131], a[80:95]\n"
    "v_mfma_f32_32x32x16_bf16 a[96:111], v[184:187], v[128:131], a[96:111]\n"
    "v_mfma_f32_32x32x16_bf16 a[112:127], v[188:191], v[128:131], a[112:127]\n"
    "v_mfma_f32_32x32x16_bf16 a[64:79], v[192:195], v[132:135], a[64:79]\n"
    "v_mfma_f32_32x32x16_bf16 a[80:95], v[196:199], v[132:135], a[80:95]\n"
    "v_mfma_f32_32x32x16_bf16 a[96:111], v[200:203], v[132:135], a[96:111]\n"
    "v_mfma_f32_32x32x16_bf16 a[112:127], v[204:207], v[132:135], a[112:127]\n"
    "v_mfma_f32_32x32x16_bf16 a[64:79], v[208:211], v[136:139], a[64:79]\n"
    "v_mfma_f32_32x32x16_bf16 a[80:95], v[212:215], v[136:139], a[80:95]\n"
    "v_mfma_f32_32x32x16_bf16 a[96:111], v[216:219], v[136:139], a[96:111]\n"
    "v_mfma_f32_32x32x16_bf16 a[112:127], v[220:223], v[136:139], a[112:127]\n"
    "v_mfma_f32_32x32x16_bf16 a[64:79], v[224:227], v[140:143], a[64:79]\n"
    "v_mfma_f32_32x32x16_bf16 a[80:95], v[228:231], v[140:143], a[80:95]\n"
    "v_mfma_f32_32x32x16_bf16 a[96:111], v[232:235], v[140:143], a[96:111]\n"
    "v_mfma_f32_32x32x16_bf16 a[112:127], v[236:239], v[140:143], a[112:127]\n"
    "s_cmp_ge_u32 s57, s58\n"
    "s_cbranch_scc1 .Lat_epi_%=\n"
    "s_add_u32 s63, s62, 0x1000000\n"
    "s_add_u32 s56, s56, 1\n"
    "s_add_u32 s62, s62, 0x4000\n"
    "s_cmp_eq_u32 s56, s61\n"
    "s_cselect_b32 s62, s64, s62\n"
    "s_waitcnt vmcnt(8)\n"
    "s_barrier\n"
    "s_and_b32 s65, s56, 3\n"
    "s_lshl_b32 s65, s65, 14\n"
    "s_add_u32 m0, s67, s65\n"
    "s_nop 0\n"
    "buffer_load_dwordx4 v0, s[40:43], s62 offen offset:0 lds\n"
    "buffer_load_dwordx4 v0, s[40:43], s62 offen offset:1024 lds\n"
    "buffer_load_dwordx4 v0, s[40:43], s62 offen offset:2048 lds\n"
    "buffer_load_dwordx4 v0, s[40:43], s62 offen offset:3072 lds\n"
    "s_add_u32 s65, s65, 0xc000\n"
    "s_and_b32 s65, s65, 0xffff\n"
    "s_add_u32 m0, s68, s65\n"
    "s_nop 0\n"
    "buffer_load_dwordx4 v0, s[40:43], s63 offen offset:0 lds\n"
    "buffer_load_dwordx4 v0, s[40:43], s63 offen offset:1024 lds\n"
    "buffer_load_dwordx4 v0, s[40:43], s63 offen offset:2048 lds\n"
    "buffer_load_dwordx4 v0, s[40:43], s63 offen offset:3072 lds\n"
    ".Lat_epi_%=:\n"
    "s_lshl_b32 s70, s60, 4\n"
    "s_lshl_b32 s66, s36, 2\n"
    "s_add_u32 s70, s70, s66\n"
    "s_lshl_b32 s70, s70, 17\n"
    "s_add_u32 s70, s70, 0x7800000\n"
    "s_lshl_b32 s71, s60, 9\n"
    "s_add_u32 s71, s71, s66\n"
    "s_lshl_b32 s66, s37, 4\n"
    "s_add_u32 s71, s71, s66\n"
    "s_add_u32 s71, s71, 24576\n"
    "s_mov_b32 s60, s38\n"
    "s_lshl_b32 s65, s60, 3\n"
    "s_lshl_b32 s66, s36, 1\n"
    "s_add_u32 s65, s65, s66\n"
    "s_lshl_b32 s65, s65, 13\n"
    "s_lshl_b32 s66, s37, 19\n"
    "s_add_u32 s65, s65, s66\n"
    "s_add_u32 s65, s65, 0x3800000\n"
    "buffer_load_dwordx4 a[128:131], v0, s[40:43], s65 offen offset:0\n"
    "buffer_load_dwordx4 a[132:135], v0, s[40:43], s65 offen offset:1024\n"
    "buffer_load_dwordx4 a[136:139], v0, s[40:43], s65 offen offset:2048\n"
    "buffer_load_dwordx4 a[140:143], v0, s[40:43], s65 offen offset:3072\n"
    "s_add_u32 s65, s65, 0x1000\n"
    "buffer_load_dwordx4 a[144:147], v0, s[40:43], s65 offen offset:0\n"
    "buffer_load_dwordx4 a[148:151], v0, s[40:43], s65 offen offset:1024\n"
    "buffer_load_dwordx4 a[152:155], v0, s[40:43], s65 offen offset:2048\n"
    "buffer_load_dwordx4 a[156:159], v0, s[40:43], s65 offen offset:3072\n"
    "s_add_u32 s65, s65, 0x1000\n"
    "buffer_load_dwordx4 a[160:163], v0, s[40:43], s65 offen offset:0\n"
    "buffer_load_dwordx4 a[164:167], v0, s[40:43], s65 offen offset:1024\n"
    "buffer_load_dwordx4 a[168:171], v0, s[40:43], s65 offen offset:2048\n"
    "buffer_load_dwordx4 a[172:175], v0, s[40:43], s65 offen offset:3072\n"
    "s_add_u32 s65, s65, 0x1000\n"
    "buffer_load_dwordx4 a[176:179], v0, s[40:43], s65 offen offset:0\n"
    "buffer_load_dwordx4 a[180:183], v0, s[40:43], s65 offen offset:1024\n"
    "buffer_load_dwordx4 a[184:187], v0, s[40:43], s65 offen offset:2048\n"
    "buffer_load_dwordx4 a[188:191], v0, s[40:43], s65 offen offset:3072\n"
    "v_add_f32 v8, v8, v9\n"
    "v_mov_b32 v9, v8\n"
    "s_nop 1\n"
    "v_permlane32_swap_b32 v8, v9\n"
    "v_add_f32 v8, v8, v9\n"
    "v_rcp_f32 v8, v8\n"
    "v_accvgpr_read_b32 v16, a0\n"
    "v_accvgpr_read_b32 v17, a1\n"
    "v_accvgpr_read_b32 v18, a2\n"
    "v_accvgpr_read_b32 v19, a3\n"
    "v_accvgpr_read_b32 v20, a4\n"
    "v_accvgpr_read_b32 v21, a5\n"
    "v_accvgpr_read_b32 v22, a6\n"
    "v_accvgpr_read_b32 v23, a7\n"
    "v_pk_mul_f32 v[16:17], v[16:17], v[8:9] op_sel_hi:[1,0]\n"
    "v_pk_mul_f32 v[18:19], v[18:19], v[8:9] op_sel_hi:[1,0]\n"
    "v_pk_mul_f32 v[20:21], v[20:21], v[8:9] op_sel_hi:[1,0]\n"
    "v_pk_mul_f32 v[22:23], v[22:23], v[8:9] op_sel_hi:[1,0]\n"
    "v_cvt_pk_bf16_f32 v16, v16, v17\n"
    "v_cvt_pk_bf16_f32 v17, v18, v19\n"
    "v_cvt_pk_bf16_f32 v18, v20, v21\n"
    "v_cvt_pk_bf16_f32 v19, v22, v23\n"
    "buffer_store_dwordx4 v[16:19], v2, s[40:43], s70 offen offset:0 sc1\n"
    "v_accvgpr_read_b32 v24, a8\n"
    "v_accvgpr_read_b32 v25, a9\n"
    "v_accvgpr_read_b32 v26, a10\n"
    "v_accvgpr_read_b32 v27, a11\n"
    "v_accvgpr_read_b32 v28, a12\n"
    "v_accvgpr_read_b32 v29, a13\n"
    "v_accvgpr_read_b32 v30, a14\n"
    "v_accvgpr_read_b32 v31, a15\n"
    "v_pk_mul_f32 v[24:25], v[24:25], v[8:9] op_sel_hi:[1,0]\n"
    "v_pk_mul_f32 v[26:27], v[26:27], v[8:9] op_sel_hi:[1,0]\n"
    "v_pk_mul_f32 v[28:29], v[28:29], v[8:9] op_sel_hi:[1,0]\n"
    "v_pk_mul_f32 v[30:31], v[30:31], v[8:9] op_sel_hi:[1,0]\n"
    "v_cvt_pk_bf16_f32 v24, v24, v25\n"
    "v_cvt_pk_bf16_f32 v25, v26, v27\n"
    "v_cvt_pk_bf16_f32 v26, v28, v29\n"
    "v_cvt_pk_bf16_f32 v27, v30, v31\n"
    "buffer_store_dwordx4 v[24:27], v2, s[40:43], s70 offen offset:512 sc1\n"
    "v_accvgpr_read_b32 v32, a16\n"
    "v_accvgpr_read_b32 v33, a17\n"
    "v_accvgpr_read_b32 v34, a18\n"
    "v_accvgpr_read_b32 v35, a19\n"
    "v_accvgpr_read_b32 v36, a20\n"
    "v_accvgpr_read_b32 v37, a21\n"
    "v_accvgpr_read_b32 v38, a22\n"
    "v_accvgpr_read_b32 v39, a23\n"
    "v_pk_mul_f32 v[32:33], v[32:33], v[8:9] op_sel_hi:[1,0]\n"
    "v_pk_mul_f32 v[34:35], v[34:35], v[8:9] op_sel_hi:[1,0]\n"
    "v_pk_mul_f32 v[36:37], v[36:37], v[8:9] op_sel_hi:[1,0]\n"
    "v_pk_mul_f32 v[38:39], v[38:39], v[8:9] op_sel_hi:[1,0]\n"
    "v_cvt_pk_bf16_f32 v32, v32, v33\n"
    "v_cvt_pk_bf16_f32 v33, v34, v35\n"
    "v_cvt_pk_bf16_f32 v34, v36, v37\n"
    "v_cvt_pk_bf16_f32 v35, v38, v39\n"
    "buffer_store_dwordx4 v[32:35], v2, s[40:43], s70 offen offset:1024 sc1\n"
    "v_accvgpr_read_b32 v40, a24\n"
    "v_accvgpr_read_b32 v41, a25\n"
    "v_accvgpr_read_b32 v42, a26\n"
    "v_accvgpr_read_b32 v43, a27\n"
    "v_accvgpr_read_b32 v44, a28\n"
    "v_accvgpr_read_b32 v45, a29\n"
    "v_accvgpr_read_b32 v46, a30\n"
    "v_accvgpr_read_b32 v47, a31\n"
    "v_pk_mul_f32 v[40:41], v[40:41], v[8:9] op_sel_hi:[1,0]\n"
    "v_pk_mul_f32 v[42:43], v[42:43], v[8:9] op_sel_hi:[1,0]\n"
    "v_pk_mul_f32 v[44:45], v[44:45], v[8:9] op_sel_hi:[1,0]\n"
    "v_pk_mul_f32 v[46:47], v[46:47], v[8:9] op_sel_hi:[1,0]\n"
    "v_cvt_pk_bf16_f32 v40, v40, v41\n"
    "v_cvt_pk_bf16_f32 v41, v42, v43\n"
    "v_cvt_pk_bf16_f32 v42, v44, v45\n"
    "v_cvt_pk_bf16_f32 v43, v46, v47\n"
    "buffer_store_dwordx4 v[40:43], v2, s[40:43], s70 offen offset:1536 sc1\n"
    "v_accvgpr_read_b32 v16, a32\n"
    "v_accvgpr_read_b32 v17, a33\n"
    "v_accvgpr_read_b32 v18, a34\n"
    "v_accvgpr_read_b32 v19, a35\n"
    "v_accvgpr_read_b32 v20, a36\n"
    "v_accvgpr_read_b32 v21, a37\n"
    "v_accvgpr_read_b32 v22, a38\n"
    "v_accvgpr_read_b32 v23, a39\n"
    "v_pk_mul_f32 v[16:17], v[16:17], v[8:9] op_sel_hi:[1,0]\n"
    "v_pk_mul_f32 v[18:19], v[18:19], v[8:9] op_sel_hi:[1,0]\n"
    "v_pk_mul_f32 v[20:21], v[20:21], v[8:9] op_sel_hi:[1,0]\n"
    "v_pk_mul_f32 v[22:23], v[22:23], v[8:9] op_sel_hi:[1,0]\n"
    "v_cvt_pk_bf16_f32 v16, v16, v17\n"
    "v_cvt_pk_bf16_f32 v17, v18, v19\n"
    "v_cvt_pk_bf16_f32 v18, v20, v21\n"
    "v_cvt_pk_bf16_f32 v19, v22, v23\n"
    "buffer_store_dwordx4 v[16:19], v2, s[40:43], s70 offen offset:2048 sc1\n"
    "v_accvgpr_read_b32 v24, a40\n"
    "v_accvgpr_read_b32 v25, a41\n"
    "v_accvgpr_read_b32 v26, a42\n"
    "v_accvgpr_read_b32 v27, a43\n"
    "v_accvgpr_read_b32 v28, a44\n"
    "v_accvgpr_read_b32 v29, a45\n"
    "v_accvgpr_read_b32 v30, a46\n"
    "v_accvgpr_read_b32 v31, a47\n"
    "v_pk_mul_f32 v[24:25], v[24:25], v[8:9] op_sel_hi:[1,0]\n"
    "v_pk_mul_f32 v[26:27], v[26:27], v[8:9] op_sel_hi:[1,0]\n"
    "v_pk_mul_f32 v[28:29], v[28:29], v[8:9] op_sel_hi:[1,0]\n"
    "v_pk_mul_f32 v[30:31], v[30:31], v[8:9] op_sel_hi:[1,0]\n"
    "v_cvt_pk_bf16_f32 v24, v24, v25\n"
    "v_cvt_pk_bf16_f32 v25, v26, v27\n"
    "v_cvt_pk_bf16_f32 v26, v28, v29\n"
    "v_cvt_pk_bf16_f32 v27, v30, v31\n"
    "buffer_store_dwordx4 v[24:27], v2, s[40:43], s70 offen offset:2560 sc1\n"
    "v_accvgpr_read_b32 v32, a48\n"
    "v_accvgpr_read_b32 v33, a49\n"
    "v_accvgpr_read_b32 v34, a50\n"
    "v_accvgpr_read_b32 v35, a51\n"
    "v_accvgpr_read_b32 v36, a52\n"
    "v_accvgpr_read_b32 v37, a53\n"
    "v_accvgpr_read_b32 v38, a54\n"
    "v_accvgpr_read_b32 v39, a55\n"
    "v_pk_mul_f32 v[32:33], v[32:33], v[8:9] op_sel_hi:[1,0]\n"
    "v_pk_mul_f32 v[34:35], v[34:35], v[8:9] op_sel_hi:[1,0]\n"
    "v_pk_mul_f32 v[36:37], v[36:37], v[8:9] op_sel_hi:[1,0]\n"
    "v_pk_mul_f32 v[38:39], v[38:39], v[8:9] op_sel_hi:[1,0]\n"
    "v_cvt_pk_bf16_f32 v32, v32, v33\n"
    "v_cvt_pk_bf16_f32 v33, v34, v35\n"
    "v_cvt_pk_bf16_f32 v34, v36, v37\n"
    "v_cvt_pk_bf16_f32 v35, v38, v39\n"
    "buffer_store_dwordx4 v[32:35], v2, s[40:43], s70 offen offset:3072 sc1\n"
    "v_accvgpr_read_b32 v40, a56\n"
    "v_accvgpr_read_b32 v41, a57\n"
    "v_accvgpr_read_b32 v42, a58\n"
    "v_accvgpr_read_b32 v43, a59\n"
    "v_accvgpr_read_b32 v44, a60\n"
    "v_accvgpr_read_b32 v45, a61\n"
    "v_accvgpr_read_b32 v46, a62\n"
    "v_accvgpr_read_b32 v47, a63\n"
    "v_pk_mul_f32 v[40:41], v[40:41], v[8:9] op_sel_hi:[1,0]\n"
    "v_pk_mul_f32 v[42:43], v[42:43], v[8:9] op_sel_hi:[1,0]\n"
    "v_pk_mul_f32 v[44:45], v[44:45], v[8:9] op_sel_hi:[1,0]\n"
    "v_pk_mul_f32 v[46:47], v[46:47], v[8:9] op_sel_hi:[1,0]\n"
    "v_cvt_pk_bf16_f32 v40, v40, v41\n"
    "v_cvt_pk_bf16_f32 v41, v42, v43\n"
    "v_cvt_pk_bf16_f32 v42, v44, v45\n"
    "v_cvt_pk_bf16_f32 v43, v46, v47\n"
    "buffer_store_dwordx4 v[40:43], v2, s[40:43], s70 offen offset:3584 sc1\n"
    "v_add_f32 v10, v10, v11\n"
    "v_mov_b32 v11, v10\n"
    "s_nop 1\n"
    "v_permlane32_swap_b32 v10, v11\n"
    "v_add_f32 v10, v10, v11\n"
    "v_rcp_f32 v10, v10\n"
    "s_add_u32 s70, s70, 0x40000\n"
    "v_accvgpr_read_b32 v16, a64\n"
    "v_accvgpr_read_b32 v17, a65\n"
    "v_accvgpr_read_b32 v18, a66\n"
    "v_accvgpr_read_b32 v19, a67\n"
    "v_accvgpr_read_b32 v20, a68\n"
    "v_accvgpr_read_b32 v21, a69\n"
    "v_accvgpr_read_b32 v22, a70\n"
    "v_accvgpr_read_b32 v23, a71\n"
    "v_pk_mul_f32 v[16:17], v[16:17], v[10:11] op_sel_hi:[1,0]\n"
    "v_pk_mul_f32 v[18:19], v[18:19], v[10:11] op_sel_hi:[1,0]\n"
    "v_pk_mul_f32 v[20:21], v[20:21], v[10:11] op_sel_hi:[1,0]\n"
    "v_pk_mul_f32 v[22:23], v[22:23], v[10:11] op_sel_hi:[1,0]\n"
    "v_cvt_pk_bf16_f32 v16, v16, v17\n"
    "v_cvt_pk_bf16_f32 v17, v18, v19\n"
    "v_cvt_pk_bf16_f32 v18, v20, v21\n"
    "v_cvt_pk_bf16_f32 v19, v22, v23\n"
    "buffer_store_dwordx4 v[16:19], v2, s[40:43], s70 offen offset:0 sc1\n"
    "v_accvgpr_read_b32 v24, a72\n"
    "v_accvgpr_read_b32 v25, a73\n"
    "v_accvgpr_read_b32 v26, a74\n"
    "v_accvgpr_read_b32 v27, a75\n"
    "v_accvgpr_read_b32 v28, a76\n"
    "v_accvgpr_read_b32 v29, a77\n"
    "v_accvgpr_read_b32 v30, a78\n"
    "v_accvgpr_read_b32 v31, a79\n"
    "v_pk_mul_f32 v[24:25], v[24:25], v[10:11] op_sel_hi:[1,0]\n"
    "v_pk_mul_f32 v[26:27], v[26:27], v[10:11] op_sel_hi:[1,0]\n"
    "v_pk_mul_f32 v[28:29], v[28:29], v[10:11] op_sel_hi:[1,0]\n"
    "v_pk_mul_f32 v[30:31], v[30:31], v[10:11] op_sel_hi:[1,0]\n"
    "v_cvt_pk_bf16_f32 v24, v24, v25\n"
    "v_cvt_pk_bf16_f32 v25, v26, v27\n"
    "v_cvt_pk_bf16_f32 v26, v28, v29\n"
    "v_cvt_pk_bf16_f32 v27, v30, v31\n"
    "buffer_store_dwordx4 v[24:27], v2, s[40:43], s70 offen offset:512 sc1\n"
    "v_accvgpr_read_b32 v32, a80\n"
    "v_accvgpr_read_b32 v33, a81\n"
    "v_accvgpr_read_b32 v34, a82\n"
    "v_accvgpr_read_b32 v35, a83\n"
    "v_accvgpr_read_b32 v36, a84\n"
    "v_accvgpr_read_b32 v37, a85\n"
    "v_accvgpr_read_b32 v38, a86\n"
    "v_accvgpr_read_b32 v39, a87\n"
    "v_pk_mul_f32 v[32:33], v[32:33], v[10:11] op_sel_hi:[1,0]\n"
    "v_pk_mul_f32 v[34:35], v[34:35], v[10:11] op_sel_hi:[1,0]\n"
    "v_pk_mul_f32 v[36:37], v[36:37], v[10:11] op_sel_hi:[1,0]\n"
    "v_pk_mul_f32 v[38:39], v[38:39], v[10:11] op_sel_hi:[1,0]\n"
    "v_cvt_pk_bf16_f32 v32, v32, v33\n"
    "v_cvt_pk_bf16_f32 v33, v34, v35\n"
    "v_cvt_pk_bf16_f32 v34, v36, v37\n"
    "v_cvt_pk_bf16_f32 v35, v38, v39\n"
    "buffer_store_dwordx4 v[32:35], v2, s[40:43], s70 offen offset:1024 sc1\n"
    "v_accvgpr_read_b32 v40, a88\n"
    "v_accvgpr_read_b32 v41, a89\n"
    "v_accvgpr_read_b32 v42, a90\n"
    "v_accvgpr_read_b32 v43, a91\n"
    "v_accvgpr_read_b32 v44, a92\n"
    "v_accvgpr_read_b32 v45, a93\n"
    "v_accvgpr_read_b32 v46, a94\n"
    "v_accvgpr_read_b32 v47, a95\n"
    "v_pk_mul_f32 v[40:41], v[40:41], v[10:11] op_sel_hi:[1,0]\n"
    "v_pk_mul_f32 v[42:43], v[42:43], v[10:11] op_sel_hi:[1,0]\n"
    "v_pk_mul_f32 v[44:45], v[44:45], v[10:11] op_sel_hi:[1,0]\n"
    "v_pk_mul_f32 v[46:47], v[46:47], v[10:11] op_sel_hi:[1,0]\n"
    "v_cvt_pk_bf16_f32 v40, v40, v41\n"
    "v_cvt_pk_bf16_f32 v41, v42, v43\n"
    "v_cvt_pk_bf16_f32 v42, v44, v45\n"
    "v_cvt_pk_bf16_f32 v43, v46, v47\n"
    "buffer_store_dwordx4 v[40:43], v2, s[40:43], s70 offen offset:1536 sc1\n"
    "v_accvgpr_read_b32 v16, a96\n"
    "v_accvgpr_read_b32 v17, a97\n"
    "v_accvgpr_read_b32 v18, a98\n"
    "v_accvgpr_read_b32 v19, a99\n"
    "v_accvgpr_read_b32 v20, a100\n"
    "v_accvgpr_read_b32 v21, a101\n"
    "v_accvgpr_read_b32 v22, a102\n"
    "v_accvgpr_read_b32 v23, a103\n"
    "v_pk_mul_f32 v[16:17], v[16:17], v[10:11] op_sel_hi:[1,0]\n"
    "v_pk_mul_f32 v[18:19], v[18:19], v[10:11] op_sel_hi:[1,0]\n"
    "v_pk_mul_f32 v[20:21], v[20:21], v[10:11] op_sel_hi:[1,0]\n"
    "v_pk_mul_f32 v[22:23], v[22:23], v[10:11] op_sel_hi:[1,0]\n"
    "v_cvt_pk_bf16_f32 v16, v16, v17\n"
    "v_cvt_pk_bf16_f32 v17, v18, v19\n"
    "v_cvt_pk_bf16_f32 v18, v20, v21\n"
    "v_cvt_pk_bf16_f32 v19, v22, v23\n"
    "buffer_store_dwordx4 v[16:19], v2, s[40:43], s70 offen offset:2048 sc1\n"
    "v_accvgpr_read_b32 v24, a104\n"
    "v_accvgpr_read_b32 v25, a105\n"
    "v_accvgpr_read_b32 v26, a106\n"
    "v_accvgpr_read_b32 v27, a107\n"
    "v_accvgpr_read_b32 v28, a108\n"
    "v_accvgpr_read_b32 v29, a109\n"
    "v_accvgpr_read_b32 v30, a110\n"
    "v_accvgpr_read_b32 v31, a111\n"
    "v_pk_mul_f32 v[24:25], v[24:25], v[10:11] op_sel_hi:[1,0]\n"
    "v_pk_mul_f32 v[26:27], v[26:27], v[10:11] op_sel_hi:[1,0]\n"
    "v_pk_mul_f32 v[28:29], v[28:29], v[10:11] op_sel_hi:[1,0]\n"
    "v_pk_mul_f32 v[30:31], v[30:31], v[10:11] op_sel_hi:[1,0]\n"
    "v_cvt_pk_bf16_f32 v24, v24, v25\n"
    "v_cvt_pk_bf16_f32 v25, v26, v27\n"
    "v_cvt_pk_bf16_f32 v26, v28, v29\n"
    "v_cvt_pk_bf16_f32 v27, v30, v31\n"
    "buffer_store_dwordx4 v[24:27], v2, s[40:43], s70 offen offset:2560 sc1\n"
    "v_accvgpr_read_b32 v32, a112\n"
    "v_accvgpr_read_b32 v33, a113\n"
    "v_accvgpr_read_b32 v34, a114\n"
    "v_accvgpr_read_b32 v35, a115\n"
    "v_accvgpr_read_b32 v36, a116\n"
    "v_accvgpr_read_b32 v37, a117\n"
    "v_accvgpr_read_b32 v38, a118\n"
    "v_accvgpr_read_b32 v39, a119\n"
    "v_pk_mul_f32 v[32:33], v[32:33], v[10:11] op_sel_hi:[1,0]\n"
    "v_pk_mul_f32 v[34:35], v[34:35], v[10:11] op_sel_hi:[1,0]\n"
    "v_pk_mul_f32 v[36:37], v[36:37], v[10:11] op_sel_hi:[1,0]\n"
    "v_pk_mul_f32 v[38:39], v[38:39], v[10:11] op_sel_hi:[1,0]\n"
    "v_cvt_pk_bf16_f32 v32, v32, v33\n"
    "v_cvt_pk_bf16_f32 v33, v34, v35\n"
    "v_cvt_pk_bf16_f32 v34, v36, v37\n"
    "v_cvt_pk_bf16_f32 v35, v38, v39\n"
    "buffer_store_dwordx4 v[32:35], v2, s[40:43], s70 offen offset:3072 sc1\n"
    "v_accvgpr_read_b32 v40, a120\n"
    "v_accvgpr_read_b32 v41, a121\n"
    "v_accvgpr_read_b32 v42, a122\n"
    "v_accvgpr_read_b32 v43, a123\n"
    "v_accvgpr_read_b32 v44, a124\n"
    "v_accvgpr_read_b32 v45, a125\n"
    "v_accvgpr_read_b32 v46, a126\n"
    "v_accvgpr_read_b32 v47, a127\n"
    "v_pk_mul_f32 v[40:41], v[40:41], v[10:11] op_sel_hi:[1,0]\n"
    "v_pk_mul_f32 v[42:43], v[42:43], v[10:11] op_sel_hi:[1,0]\n"
    "v_pk_mul_f32 v[44:45], v[44:45], v[10:11] op_sel_hi:[1,0]\n"
    "v_pk_mul_f32 v[46:47], v[46:47], v[10:11] op_sel_hi:[1,0]\n"
    "v_cvt_pk_bf16_f32 v40, v40, v41\n"
    "v_cvt_pk_bf16_f32 v41, v42, v43\n"
    "v_cvt_pk_bf16_f32 v42, v44, v45\n"
    "v_cvt_pk_bf16_f32 v43, v46, v47\n"
    "buffer_store_dwordx4 v[40:43], v2, s[40:43], s70 offen offset:3584 sc1\n"
    "v_mov_b32 v5, 1\n"
    "s_waitcnt vmcnt(0)\n"
    "buffer_store_dword v5, off, s[40:43], s71 sc1\n"
    ".Lat_idle_%=:\n"
    "s_add_u32 s57, s57, 1\n"
    "s_cmp_ge_u32 s57, s58\n"
    "s_cbranch_scc1 .Lat_bend_%=\n"
    "s_add_u32 s63, s62, 0x1000000\n"
    "s_add_u32 s56, s56, 1\n"
    "s_add_u32 s62, s62, 0x4000\n"
    "s_cmp_eq_u32 s56, s61\n"
    "s_cselect_b32 s62, s64, s62\n"
    "s_waitcnt vmcnt(8)\n"
    "s_barrier\n"
    "s_and_b32 s65, s56, 3\n"
    "s_lshl_b32 s65, s65, 14\n"
    "s_add_u32 m0, s67, s65\n"
    "s_nop 0\n"
    "buffer_load_dwordx4 v0, s[40:43], s62 offen offset:0 lds\n"
    "buffer_load_dwordx4 v0, s[40:43], s62 offen offset:1024 lds\n"
    "buffer_load_dwordx4 v0, s[40:43], s62 offen offset:2048 lds\n"
    "buffer_load_dwordx4 v0, s[40:43], s62 offen offset:3072 lds\n"
    "s_add_u32 s65, s65, 0xc000\n"
    "s_and_b32 s65, s65, 0xffff\n"
    "s_add_u32 m0, s68, s65\n"
    "s_nop 0\n"
    "buffer_load_dwordx4 v0, s[40:43], s63 offen offset:0 lds\n"
    "buffer_load_dwordx4 v0, s[40:43], s63 offen offset:1024 lds\n"
    "buffer_load_dwordx4 v0, s[40:43], s63 offen offset:2048 lds\n"
    "buffer_load_dwordx4 v0, s[40:43], s63 offen offset:3072 lds\n"
    "s_branch .Lat_idle_%=\n"
    ".Lat_dg1_%=:\n"
    "s_nop 11\n"
    "v_mov_b32 v5, 0xff800000\n"
    "v_cmp_gt_i32 vcc, 0, v1\n"
    "s_nop 1\n"
    "v_cndmask_b32 v16, v16, v5, vcc\n"
    "v_mov_b32 v32, v5\n"
    "v_cmp_gt_i32 vcc, 1, v1\n"
    "s_nop 1\n"
    "v_cndmask_b32 v17, v17, v5, vcc\n"
    "v_mov_b32 v33, v5\n"
    "v_cmp_gt_i32 vcc, 2, v1\n"
    "s_nop 1\n"
    "v_cndmask_b32 v18, v18, v5, vcc\n"
    "v_mov_b32 v34, v5\n"
    "v_cmp_gt_i32 vcc, 3, v1\n"
    "s_nop 1\n"
    "v_cndmask_b32 v19, v19, v5, vcc\n"
    "v_mov_b32 v35, v5\n"
    "v_cmp_gt_i32 vcc, 8, v1\n"
    "s_nop 1\n"
    "v_cndmask_b32 v20, v20, v5, vcc\n"
    "v_mov_b32 v36, v5\n"
    "v_cmp_gt_i32 vcc, 9, v1\n"
    "s_nop 1\n"
    "v_cndmask_b32 v21, v21, v5, vcc\n"
    "v_mov_b32 v37, v5\n"
    "v_cmp_gt_i32 vcc, 10, v1\n"
    "s_nop 1\n"
    "v_cndmask_b32 v22, v22, v5, vcc\n"
    "v_mov_b32 v38, v5\n"
    "v_cmp_gt_i32 vcc, 11, v1\n"
    "s_nop 1\n"
    "v_cndmask_b32 v23, v23, v5, vcc\n"
    "v_mov_b32 v39, v5\n"
    "v_cmp_gt_i32 vcc, 16, v1\n"
    "s_nop 1\n"
    "v_cndmask_b32 v24, v24, v5, vcc\n"
    "v_mov_b32 v40, v5\n"
    "v_cmp_gt_i32 vcc, 17, v1\n"
    "s_nop 1\n"
    "v_cndmask_b32 v25, v25, v5, vcc\n"
    "v_mov_b32 v41, v5\n"
    "v_cmp_gt_i32 vcc, 18, v1\n"
    "s_nop 1\n"
    "v_cndmask_b32 v26, v26, v5, vcc\n"
    "v_mov_b32 v42, v5\n"
    "v_cmp_gt_i32 vcc, 19, v1\n"
    "s_nop 1\n"
    "v_cndmask_b32 v27, v27, v5, vcc\n"
    "v_mov_b32 v43, v5\n"
    "v_cmp_gt_i32 vcc, 24, v1\n"
    "s_nop 1\n"
    "v_cndmask_b32 v28, v28, v5, vcc\n"
    "v_mov_b32 v44, v5\n"
    "v_cmp_gt_i32 vcc, 25, v1\n"
    "s_nop 1\n"
    "v_cndmask_b32 v29, v29, v5, vcc\n"
    "v_mov_b32 v45, v5\n"
    "v_cmp_gt_i32 vcc, 26, v1\n"
    "s_nop 1\n"
    "v_cndmask_b32 v30, v30, v5, vcc\n"
    "v_mov_b32 v46, v5\n"
    "v_cmp_gt_i32 vcc, 27, v1\n"
    "s_nop 1\n"
    "v_cndmask_b32 v31, v31, v5, vcc\n"
    "v_mov_b32 v47, v5\n"
    "s_branch .Lat_dgr1_%=\n"
    ".Lat_dg2_%=:\n"
    "s_nop 11\n"
    "v_mov_b32 v5, 0xff800000\n"
    "v_cmp_gt_i32 vcc, 0, v1\n"
    "s_nop 1\n"
    "v_cndmask_b32 v64, v64, v5, vcc\n"
    "v_cmp_gt_i32 vcc, 1, v1\n"
    "s_nop 1\n"
    "v_cndmask_b32 v65, v65, v5, vcc\n"
    "v_cmp_gt_i32 vcc, 2, v1\n"
    "s_nop 1\n"
    "v_cndmask_b32 v66, v66, v5, vcc\n"
    "v_cmp_gt_i32 vcc, 3, v1\n"
    "s_nop 1\n"
    "v_cndmask_b32 v67, v67, v5, vcc\n"
    "v_cmp_gt_i32 vcc, 8, v1\n"
    "s_nop 1\n"
    "v_cndmask_b32 v68, v68, v5, vcc\n"
    "v_cmp_gt_i32 vcc, 9, v1\n"
    "s_nop 1\n"
    "v_cndmask_b32 v69, v69, v5, vcc\n"
    "v_cmp_gt_i32 vcc, 10, v1\n"
    "s_nop 1\n"
    "v_cndmask_b32 v70, v70, v5, vcc\n"
    "v_cmp_gt_i32 vcc, 11, v1\n"
    "s_nop 1\n"
    "v_cndmask_b32 v71, v71, v5, vcc\n"
    "v_cmp_gt_i32 vcc, 16, v1\n"
    "s_nop 1\n"
    "v_cndmask_b32 v72, v72, v5, vcc\n"
    "v_cmp_gt_i32 vcc, 17, v1\n"
    "s_nop 1\n"
    "v_cndmask_b32 v73, v73, v5, vcc\n"
    "v_cmp_gt_i32 vcc, 18, v1\n"
    "s_nop 1\n"
    "v_cndmask_b32 v74, v74, v5, vcc\n"
    "v_cmp_gt_i32 vcc, 19, v1\n"
    "s_nop 1\n"
    "v_cndmask_b32 v75, v75, v5, vcc\n"
    "v_cmp_gt_i32 vcc, 24, v1\n"
    "s_nop 1\n"
    "v_cndmask_b32 v76, v76, v5, vcc\n"
    "v_cmp_gt_i32 vcc, 25, v1\n"
    "s_nop 1\n"
    "v_cndmask_b32 v77, v77, v5, vcc\n"
    "v_cmp_gt_i32 vcc, 26, v1\n"
    "s_nop 1\n"
    "v_cndmask_b32 v78, v78, v5, vcc\n"
    "v_cmp_gt_i32 vcc, 27, v1\n"
    "s_nop 1\n"
    "v_cndmask_b32 v79, v79, v5, vcc\n"
    "s_branch .Lat_dgr2_%=\n"
    ".Lat_dg3_%=:\n"
    "s_nop 11\n"
    "v_mov_b32 v5, 0xff800000\n"
    "v_cmp_gt_i32 vcc, 0, v1\n"
    "s_nop 1\n"
    "v_cndmask_b32 v16, v16, v5, vcc\n"
    "v_mov_b32 v32, v5\n"
    "v_cmp_gt_i32 vcc, 1, v1\n"
    "s_nop 1\n"
    "v_cndmask_b32 v17, v17, v5, vcc\n"
    "v_mov_b32 v33, v5\n"
    "v_cmp_gt_i32 vcc, 2, v1\n"
    "s_nop 1\n"
    "v_cndmask_b32 v18, v18, v5, vcc\n"
    "v_mov_b32 v34, v5\n"
    "v_cmp_gt_i32 vcc, 3, v1\n"
    "s_nop 1\n"
    "v_cndmask_b32 v19, v19, v5, vcc\n"
    "v_mov_b32 v35, v5\n"
    "v_cmp_gt_i32 vcc, 8, v1\n"
    "s_nop 1\n"
    "v_cndmask_b32 v20, v20, v5, vcc\n"
    "v_mov_b32 v36, v5\n"
    "v_cmp_gt_i32 vcc, 9, v1\n"
    "s_nop 1\n"
    "v_cndmask_b32 v21, v21, v5, vcc\n"
    "v_mov_b32 v37, v5\n"
    "v_cmp_gt_i32 vcc, 10, v1\n"
    "s_nop 1\n"
    "v_cndmask_b32 v22, v22, v5, vcc\n"
    "v_mov_b32 v38, v5\n"
    "v_cmp_gt_i32 vcc, 11, v1\n"
    "s_nop 1\n"
    "v_cndmask_b32 v23, v23, v5, vcc\n"
    "v_mov_b32 v39, v5\n"
    "v_cmp_gt_i32 vcc, 16, v1\n"
    "s_nop 1\n"
    "v_cndmask_b32 v24, v24, v5, vcc\n"
    "v_mov_b32 v40, v5\n"
    "v_cmp_gt_i32 vcc, 17, v1\n"
    "s_nop 1\n"
    "v_cndmask_b32 v25, v25, v5, vcc\n"
    "v_mov_b32 v41, v5\n"
    "v_cmp_gt_i32 vcc, 18, v1\n"
    "s_nop 1\n"
    "v_cndmask_b32 v26, v26, v5, vcc\n"
    "v_mov_b32 v42, v5\n"
    "v_cmp_gt_i32 vcc, 19, v1\n"
    "s_nop 1\n"
    "v_cndmask_b32 v27, v27, v5, vcc\n"
    "v_mov_b32 v43, v5\n"
    "v_cmp_gt_i32 vcc, 24, v1\n"
    "s_nop 1\n"
    "v_cndmask_b32 v28, v28, v5, vcc\n"
    "v_mov_b32 v44, v5\n"
    "v_cmp_gt_i32 vcc, 25, v1\n"
    "s_nop 1\n"
    "v_cndmask_b32 v29, v29, v5, vcc\n"
    "v_mov_b32 v45, v5\n"
    "v_cmp_gt_i32 vcc, 26, v1\n"
    "s_nop 1\n"
    "v_cndmask_b32 v30, v30, v5, vcc\n"
    "v_mov_b32 v46, v5\n"
    "v_cmp_gt_i32 vcc, 27, v1\n"
    "s_nop 1\n"
    "v_cndmask_b32 v31, v31, v5, vcc\n"
    "v_mov_b32 v47, v5\n"
    "s_branch .Lat_dgr3_%=\n"
    ".Lat_rs3_%=:\n"
    "v_max_f32 v12, 0, v12\n"
    "v_exp_f32 v5, -v12\n"
    "v_sub_f32 v16, v16, v12\n"
    "v_sub_f32 v32, v32, v12\n"
    "v_sub_f32 v80, v80, v12\n"
    "v_sub_f32 v17, v17, v12\n"
    "v_sub_f32 v33, v33, v12\n"
    "v_sub_f32 v81, v81, v12\n"
    "v_sub_f32 v18, v18, v12\n"
    "v_sub_f32 v34, v34, v12\n"
    "v_sub_f32 v82, v82, v12\n"
    "v_sub_f32 v19, v19, v12\n"
    "v_sub_f32 v35, v35, v12\n"
    "v_sub_f32 v83, v83, v12\n"
    "v_sub_f32 v20, v20, v12\n"
    "v_sub_f32 v36, v36, v12\n"
    "v_sub_f32 v84, v84, v12\n"
    "v_sub_f32 v21, v21, v12\n"
    "v_sub_f32 v37, v37, v12\n"
    "v_sub_f32 v85, v85, v12\n"
    "v_sub_f32 v22, v22, v12\n"
    "v_sub_f32 v38, v38, v12\n"
    "v_sub_f32 v86, v86, v12\n"
    "v_sub_f32 v23, v23, v12\n"
    "v_sub_f32 v39, v39, v12\n"
    "v_sub_f32 v87, v87, v12\n"
    "v_sub_f32 v24, v24, v12\n"
    "v_sub_f32 v40, v40, v12\n"
    "v_sub_f32 v88, v88, v12\n"
    "v_sub_f32 v25, v25, v12\n"
    "v_sub_f32 v41, v41, v12\n"
    "v_sub_f32 v89, v89, v12\n"
    "v_sub_f32 v26, v26, v12\n"
    "v_sub_f32 v42, v42, v12\n"
    "v_sub_f32 v90, v90, v12\n"
    "v_sub_f32 v27, v27, v12\n"
    "v_sub_f32 v43, v43, v12\n"
    "v_sub_f32 v91, v91, v12\n"
    "v_sub_f32 v28, v28, v12\n"
    "v_sub_f32 v44, v44, v12\n"
    "v_sub_f32 v92, v92, v12\n"
    "v_sub_f32 v29, v29, v12\n"
    "v_sub_f32 v45, v45, v12\n"
    "v_sub_f32 v93, v93, v12\n"
    "v_sub_f32 v30, v30, v12\n"
    "v_sub_f32 v46, v46, v12\n"
    "v_sub_f32 v94, v94, v12\n"
    "v_sub_f32 v31, v31, v12\n"
    "v_sub_f32 v47, v47, v12\n"
    "v_sub_f32 v95, v95, v12\n"
    "v_mul_f32 v8, v5, v8\n"
    "v_mul_f32 v9, v5, v9\n"
    "v_accvgpr_read_b32 v6, a0\n"
    "v_accvgpr_read_b32 v7, a1\n"
    "v_mul_f32 v6, v5, v6\n"
    "v_mul_f32 v7, v5, v7\n"
    "v_accvgpr_write_b32 a0, v6\n"
    "v_accvgpr_write_b32 a1, v7\n"
    "v_accvgpr_read_b32 v6, a2\n"
    "v_accvgpr_read_b32 v7, a3\n"
    "v_mul_f32 v6, v5, v6\n"
    "v_mul_f32 v7, v5, v7\n"
    "v_accvgpr_write_b32 a2, v6\n"
    "v_accvgpr_write_b32 a3, v7\n"
    "v_accvgpr_read_b32 v6, a4\n"
    "v_accvgpr_read_b32 v7, a5\n"
    "v_mul_f32 v6, v5, v6\n"
    "v_mul_f32 v7, v5, v7\n"
    "v_accvgpr_write_b32 a4, v6\n"
    "v_accvgpr_write_b32 a5, v7\n"
    "v_accvgpr_read_b32 v6, a6\n"
    "v_accvgpr_read_b32 v7, a7\n"
    "v_mul_f32 v6, v5, v6\n"
    "v_mul_f32 v7, v5, v7\n"
    "v_accvgpr_write_b32 a6, v6\n"
    "v_accvgpr_write_b32 a7, v7\n"
    "v_accvgpr_read_b32 v6, a8\n"
    "v_accvgpr_read_b32 v7, a9\n"
    "v_mul_f32 v6, v5, v6\n"
    "v_mul_f32 v7, v5, v7\n"
    "v_accvgpr_write_b32 a8, v6\n"
    "v_accvgpr_write_b32 a9, v7\n"
    "v_accvgpr_read_b32 v6, a10\n"
    "v_accvgpr_read_b32 v7, a11\n"
    "v_mul_f32 v6, v5, v6\n"
    "v_mul_f32 v7, v5, v7\n"
    "v_accvgpr_write_b32 a10, v6\n"
    "v_accvgpr_write_b32 a11, v7\n"
    "v_accvgpr_read_b32 v6, a12\n"
    "v_accvgpr_read_b32 v7, a13\n"
    "v_mul_f32 v6, v5, v6\n"
    "v_mul_f32 v7, v5, v7\n"
    "v_accvgpr_write_b32 a12, v6\n"
    "v_accvgpr_write_b32 a13, v7\n"
    "v_accvgpr_read_b32 v6, a14\n"
    "v_accvgpr_read_b32 v7, a15\n"
    "v_mul_f32 v6, v5, v6\n"
    "v_mul_f32 v7, v5, v7\n"
    "v_accvgpr_write_b32 a14, v6\n"
    "v_accvgpr_write_b32 a15, v7\n"
    "v_accvgpr_read_b32 v6, a16\n"
    "v_accvgpr_read_b32 v7, a17\n"
    "v_mul_f32 v6, v5, v6\n"
    "v_mul_f32 v7, v5, v7\n"
    "v_accvgpr_write_b32 a16, v6\n"
    "v_accvgpr_write_b32 a17, v7\n"
    "v_accvgpr_read_b32 v6, a18\n"
    "v_accvgpr_read_b32 v7, a19\n"
    "v_mul_f32 v6, v5, v6\n"
    "v_mul_f32 v7, v5, v7\n"
    "v_accvgpr_write_b32 a18, v6\n"
    "v_accvgpr_write_b32 a19, v7\n"
    "v_accvgpr_read_b32 v6, a20\n"
    "v_accvgpr_read_b32 v7, a21\n"
    "v_mul_f32 v6, v5, v6\n"
    "v_mul_f32 v7, v5, v7\n"
    "v_accvgpr_write_b32 a20, v6\n"
    "v_accvgpr_write_b32 a21, v7\n"
    "v_accvgpr_read_b32 v6, a22\n"
    "v_accvgpr_read_b32 v7, a23\n"
    "v_mul_f32 v6, v5, v6\n"
    "v_mul_f32 v7, v5, v7\n"
    "v_accvgpr_write_b32 a22, v6\n"
    "v_accvgpr_write_b32 a23, v7\n"
    "v_accvgpr_read_b32 v6, a24\n"
    "v_accvgpr_read_b32 v7, a25\n"
    "v_mul_f32 v6, v5, v6\n"
    "v_mul_f32 v7, v5, v7\n"
    "v_accvgpr_write_b32 a24, v6\n"
    "v_accvgpr_write_b32 a25, v7\n"
    "v_accvgpr_read_b32 v6, a26\n"
    "v_accvgpr_read_b32 v7, a27\n"
    "v_mul_f32 v6, v5, v6\n"
    "v_mul_f32 v7, v5, v7\n"
    "v_accvgpr_write_b32 a26, v6\n"
    "v_accvgpr_write_b32 a27, v7\n"
    "v_accvgpr_read_b32 v6, a28\n"
    "v_accvgpr_read_b32 v7, a29\n"
    "v_mul_f32 v6, v5, v6\n"
    "v_mul_f32 v7, v5, v7\n"
    "v_accvgpr_write_b32 a28, v6\n"
    "v_accvgpr_write_b32 a29, v7\n"
    "v_accvgpr_read_b32 v6, a30\n"
    "v_accvgpr_read_b32 v7, a31\n"
    "v_mul_f32 v6, v5, v6\n"
    "v_mul_f32 v7, v5, v7\n"
    "v_accvgpr_write_b32 a30, v6\n"
    "v_accvgpr_write_b32 a31, v7\n"
    "v_accvgpr_read_b32 v6, a32\n"
    "v_accvgpr_read_b32 v7, a33\n"
    "v_mul_f32 v6, v5, v6\n"
    "v_mul_f32 v7, v5, v7\n"
    "v_accvgpr_write_b32 a32, v6\n"
    "v_accvgpr_write_b32 a33, v7\n"
    "v_accvgpr_read_b32 v6, a34\n"
    "v_accvgpr_read_b32 v7, a35\n"
    "v_mul_f32 v6, v5, v6\n"
    "v_mul_f32 v7, v5, v7\n"
    "v_accvgpr_write_b32 a34, v6\n"
    "v_accvgpr_write_b32 a35, v7\n"
    "v_accvgpr_read_b32 v6, a36\n"
    "v_accvgpr_read_b32 v7, a37\n"
    "v_mul_f32 v6, v5, v6\n"
    "v_mul_f32 v7, v5, v7\n"
    "v_accvgpr_write_b32 a36, v6\n"
    "v_accvgpr_write_b32 a37, v7\n"
    "v_accvgpr_read_b32 v6, a38\n"
    "v_accvgpr_read_b32 v7, a39\n"
    "v_mul_f32 v6, v5, v6\n"
    "v_mul_f32 v7, v5, v7\n"
    "v_accvgpr_write_b32 a38, v6\n"
    "v_accvgpr_write_b32 a39, v7\n"
    "v_accvgpr_read_b32 v6, a40\n"
    "v_accvgpr_read_b32 v7, a41\n"
    "v_mul_f32 v6, v5, v6\n"
    "v_mul_f32 v7, v5, v7\n"
    "v_accvgpr_write_b32 a40, v6\n"
    "v_accvgpr_write_b32 a41, v7\n"
    "v_accvgpr_read_b32 v6, a42\n"
    "v_accvgpr_read_b32 v7, a43\n"
    "v_mul_f32 v6, v5, v6\n"
    "v_mul_f32 v7, v5, v7\n"
    "v_accvgpr_write_b32 a42, v6\n"
    "v_accvgpr_write_b32 a43, v7\n"
    "v_accvgpr_read_b32 v6, a44\n"
    "v_accvgpr_read_b32 v7, a45\n"
    "v_mul_f32 v6, v5, v6\n"
    "v_mul_f32 v7, v5, v7\n"
    "v_accvgpr_write_b32 a44, v6\n"
    "v_accvgpr_write_b32 a45, v7\n"
    "v_accvgpr_read_b32 v6, a46\n"
    "v_accvgpr_read_b32 v7, a47\n"
    "v_mul_f32 v6, v5, v6\n"
    "v_mul_f32 v7, v5, v7\n"
    "v_accvgpr_write_b32 a46, v6\n"
    "v_accvgpr_write_b32 a47, v7\n"
    "v_accvgpr_read_b32 v6, a48\n"
    "v_accvgpr_read_b32 v7, a49\n"
    "v_mul_f32 v6, v5, v6\n"
    "v_mul_f32 v7, v5, v7\n"
    "v_accvgpr_write_b32 a48, v6\n"
    "v_accvgpr_write_b32 a49, v7\n"
    "v_accvgpr_read_b32 v6, a50\n"
    "v_accvgpr_read_b32 v7, a51\n"
    "v_mul_f32 v6, v5, v6\n"
    "v_mul_f32 v7, v5, v7\n"
    "v_accvgpr_write_b32 a50, v6\n"
    "v_accvgpr_write_b32 a51, v7\n"
    "v_accvgpr_read_b32 v6, a52\n"
    "v_accvgpr_read_b32 v7, a53\n"
    "v_mul_f32 v6, v5, v6\n"
    "v_mul_f32 v7, v5, v7\n"
    "v_accvgpr_write_b32 a52, v6\n"
    "v_accvgpr_write_b32 a53, v7\n"
    "v_accvgpr_read_b32 v6, a54\n"
    "v_accvgpr_read_b32 v7, a55\n"
    "v_mul_f32 v6, v5, v6\n"
    "v_mul_f32 v7, v5, v7\n"
    "v_accvgpr_write_b32 a54, v6\n"
    "v_accvgpr_write_b32 a55, v7\n"
    "v_accvgpr_read_b32 v6, a56\n"
    "v_accvgpr_read_b32 v7, a57\n"
    "v_mul_f32 v6, v5, v6\n"
    "v_mul_f32 v7, v5, v7\n"
    "v_accvgpr_write_b32 a56, v6\n"
    "v_accvgpr_write_b32 a57, v7\n"
    "v_accvgpr_read_b32 v6, a58\n"
    "v_accvgpr_read_b32 v7, a59\n"
    "v_mul_f32 v6, v5, v6\n"
    "v_mul_f32 v7, v5, v7\n"
    "v_accvgpr_write_b32 a58, v6\n"
    "v_accvgpr_write_b32 a59, v7\n"
    "v_accvgpr_read_b32 v6, a60\n"
    "v_accvgpr_read_b32 v7, a61\n"
    "v_mul_f32 v6, v5, v6\n"
    "v_mul_f32 v7, v5, v7\n"
    "v_accvgpr_write_b32 a60, v6\n"
    "v_accvgpr_write_b32 a61, v7\n"
    "v_accvgpr_read_b32 v6, a62\n"
    "v_accvgpr_read_b32 v7, a63\n"
    "v_mul_f32 v6, v5, v6\n"
    "v_mul_f32 v7, v5, v7\n"
    "v_accvgpr_write_b32 a62, v6\n"
    "v_accvgpr_write_b32 a63, v7\n"
    "s_branch .Lat_rsr3_%=\n"
    ".Lat_dg4_%=:\n"
    "s_nop 11\n"
    "v_mov_b32 v5, 0xff800000\n"
    "v_cmp_gt_i32 vcc, 0, v1\n"
    "s_nop 1\n"
    "v_cndmask_b32 v64, v64, v5, vcc\n"
    "v_cmp_gt_i32 vcc, 1, v1\n"
    "s_nop 1\n"
    "v_cndmask_b32 v65, v65, v5, vcc\n"
    "v_cmp_gt_i32 vcc, 2, v1\n"
    "s_nop 1\n"
    "v_cndmask_b32 v66, v66, v5, vcc\n"
    "v_cmp_gt_i32 vcc, 3, v1\n"
    "s_nop 1\n"
    "v_cndmask_b32 v67, v67, v5, vcc\n"
    "v_cmp_gt_i32 vcc, 8, v1\n"
    "s_nop 1\n"
    "v_cndmask_b32 v68, v68, v5, vcc\n"
    "v_cmp_gt_i32 vcc, 9, v1\n"
    "s_nop 1\n"
    "v_cndmask_b32 v69, v69, v5, vcc\n"
    "v_cmp_gt_i32 vcc, 10, v1\n"
    "s_nop 1\n"
    "v_cndmask_b32 v70, v70, v5, vcc\n"
    "v_cmp_gt_i32 vcc, 11, v1\n"
    "s_nop 1\n"
    "v_cndmask_b32 v71, v71, v5, vcc\n"
    "v_cmp_gt_i32 vcc, 16, v1\n"
    "s_nop 1\n"
    "v_cndmask_b32 v72, v72, v5, vcc\n"
    "v_cmp_gt_i32 vcc, 17, v1\n"
    "s_nop 1\n"
    "v_cndmask_b32 v73, v73, v5, vcc\n"
    "v_cmp_gt_i32 vcc, 18, v1\n"
    "s_nop 1\n"
    "v_cndmask_b32 v74, v74, v5, vcc\n"
    "v_cmp_gt_i32 vcc, 19, v1\n"
    "s_nop 1\n"
    "v_cndmask_b32 v75, v75, v5, vcc\n"
    "v_cmp_gt_i32 vcc, 24, v1\n"
    "s_nop 1\n"
    "v_cndmask_b32 v76, v76, v5, vcc\n"
    "v_cmp_gt_i32 vcc, 25, v1\n"
    "s_nop 1\n"
    "v_cndmask_b32 v77, v77, v5, vcc\n"
    "v_cmp_gt_i32 vcc, 26, v1\n"
    "s_nop 1\n"
    "v_cndmask_b32 v78, v78, v5, vcc\n"
    "v_cmp_gt_i32 vcc, 27, v1\n"
    "s_nop 1\n"
    "v_cndmask_b32 v79, v79, v5, vcc\n"
    "s_branch .Lat_dgr4_%=\n"
    ".Lat_rs4_%=:\n"
    "v_max_f32 v14, 0, v14\n"
    "v_exp_f32 v5, -v14\n"
    "v_sub_f32 v48, v48, v14\n"
    "v_sub_f32 v64, v64, v14\n"
    "v_sub_f32 v96, v96, v14\n"
    "v_sub_f32 v49, v49, v14\n"
    "v_sub_f32 v65, v65, v14\n"
    "v_sub_f32 v97, v97, v14\n"
    "v_sub_f32 v50, v50, v14\n"
    "v_sub_f32 v66, v66, v14\n"
    "v_sub_f32 v98, v98, v14\n"
    "v_sub_f32 v51, v51, v14\n"
    "v_sub_f32 v67, v67, v14\n"
    "v_sub_f32 v99, v99, v14\n"
    "v_sub_f32 v52, v52, v14\n"
    "v_sub_f32 v68, v68, v14\n"
    "v_sub_f32 v100, v100, v14\n"
    "v_sub_f32 v53, v53, v14\n"
    "v_sub_f32 v69, v69, v14\n"
    "v_sub_f32 v101, v101, v14\n"
    "v_sub_f32 v54, v54, v14\n"
    "v_sub_f32 v70, v70, v14\n"
    "v_sub_f32 v102, v102, v14\n"
    "v_sub_f32 v55, v55, v14\n"
    "v_sub_f32 v71, v71, v14\n"
    "v_sub_f32 v103, v103, v14\n"
    "v_sub_f32 v56, v56, v14\n"
    "v_sub_f32 v72, v72, v14\n"
    "v_sub_f32 v104, v104, v14\n"
    "v_sub_f32 v57, v57, v14\n"
    "v_sub_f32 v73, v73, v14\n"
    "v_sub_f32 v105, v105, v14\n"
    "v_sub_f32 v58, v58, v14\n"
    "v_sub_f32 v74, v74, v14\n"
    "v_sub_f32 v106, v106, v14\n"
    "v_sub_f32 v59, v59, v14\n"
    "v_sub_f32 v75, v75, v14\n"
    "v_sub_f32 v107, v107, v14\n"
    "v_sub_f32 v60, v60, v14\n"
    "v_sub_f32 v76, v76, v14\n"
    "v_sub_f32 v108, v108, v14\n"
    "v_sub_f32 v61, v61, v14\n"
    "v_sub_f32 v77, v77, v14\n"
    "v_sub_f32 v109, v109, v14\n"
    "v_sub_f32 v62, v62, v14\n"
    "v_sub_f32 v78, v78, v14\n"
    "v_sub_f32 v110, v110, v14\n"
    "v_sub_f32 v63, v63, v14\n"
    "v_sub_f32 v79, v79, v14\n"
    "v_sub_f32 v111, v111, v14\n"
    "v_mul_f32 v10, v5, v10\n"
    "v_mul_f32 v11, v5, v11\n"
    "v_accvgpr_read_b32 v6, a64\n"
    "v_accvgpr_read_b32 v7, a65\n"
    "v_mul_f32 v6, v5, v6\n"
    "v_mul_f32 v7, v5, v7\n"
    "v_accvgpr_write_b32 a64, v6\n"
    "v_accvgpr_write_b32 a65, v7\n"
    "v_accvgpr_read_b32 v6, a66\n"
    "v_accvgpr_read_b32 v7, a67\n"
    "v_mul_f32 v6, v5, v6\n"
    "v_mul_f32 v7, v5, v7\n"
    "v_accvgpr_write_b32 a66, v6\n"
    "v_accvgpr_write_b32 a67, v7\n"
    "v_accvgpr_read_b32 v6, a68\n"
    "v_accvgpr_read_b32 v7, a69\n"
    "v_mul_f32 v6, v5, v6\n"
    "v_mul_f32 v7, v5, v7\n"
    "v_accvgpr_write_b32 a68, v6\n"
    "v_accvgpr_write_b32 a69, v7\n"
    "v_accvgpr_read_b32 v6, a70\n"
    "v_accvgpr_read_b32 v7, a71\n"
    "v_mul_f32 v6, v5, v6\n"
    "v_mul_f32 v7, v5, v7\n"
    "v_accvgpr_write_b32 a70, v6\n"
    "v_accvgpr_write_b32 a71, v7\n"
    "v_accvgpr_read_b32 v6, a72\n"
    "v_accvgpr_read_b32 v7, a73\n"
    "v_mul_f32 v6, v5, v6\n"
    "v_mul_f32 v7, v5, v7\n"
    "v_accvgpr_write_b32 a72, v6\n"
    "v_accvgpr_write_b32 a73, v7\n"
    "v_accvgpr_read_b32 v6, a74\n"
    "v_accvgpr_read_b32 v7, a75\n"
    "v_mul_f32 v6, v5, v6\n"
    "v_mul_f32 v7, v5, v7\n"
    "v_accvgpr_write_b32 a74, v6\n"
    "v_accvgpr_write_b32 a75, v7\n"
    "v_accvgpr_read_b32 v6, a76\n"
    "v_accvgpr_read_b32 v7, a77\n"
    "v_mul_f32 v6, v5, v6\n"
    "v_mul_f32 v7, v5, v7\n"
    "v_accvgpr_write_b32 a76, v6\n"
    "v_accvgpr_write_b32 a77, v7\n"
    "v_accvgpr_read_b32 v6, a78\n"
    "v_accvgpr_read_b32 v7, a79\n"
    "v_mul_f32 v6, v5, v6\n"
    "v_mul_f32 v7, v5, v7\n"
    "v_accvgpr_write_b32 a78, v6\n"
    "v_accvgpr_write_b32 a79, v7\n"
    "v_accvgpr_read_b32 v6, a80\n"
    "v_accvgpr_read_b32 v7, a81\n"
    "v_mul_f32 v6, v5, v6\n"
    "v_mul_f32 v7, v5, v7\n"
    "v_accvgpr_write_b32 a80, v6\n"
    "v_accvgpr_write_b32 a81, v7\n"
    "v_accvgpr_read_b32 v6, a82\n"
    "v_accvgpr_read_b32 v7, a83\n"
    "v_mul_f32 v6, v5, v6\n"
    "v_mul_f32 v7, v5, v7\n"
    "v_accvgpr_write_b32 a82, v6\n"
    "v_accvgpr_write_b32 a83, v7\n"
    "v_accvgpr_read_b32 v6, a84\n"
    "v_accvgpr_read_b32 v7, a85\n"
    "v_mul_f32 v6, v5, v6\n"
    "v_mul_f32 v7, v5, v7\n"
    "v_accvgpr_write_b32 a84, v6\n"
    "v_accvgpr_write_b32 a85, v7\n"
    "v_accvgpr_read_b32 v6, a86\n"
    "v_accvgpr_read_b32 v7, a87\n"
    "v_mul_f32 v6, v5, v6\n"
    "v_mul_f32 v7, v5, v7\n"
    "v_accvgpr_write_b32 a86, v6\n"
    "v_accvgpr_write_b32 a87, v7\n"
    "v_accvgpr_read_b32 v6, a88\n"
    "v_accvgpr_read_b32 v7, a89\n"
    "v_mul_f32 v6, v5, v6\n"
    "v_mul_f32 v7, v5, v7\n"
    "v_accvgpr_write_b32 a88, v6\n"
    "v_accvgpr_write_b32 a89, v7\n"
    "v_accvgpr_read_b32 v6, a90\n"
    "v_accvgpr_read_b32 v7, a91\n"
    "v_mul_f32 v6, v5, v6\n"
    "v_mul_f32 v7, v5, v7\n"
    "v_accvgpr_write_b32 a90, v6\n"
    "v_accvgpr_write_b32 a91, v7\n"
    "v_accvgpr_read_b32 v6, a92\n"
    "v_accvgpr_read_b32 v7, a93\n"
    "v_mul_f32 v6, v5, v6\n"
    "v_mul_f32 v7, v5, v7\n"
    "v_accvgpr_write_b32 a92, v6\n"
    "v_accvgpr_write_b32 a93, v7\n"
    "v_accvgpr_read_b32 v6, a94\n"
    "v_accvgpr_read_b32 v7, a95\n"
    "v_mul_f32 v6, v5, v6\n"
    "v_mul_f32 v7, v5, v7\n"
    "v_accvgpr_write_b32 a94, v6\n"
    "v_accvgpr_write_b32 a95, v7\n"
    "v_accvgpr_read_b32 v6, a96\n"
    "v_accvgpr_read_b32 v7, a97\n"
    "v_mul_f32 v6, v5, v6\n"
    "v_mul_f32 v7, v5, v7\n"
    "v_accvgpr_write_b32 a96, v6\n"
    "v_accvgpr_write_b32 a97, v7\n"
    "v_accvgpr_read_b32 v6, a98\n"
    "v_accvgpr_read_b32 v7, a99\n"
    "v_mul_f32 v6, v5, v6\n"
    "v_mul_f32 v7, v5, v7\n"
    "v_accvgpr_write_b32 a98, v6\n"
    "v_accvgpr_write_b32 a99, v7\n"
    "v_accvgpr_read_b32 v6, a100\n"
    "v_accvgpr_read_b32 v7, a101\n"
    "v_mul_f32 v6, v5, v6\n"
    "v_mul_f32 v7, v5, v7\n"
    "v_accvgpr_write_b32 a100, v6\n"
    "v_accvgpr_write_b32 a101, v7\n"
    "v_accvgpr_read_b32 v6, a102\n"
    "v_accvgpr_read_b32 v7, a103\n"
    "v_mul_f32 v6, v5, v6\n"
    "v_mul_f32 v7, v5, v7\n"
    "v_accvgpr_write_b32 a102, v6\n"
    "v_accvgpr_write_b32 a103, v7\n"
    "v_accvgpr_read_b32 v6, a104\n"
    "v_accvgpr_read_b32 v7, a105\n"
    "v_mul_f32 v6, v5, v6\n"
    "v_mul_f32 v7, v5, v7\n"
    "v_accvgpr_write_b32 a104, v6\n"
    "v_accvgpr_write_b32 a105, v7\n"
    "v_accvgpr_read_b32 v6, a106\n"
    "v_accvgpr_read_b32 v7, a107\n"
    "v_mul_f32 v6, v5, v6\n"
    "v_mul_f32 v7, v5, v7\n"
    "v_accvgpr_write_b32 a106, v6\n"
    "v_accvgpr_write_b32 a107, v7\n"
    "v_accvgpr_read_b32 v6, a108\n"
    "v_accvgpr_read_b32 v7, a109\n"
    "v_mul_f32 v6, v5, v6\n"
    "v_mul_f32 v7, v5, v7\n"
    "v_accvgpr_write_b32 a108, v6\n"
    "v_accvgpr_write_b32 a109, v7\n"
    "v_accvgpr_read_b32 v6, a110\n"
    "v_accvgpr_read_b32 v7, a111\n"
    "v_mul_f32 v6, v5, v6\n"
    "v_mul_f32 v7, v5, v7\n"
    "v_accvgpr_write_b32 a110, v6\n"
    "v_accvgpr_write_b32 a111, v7\n"
    "v_accvgpr_read_b32 v6, a112\n"
    "v_accvgpr_read_b32 v7, a113\n"
    "v_mul_f32 v6, v5, v6\n"
    "v_mul_f32 v7, v5, v7\n"
    "v_accvgpr_write_b32 a112, v6\n"
    "v_accvgpr_write_b32 a113, v7\n"
    "v_accvgpr_read_b32 v6, a114\n"
    "v_accvgpr_read_b32 v7, a115\n"
    "v_mul_f32 v6, v5, v6\n"
    "v_mul_f32 v7, v5, v7\n"
    "v_accvgpr_write_b32 a114, v6\n"
    "v_accvgpr_write_b32 a115, v7\n"
    "v_accvgpr_read_b32 v6, a116\n"
    "v_accvgpr_read_b32 v7, a117\n"
    "v_mul_f32 v6, v5, v6\n"
    "v_mul_f32 v7, v5, v7\n"
    "v_accvgpr_write_b32 a116, v6\n"
    "v_accvgpr_write_b32 a117, v7\n"
    "v_accvgpr_read_b32 v6, a118\n"
    "v_accvgpr_read_b32 v7, a119\n"
    "v_mul_f32 v6, v5, v6\n"
    "v_mul_f32 v7, v5, v7\n"
    "v_accvgpr_write_b32 a118, v6\n"
    "v_accvgpr_write_b32 a119, v7\n"
    "v_accvgpr_read_b32 v6, a120\n"
    "v_accvgpr_read_b32 v7, a121\n"
    "v_mul_f32 v6, v5, v6\n"
    "v_mul_f32 v7, v5, v7\n"
    "v_accvgpr_write_b32 a120, v6\n"
    "v_accvgpr_write_b32 a121, v7\n"
    "v_accvgpr_read_b32 v6, a122\n"
    "v_accvgpr_read_b32 v7, a123\n"
    "v_mul_f32 v6, v5, v6\n"
    "v_mul_f32 v7, v5, v7\n"
    "v_accvgpr_write_b32 a122, v6\n"
    "v_accvgpr_write_b32 a123, v7\n"
    "v_accvgpr_read_b32 v6, a124\n"
    "v_accvgpr_read_b32 v7, a125\n"
    "v_mul_f32 v6, v5, v6\n"
    "v_mul_f32 v7, v5, v7\n"
    "v_accvgpr_write_b32 a124, v6\n"
    "v_accvgpr_write_b32 a125, v7\n"
    "v_accvgpr_read_b32 v6, a126\n"
    "v_accvgpr_read_b32 v7, a127\n"
    "v_mul_f32 v6, v5, v6\n"
    "v_mul_f32 v7, v5, v7\n"
    "v_accvgpr_write_b32 a126, v6\n"
    "v_accvgpr_write_b32 a127, v7\n"
    "s_branch .Lat_rsr4_%=\n"
    ".Lat_dg5_%=:\n"
    "s_nop 11\n"
    "v_mov_b32 v5, 0xff800000\n"
    "v_cmp_gt_i32 vcc, 0, v1\n"
    "s_nop 1\n"
    "v_cndmask_b32 v16, v16, v5, vcc\n"
    "v_mov_b32 v32, v5\n"
    "v_cmp_gt_i32 vcc, 1, v1\n"
    "s_nop 1\n"
    "v_cndmask_b32 v17, v17, v5, vcc\n"
    "v_mov_b32 v33, v5\n"
    "v_cmp_gt_i32 vcc, 2, v1\n"
    "s_nop 1\n"
    "v_cndmask_b32 v18, v18, v5, vcc\n"
    "v_mov_b32 v34, v5\n"
    "v_cmp_gt_i32 vcc, 3, v1\n"
    "s_nop 1\n"
    "v_cndmask_b32 v19, v19, v5, vcc\n"
    "v_mov_b32 v35, v5\n"
    "v_cmp_gt_i32 vcc, 8, v1\n"
    "s_nop 1\n"
    "v_cndmask_b32 v20, v20, v5, vcc\n"
    "v_mov_b32 v36, v5\n"
    "v_cmp_gt_i32 vcc, 9, v1\n"
    "s_nop 1\n"
    "v_cndmask_b32 v21, v21, v5, vcc\n"
    "v_mov_b32 v37, v5\n"
    "v_cmp_gt_i32 vcc, 10, v1\n"
    "s_nop 1\n"
    "v_cndmask_b32 v22, v22, v5, vcc\n"
    "v_mov_b32 v38, v5\n"
    "v_cmp_gt_i32 vcc, 11, v1\n"
    "s_nop 1\n"
    "v_cndmask_b32 v23, v23, v5, vcc\n"
    "v_mov_b32 v39, v5\n"
    "v_cmp_gt_i32 vcc, 16, v1\n"
    "s_nop 1\n"
    "v_cndmask_b32 v24, v24, v5, vcc\n"
    "v_mov_b32 v40, v5\n"
    "v_cmp_gt_i32 vcc, 17, v1\n"
    "s_nop 1\n"
    "v_cndmask_b32 v25, v25, v5, vcc\n"
    "v_mov_b32 v41, v5\n"
    "v_cmp_gt_i32 vcc, 18, v1\n"
    "s_nop 1\n"
    "v_cndmask_b32 v26, v26, v5, vcc\n"
    "v_mov_b32 v42, v5\n"
    "v_cmp_gt_i32 vcc, 19, v1\n"
    "s_nop 1\n"
    "v_cndmask_b32 v27, v27, v5, vcc\n"
    "v_mov_b32 v43, v5\n"
    "v_cmp_gt_i32 vcc, 24, v1\n"
    "s_nop 1\n"
    "v_cndmask_b32 v28, v28, v5, vcc\n"
    "v_mov_b32 v44, v5\n"
    "v_cmp_gt_i32 vcc, 25, v1\n"
    "s_nop 1\n"
    "v_cndmask_b32 v29, v29, v5, vcc\n"
    "v_mov_b32 v45, v5\n"
    "v_cmp_gt_i32 vcc, 26, v1\n"
    "s_nop 1\n"
    "v_cndmask_b32 v30, v30, v5, vcc\n"
    "v_mov_b32 v46, v5\n"
    "v_cmp_gt_i32 vcc, 27, v1\n"
    "s_nop 1\n"
    "v_cndmask_b32 v31, v31, v5, vcc\n"
    "v_mov_b32 v47, v5\n"
    "s_branch .Lat_dgr5_%=\n"
    ".Lat_rs5_%=:\n"
    "v_max_f32 v12, 0, v12\n"
    "v_exp_f32 v5, -v12\n"
    "v_sub_f32 v16, v16, v12\n"
    "v_sub_f32 v32, v32, v12\n"
    "v_sub_f32 v80, v80, v12\n"
    "v_sub_f32 v17, v17, v12\n"
    "v_sub_f32 v33, v33, v12\n"
    "v_sub_f32 v81, v81, v12\n"
    "v_sub_f32 v18, v18, v12\n"
    "v_sub_f32 v34, v34, v12\n"
    "v_sub_f32 v82, v82, v12\n"
    "v_sub_f32 v19, v19, v12\n"
    "v_sub_f32 v35, v35, v12\n"
    "v_sub_f32 v83, v83, v12\n"
    "v_sub_f32 v20, v20, v12\n"
    "v_sub_f32 v36, v36, v12\n"
    "v_sub_f32 v84, v84, v12\n"
    "v_sub_f32 v21, v21, v12\n"
    "v_sub_f32 v37, v37, v12\n"
    "v_sub_f32 v85, v85, v12\n"
    "v_sub_f32 v22, v22, v12\n"
    "v_sub_f32 v38, v38, v12\n"
    "v_sub_f32 v86, v86, v12\n"
    "v_sub_f32 v23, v23, v12\n"
    "v_sub_f32 v39, v39, v12\n"
    "v_sub_f32 v87, v87, v12\n"
    "v_sub_f32 v24, v24, v12\n"
    "v_sub_f32 v40, v40, v12\n"
    "v_sub_f32 v88, v88, v12\n"
    "v_sub_f32 v25, v25, v12\n"
    "v_sub_f32 v41, v41, v12\n"
    "v_sub_f32 v89, v89, v12\n"
    "v_sub_f32 v26, v26, v12\n"
    "v_sub_f32 v42, v42, v12\n"
    "v_sub_f32 v90, v90, v12\n"
    "v_sub_f32 v27, v27, v12\n"
    "v_sub_f32 v43, v43, v12\n"
    "v_sub_f32 v91, v91, v12\n"
    "v_sub_f32 v28, v28, v12\n"
    "v_sub_f32 v44, v44, v12\n"
    "v_sub_f32 v92, v92, v12\n"
    "v_sub_f32 v29, v29, v12\n"
    "v_sub_f32 v45, v45, v12\n"
    "v_sub_f32 v93, v93, v12\n"
    "v_sub_f32 v30, v30, v12\n"
    "v_sub_f32 v46, v46, v12\n"
    "v_sub_f32 v94, v94, v12\n"
    "v_sub_f32 v31, v31, v12\n"
    "v_sub_f32 v47, v47, v12\n"
    "v_sub_f32 v95, v95, v12\n"
    "v_mul_f32 v8, v5, v8\n"
    "v_mul_f32 v9, v5, v9\n"
    "v_accvgpr_read_b32 v6, a0\n"
    "v_accvgpr_read_b32 v7, a1\n"
    "v_mul_f32 v6, v5, v6\n"
    "v_mul_f32 v7, v5, v7\n"
    "v_accvgpr_write_b32 a0, v6\n"
    "v_accvgpr_write_b32 a1, v7\n"
    "v_accvgpr_read_b32 v6, a2\n"
    "v_accvgpr_read_b32 v7, a3\n"
    "v_mul_f32 v6, v5, v6\n"
    "v_mul_f32 v7, v5, v7\n"
    "v_accvgpr_write_b32 a2, v6\n"
    "v_accvgpr_write_b32 a3, v7\n"
    "v_accvgpr_read_b32 v6, a4\n"
    "v_accvgpr_read_b32 v7, a5\n"
    "v_mul_f32 v6, v5, v6\n"
    "v_mul_f32 v7, v5, v7\n"
    "v_accvgpr_write_b32 a4, v6\n"
    "v_accvgpr_write_b32 a5, v7\n"
    "v_accvgpr_read_b32 v6, a6\n"
    "v_accvgpr_read_b32 v7, a7\n"
    "v_mul_f32 v6, v5, v6\n"
    "v_mul_f32 v7, v5, v7\n"
    "v_accvgpr_write_b32 a6, v6\n"
    "v_accvgpr_write_b32 a7, v7\n"
    "v_accvgpr_read_b32 v6, a8\n"
    "v_accvgpr_read_b32 v7, a9\n"
    "v_mul_f32 v6, v5, v6\n"
    "v_mul_f32 v7, v5, v7\n"
    "v_accvgpr_write_b32 a8, v6\n"
    "v_accvgpr_write_b32 a9, v7\n"
    "v_accvgpr_read_b32 v6, a10\n"
    "v_accvgpr_read_b32 v7, a11\n"
    "v_mul_f32 v6, v5, v6\n"
    "v_mul_f32 v7, v5, v7\n"
    "v_accvgpr_write_b32 a10, v6\n"
    "v_accvgpr_write_b32 a11, v7\n"
    "v_accvgpr_read_b32 v6, a12\n"
    "v_accvgpr_read_b32 v7, a13\n"
    "v_mul_f32 v6, v5, v6\n"
    "v_mul_f32 v7, v5, v7\n"
    "v_accvgpr_write_b32 a12, v6\n"
    "v_accvgpr_write_b32 a13, v7\n"
    "v_accvgpr_read_b32 v6, a14\n"
    "v_accvgpr_read_b32 v7, a15\n"
    "v_mul_f32 v6, v5, v6\n"
    "v_mul_f32 v7, v5, v7\n"
    "v_accvgpr_write_b32 a14, v6\n"
    "v_accvgpr_write_b32 a15, v7\n"
    "v_accvgpr_read_b32 v6, a16\n"
    "v_accvgpr_read_b32 v7, a17\n"
    "v_mul_f32 v6, v5, v6\n"
    "v_mul_f32 v7, v5, v7\n"
    "v_accvgpr_write_b32 a16, v6\n"
    "v_accvgpr_write_b32 a17, v7\n"
    "v_accvgpr_read_b32 v6, a18\n"
    "v_accvgpr_read_b32 v7, a19\n"
    "v_mul_f32 v6, v5, v6\n"
    "v_mul_f32 v7, v5, v7\n"
    "v_accvgpr_write_b32 a18, v6\n"
    "v_accvgpr_write_b32 a19, v7\n"
    "v_accvgpr_read_b32 v6, a20\n"
    "v_accvgpr_read_b32 v7, a21\n"
    "v_mul_f32 v6, v5, v6\n"
    "v_mul_f32 v7, v5, v7\n"
    "v_accvgpr_write_b32 a20, v6\n"
    "v_accvgpr_write_b32 a21, v7\n"
    "v_accvgpr_read_b32 v6, a22\n"
    "v_accvgpr_read_b32 v7, a23\n"
    "v_mul_f32 v6, v5, v6\n"
    "v_mul_f32 v7, v5, v7\n"
    "v_accvgpr_write_b32 a22, v6\n"
    "v_accvgpr_write_b32 a23, v7\n"
    "v_accvgpr_read_b32 v6, a24\n"
    "v_accvgpr_read_b32 v7, a25\n"
    "v_mul_f32 v6, v5, v6\n"
    "v_mul_f32 v7, v5, v7\n"
    "v_accvgpr_write_b32 a24, v6\n"
    "v_accvgpr_write_b32 a25, v7\n"
    "v_accvgpr_read_b32 v6, a26\n"
    "v_accvgpr_read_b32 v7, a27\n"
    "v_mul_f32 v6, v5, v6\n"
    "v_mul_f32 v7, v5, v7\n"
    "v_accvgpr_write_b32 a26, v6\n"
    "v_accvgpr_write_b32 a27, v7\n"
    "v_accvgpr_read_b32 v6, a28\n"
    "v_accvgpr_read_b32 v7, a29\n"
    "v_mul_f32 v6, v5, v6\n"
    "v_mul_f32 v7, v5, v7\n"
    "v_accvgpr_write_b32 a28, v6\n"
    "v_accvgpr_write_b32 a29, v7\n"
    "v_accvgpr_read_b32 v6, a30\n"
    "v_accvgpr_read_b32 v7, a31\n"
    "v_mul_f32 v6, v5, v6\n"
    "v_mul_f32 v7, v5, v7\n"
    "v_accvgpr_write_b32 a30, v6\n"
    "v_accvgpr_write_b32 a31, v7\n"
    "v_accvgpr_read_b32 v6, a32\n"
    "v_accvgpr_read_b32 v7, a33\n"
    "v_mul_f32 v6, v5, v6\n"
    "v_mul_f32 v7, v5, v7\n"
    "v_accvgpr_write_b32 a32, v6\n"
    "v_accvgpr_write_b32 a33, v7\n"
    "v_accvgpr_read_b32 v6, a34\n"
    "v_accvgpr_read_b32 v7, a35\n"
    "v_mul_f32 v6, v5, v6\n"
    "v_mul_f32 v7, v5, v7\n"
    "v_accvgpr_write_b32 a34, v6\n"
    "v_accvgpr_write_b32 a35, v7\n"
    "v_accvgpr_read_b32 v6, a36\n"
    "v_accvgpr_read_b32 v7, a37\n"
    "v_mul_f32 v6, v5, v6\n"
    "v_mul_f32 v7, v5, v7\n"
    "v_accvgpr_write_b32 a36, v6\n"
    "v_accvgpr_write_b32 a37, v7\n"
    "v_accvgpr_read_b32 v6, a38\n"
    "v_accvgpr_read_b32 v7, a39\n"
    "v_mul_f32 v6, v5, v6\n"
    "v_mul_f32 v7, v5, v7\n"
    "v_accvgpr_write_b32 a38, v6\n"
    "v_accvgpr_write_b32 a39, v7\n"
    "v_accvgpr_read_b32 v6, a40\n"
    "v_accvgpr_read_b32 v7, a41\n"
    "v_mul_f32 v6, v5, v6\n"
    "v_mul_f32 v7, v5, v7\n"
    "v_accvgpr_write_b32 a40, v6\n"
    "v_accvgpr_write_b32 a41, v7\n"
    "v_accvgpr_read_b32 v6, a42\n"
    "v_accvgpr_read_b32 v7, a43\n"
    "v_mul_f32 v6, v5, v6\n"
    "v_mul_f32 v7, v5, v7\n"
    "v_accvgpr_write_b32 a42, v6\n"
    "v_accvgpr_write_b32 a43, v7\n"
    "v_accvgpr_read_b32 v6, a44\n"
    "v_accvgpr_read_b32 v7, a45\n"
    "v_mul_f32 v6, v5, v6\n"
    "v_mul_f32 v7, v5, v7\n"
    "v_accvgpr_write_b32 a44, v6\n"
    "v_accvgpr_write_b32 a45, v7\n"
    "v_accvgpr_read_b32 v6, a46\n"
    "v_accvgpr_read_b32 v7, a47\n"
    "v_mul_f32 v6, v5, v6\n"
    "v_mul_f32 v7, v5, v7\n"
    "v_accvgpr_write_b32 a46, v6\n"
    "v_accvgpr_write_b32 a47, v7\n"
    "v_accvgpr_read_b32 v6, a48\n"
    "v_accvgpr_read_b32 v7, a49\n"
    "v_mul_f32 v6, v5, v6\n"
    "v_mul_f32 v7, v5, v7\n"
    "v_accvgpr_write_b32 a48, v6\n"
    "v_accvgpr_write_b32 a49, v7\n"
    "v_accvgpr_read_b32 v6, a50\n"
    "v_accvgpr_read_b32 v7, a51\n"
    "v_mul_f32 v6, v5, v6\n"
    "v_mul_f32 v7, v5, v7\n"
    "v_accvgpr_write_b32 a50, v6\n"
    "v_accvgpr_write_b32 a51, v7\n"
    "v_accvgpr_read_b32 v6, a52\n"
    "v_accvgpr_read_b32 v7, a53\n"
    "v_mul_f32 v6, v5, v6\n"
    "v_mul_f32 v7, v5, v7\n"
    "v_accvgpr_write_b32 a52, v6\n"
    "v_accvgpr_write_b32 a53, v7\n"
    "v_accvgpr_read_b32 v6, a54\n"
    "v_accvgpr_read_b32 v7, a55\n"
    "v_mul_f32 v6, v5, v6\n"
    "v_mul_f32 v7, v5, v7\n"
    "v_accvgpr_write_b32 a54, v6\n"
    "v_accvgpr_write_b32 a55, v7\n"
    "v_accvgpr_read_b32 v6, a56\n"
    "v_accvgpr_read_b32 v7, a57\n"
    "v_mul_f32 v6, v5, v6\n"
    "v_mul_f32 v7, v5, v7\n"
    "v_accvgpr_write_b32 a56, v6\n"
    "v_accvgpr_write_b32 a57, v7\n"
    "v_accvgpr_read_b32 v6, a58\n"
    "v_accvgpr_read_b32 v7, a59\n"
    "v_mul_f32 v6, v5, v6\n"
    "v_mul_f32 v7, v5, v7\n"
    "v_accvgpr_write_b32 a58, v6\n"
    "v_accvgpr_write_b32 a59, v7\n"
    "v_accvgpr_read_b32 v6, a60\n"
    "v_accvgpr_read_b32 v7, a61\n"
    "v_mul_f32 v6, v5, v6\n"
    "v_mul_f32 v7, v5, v7\n"
    "v_accvgpr_write_b32 a60, v6\n"
    "v_accvgpr_write_b32 a61, v7\n"
    "v_accvgpr_read_b32 v6, a62\n"
    "v_accvgpr_read_b32 v7, a63\n"
    "v_mul_f32 v6, v5, v6\n"
    "v_mul_f32 v7, v5, v7\n"
    "v_accvgpr_write_b32 a62, v6\n"
    "v_accvgpr_write_b32 a63, v7\n"
    "s_branch .Lat_rsr5_%=\n"
    ".Lat_dg6_%=:\n"
    "s_nop 11\n"
    "v_mov_b32 v5, 0xff800000\n"
    "v_cmp_gt_i32 vcc, 0, v1\n"
    "s_nop 1\n"
    "v_cndmask_b32 v64, v64, v5, vcc\n"
    "v_cmp_gt_i32 vcc, 1, v1\n"
    "s_nop 1\n"
    "v_cndmask_b32 v65, v65, v5, vcc\n"
    "v_cmp_gt_i32 vcc, 2, v1\n"
    "s_nop 1\n"
    "v_cndmask_b32 v66, v66, v5, vcc\n"
    "v_cmp_gt_i32 vcc, 3, v1\n"
    "s_nop 1\n"
    "v_cndmask_b32 v67, v67, v5, vcc\n"
    "v_cmp_gt_i32 vcc, 8, v1\n"
    "s_nop 1\n"
    "v_cndmask_b32 v68, v68, v5, vcc\n"
    "v_cmp_gt_i32 vcc, 9, v1\n"
    "s_nop 1\n"
    "v_cndmask_b32 v69, v69, v5, vcc\n"
    "v_cmp_gt_i32 vcc, 10, v1\n"
    "s_nop 1\n"
    "v_cndmask_b32 v70, v70, v5, vcc\n"
    "v_cmp_gt_i32 vcc, 11, v1\n"
    "s_nop 1\n"
    "v_cndmask_b32 v71, v71, v5, vcc\n"
    "v_cmp_gt_i32 vcc, 16, v1\n"
    "s_nop 1\n"
    "v_cndmask_b32 v72, v72, v5, vcc\n"
    "v_cmp_gt_i32 vcc, 17, v1\n"
    "s_nop 1\n"
    "v_cndmask_b32 v73, v73, v5, vcc\n"
    "v_cmp_gt_i32 vcc, 18, v1\n"
    "s_nop 1\n"
    "v_cndmask_b32 v74, v74, v5, vcc\n"
    "v_cmp_gt_i32 vcc, 19, v1\n"
    "s_nop 1\n"
    "v_cndmask_b32 v75, v75, v5, vcc\n"
    "v_cmp_gt_i32 vcc, 24, v1\n"
    "s_nop 1\n"
    "v_cndmask_b32 v76, v76, v5, vcc\n"
    "v_cmp_gt_i32 vcc, 25, v1\n"
    "s_nop 1\n"
    "v_cndmask_b32 v77, v77, v5, vcc\n"
    "v_cmp_gt_i32 vcc, 26, v1\n"
    "s_nop 1\n"
    "v_cndmask_b32 v78, v78, v5, vcc\n"
    "v_cmp_gt_i32 vcc, 27, v1\n"
    "s_nop 1\n"
    "v_cndmask_b32 v79, v79, v5, vcc\n"
    "s_branch .Lat_dgr6_%=\n"
    ".Lat_rs6_%=:\n"
    "v_max_f32 v14, 0, v14\n"
    "v_exp_f32 v5, -v14\n"
    "v_sub_f32 v48, v48, v14\n"
    "v_sub_f32 v64, v64, v14\n"
    "v_sub_f32 v96, v96, v14\n"
    "v_sub_f32 v49, v49, v14\n"
    "v_sub_f32 v65, v65, v14\n"
    "v_sub_f32 v97, v97, v14\n"
    "v_sub_f32 v50, v50, v14\n"
    "v_sub_f32 v66, v66, v14\n"
    "v_sub_f32 v98, v98, v14\n"
    "v_sub_f32 v51, v51, v14\n"
    "v_sub_f32 v67, v67, v14\n"
    "v_sub_f32 v99, v99, v14\n"
    "v_sub_f32 v52, v52, v14\n"
    "v_sub_f32 v68, v68, v14\n"
    "v_sub_f32 v100, v100, v14\n"
    "v_sub_f32 v53, v53, v14\n"
    "v_sub_f32 v69, v69, v14\n"
    "v_sub_f32 v101, v101, v14\n"
    "v_sub_f32 v54, v54, v14\n"
    "v_sub_f32 v70, v70, v14\n"
    "v_sub_f32 v102, v102, v14\n"
    "v_sub_f32 v55, v55, v14\n"
    "v_sub_f32 v71, v71, v14\n"
    "v_sub_f32 v103, v103, v14\n"
    "v_sub_f32 v56, v56, v14\n"
    "v_sub_f32 v72, v72, v14\n"
    "v_sub_f32 v104, v104, v14\n"
    "v_sub_f32 v57, v57, v14\n"
    "v_sub_f32 v73, v73, v14\n"
    "v_sub_f32 v105, v105, v14\n"
    "v_sub_f32 v58, v58, v14\n"
    "v_sub_f32 v74, v74, v14\n"
    "v_sub_f32 v106, v106, v14\n"
    "v_sub_f32 v59, v59, v14\n"
    "v_sub_f32 v75, v75, v14\n"
    "v_sub_f32 v107, v107, v14\n"
    "v_sub_f32 v60, v60, v14\n"
    "v_sub_f32 v76, v76, v14\n"
    "v_sub_f32 v108, v108, v14\n"
    "v_sub_f32 v61, v61, v14\n"
    "v_sub_f32 v77, v77, v14\n"
    "v_sub_f32 v109, v109, v14\n"
    "v_sub_f32 v62, v62, v14\n"
    "v_sub_f32 v78, v78, v14\n"
    "v_sub_f32 v110, v110, v14\n"
    "v_sub_f32 v63, v63, v14\n"
    "v_sub_f32 v79, v79, v14\n"
    "v_sub_f32 v111, v111, v14\n"
    "v_mul_f32 v10, v5, v10\n"
    "v_mul_f32 v11, v5, v11\n"
    "v_accvgpr_read_b32 v6, a64\n"
    "v_accvgpr_read_b32 v7, a65\n"
    "v_mul_f32 v6, v5, v6\n"
    "v_mul_f32 v7, v5, v7\n"
    "v_accvgpr_write_b32 a64, v6\n"
    "v_accvgpr_write_b32 a65, v7\n"
    "v_accvgpr_read_b32 v6, a66\n"
    "v_accvgpr_read_b32 v7, a67\n"
    "v_mul_f32 v6, v5, v6\n"
    "v_mul_f32 v7, v5, v7\n"
    "v_accvgpr_write_b32 a66, v6\n"
    "v_accvgpr_write_b32 a67, v7\n"
    "v_accvgpr_read_b32 v6, a68\n"
    "v_accvgpr_read_b32 v7, a69\n"
    "v_mul_f32 v6, v5, v6\n"
    "v_mul_f32 v7, v5, v7\n"
    "v_accvgpr_write_b32 a68, v6\n"
    "v_accvgpr_write_b32 a69, v7\n"
    "v_accvgpr_read_b32 v6, a70\n"
    "v_accvgpr_read_b32 v7, a71\n"
    "v_mul_f32 v6, v5, v6\n"
    "v_mul_f32 v7, v5, v7\n"
    "v_accvgpr_write_b32 a70, v6\n"
    "v_accvgpr_write_b32 a71, v7\n"
    "v_accvgpr_read_b32 v6, a72\n"
    "v_accvgpr_read_b32 v7, a73\n"
    "v_mul_f32 v6, v5, v6\n"
    "v_mul_f32 v7, v5, v7\n"
    "v_accvgpr_write_b32 a72, v6\n"
    "v_accvgpr_write_b32 a73, v7\n"
    "v_accvgpr_read_b32 v6, a74\n"
    "v_accvgpr_read_b32 v7, a75\n"
    "v_mul_f32 v6, v5, v6\n"
    "v_mul_f32 v7, v5, v7\n"
    "v_accvgpr_write_b32 a74, v6\n"
    "v_accvgpr_write_b32 a75, v7\n"
    "v_accvgpr_read_b32 v6, a76\n"
    "v_accvgpr_read_b32 v7, a77\n"
    "v_mul_f32 v6, v5, v6\n"
    "v_mul_f32 v7, v5, v7\n"
    "v_accvgpr_write_b32 a76, v6\n"
    "v_accvgpr_write_b32 a77, v7\n"
    "v_accvgpr_read_b32 v6, a78\n"
    "v_accvgpr_read_b32 v7, a79\n"
    "v_mul_f32 v6, v5, v6\n"
    "v_mul_f32 v7, v5, v7\n"
    "v_accvgpr_write_b32 a78, v6\n"
    "v_accvgpr_write_b32 a79, v7\n"
    "v_accvgpr_read_b32 v6, a80\n"
    "v_accvgpr_read_b32 v7, a81\n"
    "v_mul_f32 v6, v5, v6\n"
    "v_mul_f32 v7, v5, v7\n"
    "v_accvgpr_write_b32 a80, v6\n"
    "v_accvgpr_write_b32 a81, v7\n"
    "v_accvgpr_read_b32 v6, a82\n"
    "v_accvgpr_read_b32 v7, a83\n"
    "v_mul_f32 v6, v5, v6\n"
    "v_mul_f32 v7, v5, v7\n"
    "v_accvgpr_write_b32 a82, v6\n"
    "v_accvgpr_write_b32 a83, v7\n"
    "v_accvgpr_read_b32 v6, a84\n"
    "v_accvgpr_read_b32 v7, a85\n"
    "v_mul_f32 v6, v5, v6\n"
    "v_mul_f32 v7, v5, v7\n"
    "v_accvgpr_write_b32 a84, v6\n"
    "v_accvgpr_write_b32 a85, v7\n"
    "v_accvgpr_read_b32 v6, a86\n"
    "v_accvgpr_read_b32 v7, a87\n"
    "v_mul_f32 v6, v5, v6\n"
    "v_mul_f32 v7, v5, v7\n"
    "v_accvgpr_write_b32 a86, v6\n"
    "v_accvgpr_write_b32 a87, v7\n"
    "v_accvgpr_read_b32 v6, a88\n"
    "v_accvgpr_read_b32 v7, a89\n"
    "v_mul_f32 v6, v5, v6\n"
    "v_mul_f32 v7, v5, v7\n"
    "v_accvgpr_write_b32 a88, v6\n"
    "v_accvgpr_write_b32 a89, v7\n"
    "v_accvgpr_read_b32 v6, a90\n"
    "v_accvgpr_read_b32 v7, a91\n"
    "v_mul_f32 v6, v5, v6\n"
    "v_mul_f32 v7, v5, v7\n"
    "v_accvgpr_write_b32 a90, v6\n"
    "v_accvgpr_write_b32 a91, v7\n"
    "v_accvgpr_read_b32 v6, a92\n"
    "v_accvgpr_read_b32 v7, a93\n"
    "v_mul_f32 v6, v5, v6\n"
    "v_mul_f32 v7, v5, v7\n"
    "v_accvgpr_write_b32 a92, v6\n"
    "v_accvgpr_write_b32 a93, v7\n"
    "v_accvgpr_read_b32 v6, a94\n"
    "v_accvgpr_read_b32 v7, a95\n"
    "v_mul_f32 v6, v5, v6\n"
    "v_mul_f32 v7, v5, v7\n"
    "v_accvgpr_write_b32 a94, v6\n"
    "v_accvgpr_write_b32 a95, v7\n"
    "v_accvgpr_read_b32 v6, a96\n"
    "v_accvgpr_read_b32 v7, a97\n"
    "v_mul_f32 v6, v5, v6\n"
    "v_mul_f32 v7, v5, v7\n"
    "v_accvgpr_write_b32 a96, v6\n"
    "v_accvgpr_write_b32 a97, v7\n"
    "v_accvgpr_read_b32 v6, a98\n"
    "v_accvgpr_read_b32 v7, a99\n"
    "v_mul_f32 v6, v5, v6\n"
    "v_mul_f32 v7, v5, v7\n"
    "v_accvgpr_write_b32 a98, v6\n"
    "v_accvgpr_write_b32 a99, v7\n"
    "v_accvgpr_read_b32 v6, a100\n"
    "v_accvgpr_read_b32 v7, a101\n"
    "v_mul_f32 v6, v5, v6\n"
    "v_mul_f32 v7, v5, v7\n"
    "v_accvgpr_write_b32 a100, v6\n"
    "v_accvgpr_write_b32 a101, v7\n"
    "v_accvgpr_read_b32 v6, a102\n"
    "v_accvgpr_read_b32 v7, a103\n"
    "v_mul_f32 v6, v5, v6\n"
    "v_mul_f32 v7, v5, v7\n"
    "v_accvgpr_write_b32 a102, v6\n"
    "v_accvgpr_write_b32 a103, v7\n"
    "v_accvgpr_read_b32 v6, a104\n"
    "v_accvgpr_read_b32 v7, a105\n"
    "v_mul_f32 v6, v5, v6\n"
    "v_mul_f32 v7, v5, v7\n"
    "v_accvgpr_write_b32 a104, v6\n"
    "v_accvgpr_write_b32 a105, v7\n"
    "v_accvgpr_read_b32 v6, a106\n"
    "v_accvgpr_read_b32 v7, a107\n"
    "v_mul_f32 v6, v5, v6\n"
    "v_mul_f32 v7, v5, v7\n"
    "v_accvgpr_write_b32 a106, v6\n"
    "v_accvgpr_write_b32 a107, v7\n"
    "v_accvgpr_read_b32 v6, a108\n"
    "v_accvgpr_read_b32 v7, a109\n"
    "v_mul_f32 v6, v5, v6\n"
    "v_mul_f32 v7, v5, v7\n"
    "v_accvgpr_write_b32 a108, v6\n"
    "v_accvgpr_write_b32 a109, v7\n"
    "v_accvgpr_read_b32 v6, a110\n"
    "v_accvgpr_read_b32 v7, a111\n"
    "v_mul_f32 v6, v5, v6\n"
    "v_mul_f32 v7, v5, v7\n"
    "v_accvgpr_write_b32 a110, v6\n"
    "v_accvgpr_write_b32 a111, v7\n"
    "v_accvgpr_read_b32 v6, a112\n"
    "v_accvgpr_read_b32 v7, a113\n"
    "v_mul_f32 v6, v5, v6\n"
    "v_mul_f32 v7, v5, v7\n"
    "v_accvgpr_write_b32 a112, v6\n"
    "v_accvgpr_write_b32 a113, v7\n"
    "v_accvgpr_read_b32 v6, a114\n"
    "v_accvgpr_read_b32 v7, a115\n"
    "v_mul_f32 v6, v5, v6\n"
    "v_mul_f32 v7, v5, v7\n"
    "v_accvgpr_write_b32 a114, v6\n"
    "v_accvgpr_write_b32 a115, v7\n"
    "v_accvgpr_read_b32 v6, a116\n"
    "v_accvgpr_read_b32 v7, a117\n"
    "v_mul_f32 v6, v5, v6\n"
    "v_mul_f32 v7, v5, v7\n"
    "v_accvgpr_write_b32 a116, v6\n"
    "v_accvgpr_write_b32 a117, v7\n"
    "v_accvgpr_read_b32 v6, a118\n"
    "v_accvgpr_read_b32 v7, a119\n"
    "v_mul_f32 v6, v5, v6\n"
    "v_mul_f32 v7, v5, v7\n"
    "v_accvgpr_write_b32 a118, v6\n"
    "v_accvgpr_write_b32 a119, v7\n"
    "v_accvgpr_read_b32 v6, a120\n"
    "v_accvgpr_read_b32 v7, a121\n"
    "v_mul_f32 v6, v5, v6\n"
    "v_mul_f32 v7, v5, v7\n"
    "v_accvgpr_write_b32 a120, v6\n"
    "v_accvgpr_write_b32 a121, v7\n"
    "v_accvgpr_read_b32 v6, a122\n"
    "v_accvgpr_read_b32 v7, a123\n"
    "v_mul_f32 v6, v5, v6\n"
    "v_mul_f32 v7, v5, v7\n"
    "v_accvgpr_write_b32 a122, v6\n"
    "v_accvgpr_write_b32 a123, v7\n"
    "v_accvgpr_read_b32 v6, a124\n"
    "v_accvgpr_read_b32 v7, a125\n"
    "v_mul_f32 v6, v5, v6\n"
    "v_mul_f32 v7, v5, v7\n"
    "v_accvgpr_write_b32 a124, v6\n"
    "v_accvgpr_write_b32 a125, v7\n"
    "v_accvgpr_read_b32 v6, a126\n"
    "v_accvgpr_read_b32 v7, a127\n"
    "v_mul_f32 v6, v5, v6\n"
    "v_mul_f32 v7, v5, v7\n"
    "v_accvgpr_write_b32 a126, v6\n"
    "v_accvgpr_write_b32 a127, v7\n"
    "s_branch .Lat_rsr6_%=\n"
    ".Lat_dg7_%=:\n"
    "s_nop 11\n"
    "v_mov_b32 v5, 0xff800000\n"
    "v_cmp_gt_i32 vcc, 0, v1\n"
    "s_nop 1\n"
    "v_cndmask_b32 v16, v16, v5, vcc\n"
    "v_mov_b32 v32, v5\n"
    "v_cmp_gt_i32 vcc, 1, v1\n"
    "s_nop 1\n"
    "v_cndmask_b32 v17, v17, v5, vcc\n"
    "v_mov_b32 v33, v5\n"
    "v_cmp_gt_i32 vcc, 2, v1\n"
    "s_nop 1\n"
    "v_cndmask_b32 v18, v18, v5, vcc\n"
    "v_mov_b32 v34, v5\n"
    "v_cmp_gt_i32 vcc, 3, v1\n"
    "s_nop 1\n"
    "v_cndmask_b32 v19, v19, v5, vcc\n"
    "v_mov_b32 v35, v5\n"
    "v_cmp_gt_i32 vcc, 8, v1\n"
    "s_nop 1\n"
    "v_cndmask_b32 v20, v20, v5, vcc\n"
    "v_mov_b32 v36, v5\n"
    "v_cmp_gt_i32 vcc, 9, v1\n"
    "s_nop 1\n"
    "v_cndmask_b32 v21, v21, v5, vcc\n"
    "v_mov_b32 v37, v5\n"
    "v_cmp_gt_i32 vcc, 10, v1\n"
    "s_nop 1\n"
    "v_cndmask_b32 v22, v22, v5, vcc\n"
    "v_mov_b32 v38, v5\n"
    "v_cmp_gt_i32 vcc, 11, v1\n"
    "s_nop 1\n"
    "v_cndmask_b32 v23, v23, v5, vcc\n"
    "v_mov_b32 v39, v5\n"
    "v_cmp_gt_i32 vcc, 16, v1\n"
    "s_nop 1\n"
    "v_cndmask_b32 v24, v24, v5, vcc\n"
    "v_mov_b32 v40, v5\n"
    "v_cmp_gt_i32 vcc, 17, v1\n"
    "s_nop 1\n"
    "v_cndmask_b32 v25, v25, v5, vcc\n"
    "v_mov_b32 v41, v5\n"
    "v_cmp_gt_i32 vcc, 18, v1\n"
    "s_nop 1\n"
    "v_cndmask_b32 v26, v26, v5, vcc\n"
    "v_mov_b32 v42, v5\n"
    "v_cmp_gt_i32 vcc, 19, v1\n"
    "s_nop 1\n"
    "v_cndmask_b32 v27, v27, v5, vcc\n"
    "v_mov_b32 v43, v5\n"
    "v_cmp_gt_i32 vcc, 24, v1\n"
    "s_nop 1\n"
    "v_cndmask_b32 v28, v28, v5, vcc\n"
    "v_mov_b32 v44, v5\n"
    "v_cmp_gt_i32 vcc, 25, v1\n"
    "s_nop 1\n"
    "v_cndmask_b32 v29, v29, v5, vcc\n"
    "v_mov_b32 v45, v5\n"
    "v_cmp_gt_i32 vcc, 26, v1\n"
    "s_nop 1\n"
    "v_cndmask_b32 v30, v30, v5, vcc\n"
    "v_mov_b32 v46, v5\n"
    "v_cmp_gt_i32 vcc, 27, v1\n"
    "s_nop 1\n"
    "v_cndmask_b32 v31, v31, v5, vcc\n"
    "v_mov_b32 v47, v5\n"
    "s_branch .Lat_dgr7_%=\n"
    ".Lat_rs7_%=:\n"
    "v_max_f32 v12, 0, v12\n"
    "v_exp_f32 v5, -v12\n"
    "v_sub_f32 v16, v16, v12\n"
    "v_sub_f32 v32, v32, v12\n"
    "v_sub_f32 v80, v80, v12\n"
    "v_sub_f32 v17, v17, v12\n"
    "v_sub_f32 v33, v33, v12\n"
    "v_sub_f32 v81, v81, v12\n"
    "v_sub_f32 v18, v18, v12\n"
    "v_sub_f32 v34, v34, v12\n"
    "v_sub_f32 v82, v82, v12\n"
    "v_sub_f32 v19, v19, v12\n"
    "v_sub_f32 v35, v35, v12\n"
    "v_sub_f32 v83, v83, v12\n"
    "v_sub_f32 v20, v20, v12\n"
    "v_sub_f32 v36, v36, v12\n"
    "v_sub_f32 v84, v84, v12\n"
    "v_sub_f32 v21, v21, v12\n"
    "v_sub_f32 v37, v37, v12\n"
    "v_sub_f32 v85, v85, v12\n"
    "v_sub_f32 v22, v22, v12\n"
    "v_sub_f32 v38, v38, v12\n"
    "v_sub_f32 v86, v86, v12\n"
    "v_sub_f32 v23, v23, v12\n"
    "v_sub_f32 v39, v39, v12\n"
    "v_sub_f32 v87, v87, v12\n"
    "v_sub_f32 v24, v24, v12\n"
    "v_sub_f32 v40, v40, v12\n"
    "v_sub_f32 v88, v88, v12\n"
    "v_sub_f32 v25, v25, v12\n"
    "v_sub_f32 v41, v41, v12\n"
    "v_sub_f32 v89, v89, v12\n"
    "v_sub_f32 v26, v26, v12\n"
    "v_sub_f32 v42, v42, v12\n"
    "v_sub_f32 v90, v90, v12\n"
    "v_sub_f32 v27, v27, v12\n"
    "v_sub_f32 v43, v43, v12\n"
    "v_sub_f32 v91, v91, v12\n"
    "v_sub_f32 v28, v28, v12\n"
    "v_sub_f32 v44, v44, v12\n"
    "v_sub_f32 v92, v92, v12\n"
    "v_sub_f32 v29, v29, v12\n"
    "v_sub_f32 v45, v45, v12\n"
    "v_sub_f32 v93, v93, v12\n"
    "v_sub_f32 v30, v30, v12\n"
    "v_sub_f32 v46, v46, v12\n"
    "v_sub_f32 v94, v94, v12\n"
    "v_sub_f32 v31, v31, v12\n"
    "v_sub_f32 v47, v47, v12\n"
    "v_sub_f32 v95, v95, v12\n"
    "v_mul_f32 v8, v5, v8\n"
    "v_mul_f32 v9, v5, v9\n"
    "v_accvgpr_read_b32 v6, a0\n"
    "v_accvgpr_read_b32 v7, a1\n"
    "v_mul_f32 v6, v5, v6\n"
    "v_mul_f32 v7, v5, v7\n"
    "v_accvgpr_write_b32 a0, v6\n"
    "v_accvgpr_write_b32 a1, v7\n"
    "v_accvgpr_read_b32 v6, a2\n"
    "v_accvgpr_read_b32 v7, a3\n"
    "v_mul_f32 v6, v5, v6\n"
    "v_mul_f32 v7, v5, v7\n"
    "v_accvgpr_write_b32 a2, v6\n"
    "v_accvgpr_write_b32 a3, v7\n"
    "v_accvgpr_read_b32 v6, a4\n"
    "v_accvgpr_read_b32 v7, a5\n"
    "v_mul_f32 v6, v5, v6\n"
    "v_mul_f32 v7, v5, v7\n"
    "v_accvgpr_write_b32 a4, v6\n"
    "v_accvgpr_write_b32 a5, v7\n"
    "v_accvgpr_read_b32 v6, a6\n"
    "v_accvgpr_read_b32 v7, a7\n"
    "v_mul_f32 v6, v5, v6\n"
    "v_mul_f32 v7, v5, v7\n"
    "v_accvgpr_write_b32 a6, v6\n"
    "v_accvgpr_write_b32 a7, v7\n"
    "v_accvgpr_read_b32 v6, a8\n"
    "v_accvgpr_read_b32 v7, a9\n"
    "v_mul_f32 v6, v5, v6\n"
    "v_mul_f32 v7, v5, v7\n"
    "v_accvgpr_write_b32 a8, v6\n"
    "v_accvgpr_write_b32 a9, v7\n"
    "v_accvgpr_read_b32 v6, a10\n"
    "v_accvgpr_read_b32 v7, a11\n"
    "v_mul_f32 v6, v5, v6\n"
    "v_mul_f32 v7, v5, v7\n"
    "v_accvgpr_write_b32 a10, v6\n"
    "v_accvgpr_write_b32 a11, v7\n"
    "v_accvgpr_read_b32 v6, a12\n"
    "v_accvgpr_read_b32 v7, a13\n"
    "v_mul_f32 v6, v5, v6\n"
    "v_mul_f32 v7, v5, v7\n"
    "v_accvgpr_write_b32 a12, v6\n"
    "v_accvgpr_write_b32 a13, v7\n"
    "v_accvgpr_read_b32 v6, a14\n"
    "v_accvgpr_read_b32 v7, a15\n"
    "v_mul_f32 v6, v5, v6\n"
    "v_mul_f32 v7, v5, v7\n"
    "v_accvgpr_write_b32 a14, v6\n"
    "v_accvgpr_write_b32 a15, v7\n"
    "v_accvgpr_read_b32 v6, a16\n"
    "v_accvgpr_read_b32 v7, a17\n"
    "v_mul_f32 v6, v5, v6\n"
    "v_mul_f32 v7, v5, v7\n"
    "v_accvgpr_write_b32 a16, v6\n"
    "v_accvgpr_write_b32 a17, v7\n"
    "v_accvgpr_read_b32 v6, a18\n"
    "v_accvgpr_read_b32 v7, a19\n"
    "v_mul_f32 v6, v5, v6\n"
    "v_mul_f32 v7, v5, v7\n"
    "v_accvgpr_write_b32 a18, v6\n"
    "v_accvgpr_write_b32 a19, v7\n"
    "v_accvgpr_read_b32 v6, a20\n"
    "v_accvgpr_read_b32 v7, a21\n"
    "v_mul_f32 v6, v5, v6\n"
    "v_mul_f32 v7, v5, v7\n"
    "v_accvgpr_write_b32 a20, v6\n"
    "v_accvgpr_write_b32 a21, v7\n"
    "v_accvgpr_read_b32 v6, a22\n"
    "v_accvgpr_read_b32 v7, a23\n"
    "v_mul_f32 v6, v5, v6\n"
    "v_mul_f32 v7, v5, v7\n"
    "v_accvgpr_write_b32 a22, v6\n"
    "v_accvgpr_write_b32 a23, v7\n"
    "v_accvgpr_read_b32 v6, a24\n"
    "v_accvgpr_read_b32 v7, a25\n"
    "v_mul_f32 v6, v5, v6\n"
    "v_mul_f32 v7, v5, v7\n"
    "v_accvgpr_write_b32 a24, v6\n"
    "v_accvgpr_write_b32 a25, v7\n"
    "v_accvgpr_read_b32 v6, a26\n"
    "v_accvgpr_read_b32 v7, a27\n"
    "v_mul_f32 v6, v5, v6\n"
    "v_mul_f32 v7, v5, v7\n"
    "v_accvgpr_write_b32 a26, v6\n"
    "v_accvgpr_write_b32 a27, v7\n"
    "v_accvgpr_read_b32 v6, a28\n"
    "v_accvgpr_read_b32 v7, a29\n"
    "v_mul_f32 v6, v5, v6\n"
    "v_mul_f32 v7, v5, v7\n"
    "v_accvgpr_write_b32 a28, v6\n"
    "v_accvgpr_write_b32 a29, v7\n"
    "v_accvgpr_read_b32 v6, a30\n"
    "v_accvgpr_read_b32 v7, a31\n"
    "v_mul_f32 v6, v5, v6\n"
    "v_mul_f32 v7, v5, v7\n"
    "v_accvgpr_write_b32 a30, v6\n"
    "v_accvgpr_write_b32 a31, v7\n"
    "v_accvgpr_read_b32 v6, a32\n"
    "v_accvgpr_read_b32 v7, a33\n"
    "v_mul_f32 v6, v5, v6\n"
    "v_mul_f32 v7, v5, v7\n"
    "v_accvgpr_write_b32 a32, v6\n"
    "v_accvgpr_write_b32 a33, v7\n"
    "v_accvgpr_read_b32 v6, a34\n"
    "v_accvgpr_read_b32 v7, a35\n"
    "v_mul_f32 v6, v5, v6\n"
    "v_mul_f32 v7, v5, v7\n"
    "v_accvgpr_write_b32 a34, v6\n"
    "v_accvgpr_write_b32 a35, v7\n"
    "v_accvgpr_read_b32 v6, a36\n"
    "v_accvgpr_read_b32 v7, a37\n"
    "v_mul_f32 v6, v5, v6\n"
    "v_mul_f32 v7, v5, v7\n"
    "v_accvgpr_write_b32 a36, v6\n"
    "v_accvgpr_write_b32 a37, v7\n"
    "v_accvgpr_read_b32 v6, a38\n"
    "v_accvgpr_read_b32 v7, a39\n"
    "v_mul_f32 v6, v5, v6\n"
    "v_mul_f32 v7, v5, v7\n"
    "v_accvgpr_write_b32 a38, v6\n"
    "v_accvgpr_write_b32 a39, v7\n"
    "v_accvgpr_read_b32 v6, a40\n"
    "v_accvgpr_read_b32 v7, a41\n"
    "v_mul_f32 v6, v5, v6\n"
    "v_mul_f32 v7, v5, v7\n"
    "v_accvgpr_write_b32 a40, v6\n"
    "v_accvgpr_write_b32 a41, v7\n"
    "v_accvgpr_read_b32 v6, a42\n"
    "v_accvgpr_read_b32 v7, a43\n"
    "v_mul_f32 v6, v5, v6\n"
    "v_mul_f32 v7, v5, v7\n"
    "v_accvgpr_write_b32 a42, v6\n"
    "v_accvgpr_write_b32 a43, v7\n"
    "v_accvgpr_read_b32 v6, a44\n"
    "v_accvgpr_read_b32 v7, a45\n"
    "v_mul_f32 v6, v5, v6\n"
    "v_mul_f32 v7, v5, v7\n"
    "v_accvgpr_write_b32 a44, v6\n"
    "v_accvgpr_write_b32 a45, v7\n"
    "v_accvgpr_read_b32 v6, a46\n"
    "v_accvgpr_read_b32 v7, a47\n"
    "v_mul_f32 v6, v5, v6\n"
    "v_mul_f32 v7, v5, v7\n"
    "v_accvgpr_write_b32 a46, v6\n"
    "v_accvgpr_write_b32 a47, v7\n"
    "v_accvgpr_read_b32 v6, a48\n"
    "v_accvgpr_read_b32 v7, a49\n"
    "v_mul_f32 v6, v5, v6\n"
    "v_mul_f32 v7, v5, v7\n"
    "v_accvgpr_write_b32 a48, v6\n"
    "v_accvgpr_write_b32 a49, v7\n"
    "v_accvgpr_read_b32 v6, a50\n"
    "v_accvgpr_read_b32 v7, a51\n"
    "v_mul_f32 v6, v5, v6\n"
    "v_mul_f32 v7, v5, v7\n"
    "v_accvgpr_write_b32 a50, v6\n"
    "v_accvgpr_write_b32 a51, v7\n"
    "v_accvgpr_read_b32 v6, a52\n"
    "v_accvgpr_read_b32 v7, a53\n"
    "v_mul_f32 v6, v5, v6\n"
    "v_mul_f32 v7, v5, v7\n"
    "v_accvgpr_write_b32 a52, v6\n"
    "v_accvgpr_write_b32 a53, v7\n"
    "v_accvgpr_read_b32 v6, a54\n"
    "v_accvgpr_read_b32 v7, a55\n"
    "v_mul_f32 v6, v5, v6\n"
    "v_mul_f32 v7, v5, v7\n"
    "v_accvgpr_write_b32 a54, v6\n"
    "v_accvgpr_write_b32 a55, v7\n"
    "v_accvgpr_read_b32 v6, a56\n"
    "v_accvgpr_read_b32 v7, a57\n"
    "v_mul_f32 v6, v5, v6\n"
    "v_mul_f32 v7, v5, v7\n"
    "v_accvgpr_write_b32 a56, v6\n"
    "v_accvgpr_write_b32 a57, v7\n"
    "v_accvgpr_read_b32 v6, a58\n"
    "v_accvgpr_read_b32 v7, a59\n"
    "v_mul_f32 v6, v5, v6\n"
    "v_mul_f32 v7, v5, v7\n"
    "v_accvgpr_write_b32 a58, v6\n"
    "v_accvgpr_write_b32 a59, v7\n"
    "v_accvgpr_read_b32 v6, a60\n"
    "v_accvgpr_read_b32 v7, a61\n"
    "v_mul_f32 v6, v5, v6\n"
    "v_mul_f32 v7, v5, v7\n"
    "v_accvgpr_write_b32 a60, v6\n"
    "v_accvgpr_write_b32 a61, v7\n"
    "v_accvgpr_read_b32 v6, a62\n"
    "v_accvgpr_read_b32 v7, a63\n"
    "v_mul_f32 v6, v5, v6\n"
    "v_mul_f32 v7, v5, v7\n"
    "v_accvgpr_write_b32 a62, v6\n"
    "v_accvgpr_write_b32 a63, v7\n"
    "s_branch .Lat_rsr7_%=\n"
    ".Lat_dg8_%=:\n"
    "s_nop 11\n"
    "v_mov_b32 v5, 0xff800000\n"
    "v_cmp_gt_i32 vcc, 0, v1\n"
    "s_nop 1\n"
    "v_cndmask_b32 v64, v64, v5, vcc\n"
    "v_cmp_gt_i32 vcc, 1, v1\n"
    "s_nop 1\n"
    "v_cndmask_b32 v65, v65, v5, vcc\n"
    "v_cmp_gt_i32 vcc, 2, v1\n"
    "s_nop 1\n"
    "v_cndmask_b32 v66, v66, v5, vcc\n"
    "v_cmp_gt_i32 vcc, 3, v1\n"
    "s_nop 1\n"
    "v_cndmask_b32 v67, v67, v5, vcc\n"
    "v_cmp_gt_i32 vcc, 8, v1\n"
    "s_nop 1\n"
    "v_cndmask_b32 v68, v68, v5, vcc\n"
    "v_cmp_gt_i32 vcc, 9, v1\n"
    "s_nop 1\n"
    "v_cndmask_b32 v69, v69, v5, vcc\n"
    "v_cmp_gt_i32 vcc, 10, v1\n"
    "s_nop 1\n"
    "v_cndmask_b32 v70, v70, v5, vcc\n"
    "v_cmp_gt_i32 vcc, 11, v1\n"
    "s_nop 1\n"
    "v_cndmask_b32 v71, v71, v5, vcc\n"
    "v_cmp_gt_i32 vcc, 16, v1\n"
    "s_nop 1\n"
    "v_cndmask_b32 v72, v72, v5, vcc\n"
    "v_cmp_gt_i32 vcc, 17, v1\n"
    "s_nop 1\n"
    "v_cndmask_b32 v73, v73, v5, vcc\n"
    "v_cmp_gt_i32 vcc, 18, v1\n"
    "s_nop 1\n"
    "v_cndmask_b32 v74, v74, v5, vcc\n"
    "v_cmp_gt_i32 vcc, 19, v1\n"
    "s_nop 1\n"
    "v_cndmask_b32 v75, v75, v5, vcc\n"
    "v_cmp_gt_i32 vcc, 24, v1\n"
    "s_nop 1\n"
    "v_cndmask_b32 v76, v76, v5, vcc\n"
    "v_cmp_gt_i32 vcc, 25, v1\n"
    "s_nop 1\n"
    "v_cndmask_b32 v77, v77, v5, vcc\n"
    "v_cmp_gt_i32 vcc, 26, v1\n"
    "s_nop 1\n"
    "v_cndmask_b32 v78, v78, v5, vcc\n"
    "v_cmp_gt_i32 vcc, 27, v1\n"
    "s_nop 1\n"
    "v_cndmask_b32 v79, v79, v5, vcc\n"
    "s_branch .Lat_dgr8_%=\n"
    ".Lat_rs8_%=:\n"
    "v_max_f32 v14, 0, v14\n"
    "v_exp_f32 v5, -v14\n"
    "v_sub_f32 v48, v48, v14\n"
    "v_sub_f32 v64, v64, v14\n"
    "v_sub_f32 v96, v96, v14\n"
    "v_sub_f32 v49, v49, v14\n"
    "v_sub_f32 v65, v65, v14\n"
    "v_sub_f32 v97, v97, v14\n"
    "v_sub_f32 v50, v50, v14\n"
    "v_sub_f32 v66, v66, v14\n"
    "v_sub_f32 v98, v98, v14\n"
    "v_sub_f32 v51, v51, v14\n"
    "v_sub_f32 v67, v67, v14\n"
    "v_sub_f32 v99, v99, v14\n"
    "v_sub_f32 v52, v52, v14\n"
    "v_sub_f32 v68, v68, v14\n"
    "v_sub_f32 v100, v100, v14\n"
    "v_sub_f32 v53, v53, v14\n"
    "v_sub_f32 v69, v69, v14\n"
    "v_sub_f32 v101, v101, v14\n"
    "v_sub_f32 v54, v54, v14\n"
    "v_sub_f32 v70, v70, v14\n"
    "v_sub_f32 v102, v102, v14\n"
    "v_sub_f32 v55, v55, v14\n"
    "v_sub_f32 v71, v71, v14\n"
    "v_sub_f32 v103, v103, v14\n"
    "v_sub_f32 v56, v56, v14\n"
    "v_sub_f32 v72, v72, v14\n"
    "v_sub_f32 v104, v104, v14\n"
    "v_sub_f32 v57, v57, v14\n"
    "v_sub_f32 v73, v73, v14\n"
    "v_sub_f32 v105, v105, v14\n"
    "v_sub_f32 v58, v58, v14\n"
    "v_sub_f32 v74, v74, v14\n"
    "v_sub_f32 v106, v106, v14\n"
    "v_sub_f32 v59, v59, v14\n"
    "v_sub_f32 v75, v75, v14\n"
    "v_sub_f32 v107, v107, v14\n"
    "v_sub_f32 v60, v60, v14\n"
    "v_sub_f32 v76, v76, v14\n"
    "v_sub_f32 v108, v108, v14\n"
    "v_sub_f32 v61, v61, v14\n"
    "v_sub_f32 v77, v77, v14\n"
    "v_sub_f32 v109, v109, v14\n"
    "v_sub_f32 v62, v62, v14\n"
    "v_sub_f32 v78, v78, v14\n"
    "v_sub_f32 v110, v110, v14\n"
    "v_sub_f32 v63, v63, v14\n"
    "v_sub_f32 v79, v79, v14\n"
    "v_sub_f32 v111, v111, v14\n"
    "v_mul_f32 v10, v5, v10\n"
    "v_mul_f32 v11, v5, v11\n"
    "v_accvgpr_read_b32 v6, a64\n"
    "v_accvgpr_read_b32 v7, a65\n"
    "v_mul_f32 v6, v5, v6\n"
    "v_mul_f32 v7, v5, v7\n"
    "v_accvgpr_write_b32 a64, v6\n"
    "v_accvgpr_write_b32 a65, v7\n"
    "v_accvgpr_read_b32 v6, a66\n"
    "v_accvgpr_read_b32 v7, a67\n"
    "v_mul_f32 v6, v5, v6\n"
    "v_mul_f32 v7, v5, v7\n"
    "v_accvgpr_write_b32 a66, v6\n"
    "v_accvgpr_write_b32 a67, v7\n"
    "v_accvgpr_read_b32 v6, a68\n"
    "v_accvgpr_read_b32 v7, a69\n"
    "v_mul_f32 v6, v5, v6\n"
    "v_mul_f32 v7, v5, v7\n"
    "v_accvgpr_write_b32 a68, v6\n"
    "v_accvgpr_write_b32 a69, v7\n"
    "v_accvgpr_read_b32 v6, a70\n"
    "v_accvgpr_read_b32 v7, a71\n"
    "v_mul_f32 v6, v5, v6\n"
    "v_mul_f32 v7, v5, v7\n"
    "v_accvgpr_write_b32 a70, v6\n"
    "v_accvgpr_write_b32 a71, v7\n"
    "v_accvgpr_read_b32 v6, a72\n"
    "v_accvgpr_read_b32 v7, a73\n"
    "v_mul_f32 v6, v5, v6\n"
    "v_mul_f32 v7, v5, v7\n"
    "v_accvgpr_write_b32 a72, v6\n"
    "v_accvgpr_write_b32 a73, v7\n"
    "v_accvgpr_read_b32 v6, a74\n"
    "v_accvgpr_read_b32 v7, a75\n"
    "v_mul_f32 v6, v5, v6\n"
    "v_mul_f32 v7, v5, v7\n"
    "v_accvgpr_write_b32 a74, v6\n"
    "v_accvgpr_write_b32 a75, v7\n"
    "v_accvgpr_read_b32 v6, a76\n"
    "v_accvgpr_read_b32 v7, a77\n"
    "v_mul_f32 v6, v5, v6\n"
    "v_mul_f32 v7, v5, v7\n"
    "v_accvgpr_write_b32 a76, v6\n"
    "v_accvgpr_write_b32 a77, v7\n"
    "v_accvgpr_read_b32 v6, a78\n"
    "v_accvgpr_read_b32 v7, a79\n"
    "v_mul_f32 v6, v5, v6\n"
    "v_mul_f32 v7, v5, v7\n"
    "v_accvgpr_write_b32 a78, v6\n"
    "v_accvgpr_write_b32 a79, v7\n"
    "v_accvgpr_read_b32 v6, a80\n"
    "v_accvgpr_read_b32 v7, a81\n"
    "v_mul_f32 v6, v5, v6\n"
    "v_mul_f32 v7, v5, v7\n"
    "v_accvgpr_write_b32 a80, v6\n"
    "v_accvgpr_write_b32 a81, v7\n"
    "v_accvgpr_read_b32 v6, a82\n"
    "v_accvgpr_read_b32 v7, a83\n"
    "v_mul_f32 v6, v5, v6\n"
    "v_mul_f32 v7, v5, v7\n"
    "v_accvgpr_write_b32 a82, v6\n"
    "v_accvgpr_write_b32 a83, v7\n"
    "v_accvgpr_read_b32 v6, a84\n"
    "v_accvgpr_read_b32 v7, a85\n"
    "v_mul_f32 v6, v5, v6\n"
    "v_mul_f32 v7, v5, v7\n"
    "v_accvgpr_write_b32 a84, v6\n"
    "v_accvgpr_write_b32 a85, v7\n"
    "v_accvgpr_read_b32 v6, a86\n"
    "v_accvgpr_read_b32 v7, a87\n"
    "v_mul_f32 v6, v5, v6\n"
    "v_mul_f32 v7, v5, v7\n"
    "v_accvgpr_write_b32 a86, v6\n"
    "v_accvgpr_write_b32 a87, v7\n"
    "v_accvgpr_read_b32 v6, a88\n"
    "v_accvgpr_read_b32 v7, a89\n"
    "v_mul_f32 v6, v5, v6\n"
    "v_mul_f32 v7, v5, v7\n"
    "v_accvgpr_write_b32 a88, v6\n"
    "v_accvgpr_write_b32 a89, v7\n"
    "v_accvgpr_read_b32 v6, a90\n"
    "v_accvgpr_read_b32 v7, a91\n"
    "v_mul_f32 v6, v5, v6\n"
    "v_mul_f32 v7, v5, v7\n"
    "v_accvgpr_write_b32 a90, v6\n"
    "v_accvgpr_write_b32 a91, v7\n"
    "v_accvgpr_read_b32 v6, a92\n"
    "v_accvgpr_read_b32 v7, a93\n"
    "v_mul_f32 v6, v5, v6\n"
    "v_mul_f32 v7, v5, v7\n"
    "v_accvgpr_write_b32 a92, v6\n"
    "v_accvgpr_write_b32 a93, v7\n"
    "v_accvgpr_read_b32 v6, a94\n"
    "v_accvgpr_read_b32 v7, a95\n"
    "v_mul_f32 v6, v5, v6\n"
    "v_mul_f32 v7, v5, v7\n"
    "v_accvgpr_write_b32 a94, v6\n"
    "v_accvgpr_write_b32 a95, v7\n"
    "v_accvgpr_read_b32 v6, a96\n"
    "v_accvgpr_read_b32 v7, a97\n"
    "v_mul_f32 v6, v5, v6\n"
    "v_mul_f32 v7, v5, v7\n"
    "v_accvgpr_write_b32 a96, v6\n"
    "v_accvgpr_write_b32 a97, v7\n"
    "v_accvgpr_read_b32 v6, a98\n"
    "v_accvgpr_read_b32 v7, a99\n"
    "v_mul_f32 v6, v5, v6\n"
    "v_mul_f32 v7, v5, v7\n"
    "v_accvgpr_write_b32 a98, v6\n"
    "v_accvgpr_write_b32 a99, v7\n"
    "v_accvgpr_read_b32 v6, a100\n"
    "v_accvgpr_read_b32 v7, a101\n"
    "v_mul_f32 v6, v5, v6\n"
    "v_mul_f32 v7, v5, v7\n"
    "v_accvgpr_write_b32 a100, v6\n"
    "v_accvgpr_write_b32 a101, v7\n"
    "v_accvgpr_read_b32 v6, a102\n"
    "v_accvgpr_read_b32 v7, a103\n"
    "v_mul_f32 v6, v5, v6\n"
    "v_mul_f32 v7, v5, v7\n"
    "v_accvgpr_write_b32 a102, v6\n"
    "v_accvgpr_write_b32 a103, v7\n"
    "v_accvgpr_read_b32 v6, a104\n"
    "v_accvgpr_read_b32 v7, a105\n"
    "v_mul_f32 v6, v5, v6\n"
    "v_mul_f32 v7, v5, v7\n"
    "v_accvgpr_write_b32 a104, v6\n"
    "v_accvgpr_write_b32 a105, v7\n"
    "v_accvgpr_read_b32 v6, a106\n"
    "v_accvgpr_read_b32 v7, a107\n"
    "v_mul_f32 v6, v5, v6\n"
    "v_mul_f32 v7, v5, v7\n"
    "v_accvgpr_write_b32 a106, v6\n"
    "v_accvgpr_write_b32 a107, v7\n"
    "v_accvgpr_read_b32 v6, a108\n"
    "v_accvgpr_read_b32 v7, a109\n"
    "v_mul_f32 v6, v5, v6\n"
    "v_mul_f32 v7, v5, v7\n"
    "v_accvgpr_write_b32 a108, v6\n"
    "v_accvgpr_write_b32 a109, v7\n"
    "v_accvgpr_read_b32 v6, a110\n"
    "v_accvgpr_read_b32 v7, a111\n"
    "v_mul_f32 v6, v5, v6\n"
    "v_mul_f32 v7, v5, v7\n"
    "v_accvgpr_write_b32 a110, v6\n"
    "v_accvgpr_write_b32 a111, v7\n"
    "v_accvgpr_read_b32 v6, a112\n"
    "v_accvgpr_read_b32 v7, a113\n"
    "v_mul_f32 v6, v5, v6\n"
    "v_mul_f32 v7, v5, v7\n"
    "v_accvgpr_write_b32 a112, v6\n"
    "v_accvgpr_write_b32 a113, v7\n"
    "v_accvgpr_read_b32 v6, a114\n"
    "v_accvgpr_read_b32 v7, a115\n"
    "v_mul_f32 v6, v5, v6\n"
    "v_mul_f32 v7, v5, v7\n"
    "v_accvgpr_write_b32 a114, v6\n"
    "v_accvgpr_write_b32 a115, v7\n"
    "v_accvgpr_read_b32 v6, a116\n"
    "v_accvgpr_read_b32 v7, a117\n"
    "v_mul_f32 v6, v5, v6\n"
    "v_mul_f32 v7, v5, v7\n"
    "v_accvgpr_write_b32 a116, v6\n"
    "v_accvgpr_write_b32 a117, v7\n"
    "v_accvgpr_read_b32 v6, a118\n"
    "v_accvgpr_read_b32 v7, a119\n"
    "v_mul_f32 v6, v5, v6\n"
    "v_mul_f32 v7, v5, v7\n"
    "v_accvgpr_write_b32 a118, v6\n"
    "v_accvgpr_write_b32 a119, v7\n"
    "v_accvgpr_read_b32 v6, a120\n"
    "v_accvgpr_read_b32 v7, a121\n"
    "v_mul_f32 v6, v5, v6\n"
    "v_mul_f32 v7, v5, v7\n"
    "v_accvgpr_write_b32 a120, v6\n"
    "v_accvgpr_write_b32 a121, v7\n"
    "v_accvgpr_read_b32 v6, a122\n"
    "v_accvgpr_read_b32 v7, a123\n"
    "v_mul_f32 v6, v5, v6\n"
    "v_mul_f32 v7, v5, v7\n"
    "v_accvgpr_write_b32 a122, v6\n"
    "v_accvgpr_write_b32 a123, v7\n"
    "v_accvgpr_read_b32 v6, a124\n"
    "v_accvgpr_read_b32 v7, a125\n"
    "v_mul_f32 v6, v5, v6\n"
    "v_mul_f32 v7, v5, v7\n"
    "v_accvgpr_write_b32 a124, v6\n"
    "v_accvgpr_write_b32 a125, v7\n"
    "v_accvgpr_read_b32 v6, a126\n"
    "v_accvgpr_read_b32 v7, a127\n"
    "v_mul_f32 v6, v5, v6\n"
    "v_mul_f32 v7, v5, v7\n"
    "v_accvgpr_write_b32 a126, v6\n"
    "v_accvgpr_write_b32 a127, v7\n"
    "s_branch .Lat_rsr8_%=\n"
    ".Lat_dg9_%=:\n"
    "s_nop 11\n"
    "v_mov_b32 v5, 0xff800000\n"
    "v_cmp_gt_i32 vcc, 0, v1\n"
    "s_nop 1\n"
    "v_cndmask_b32 v16, v16, v5, vcc\n"
    "v_mov_b32 v32, v5\n"
    "v_cmp_gt_i32 vcc, 1, v1\n"
    "s_nop 1\n"
    "v_cndmask_b32 v17, v17, v5, vcc\n"
    "v_mov_b32 v33, v5\n"
    "v_cmp_gt_i32 vcc, 2, v1\n"
    "s_nop 1\n"
    "v_cndmask_b32 v18, v18, v5, vcc\n"
    "v_mov_b32 v34, v5\n"
    "v_cmp_gt_i32 vcc, 3, v1\n"
    "s_nop 1\n"
    "v_cndmask_b32 v19, v19, v5, vcc\n"
    "v_mov_b32 v35, v5\n"
    "v_cmp_gt_i32 vcc, 8, v1\n"
    "s_nop 1\n"
    "v_cndmask_b32 v20, v20, v5, vcc\n"
    "v_mov_b32 v36, v5\n"
    "v_cmp_gt_i32 vcc, 9, v1\n"
    "s_nop 1\n"
    "v_cndmask_b32 v21, v21, v5, vcc\n"
    "v_mov_b32 v37, v5\n"
    "v_cmp_gt_i32 vcc, 10, v1\n"
    "s_nop 1\n"
    "v_cndmask_b32 v22, v22, v5, vcc\n"
    "v_mov_b32 v38, v5\n"
    "v_cmp_gt_i32 vcc, 11, v1\n"
    "s_nop 1\n"
    "v_cndmask_b32 v23, v23, v5, vcc\n"
    "v_mov_b32 v39, v5\n"
    "v_cmp_gt_i32 vcc, 16, v1\n"
    "s_nop 1\n"
    "v_cndmask_b32 v24, v24, v5, vcc\n"
    "v_mov_b32 v40, v5\n"
    "v_cmp_gt_i32 vcc, 17, v1\n"
    "s_nop 1\n"
    "v_cndmask_b32 v25, v25, v5, vcc\n"
    "v_mov_b32 v41, v5\n"
    "v_cmp_gt_i32 vcc, 18, v1\n"
    "s_nop 1\n"
    "v_cndmask_b32 v26, v26, v5, vcc\n"
    "v_mov_b32 v42, v5\n"
    "v_cmp_gt_i32 vcc, 19, v1\n"
    "s_nop 1\n"
    "v_cndmask_b32 v27, v27, v5, vcc\n"
    "v_mov_b32 v43, v5\n"
    "v_cmp_gt_i32 vcc, 24, v1\n"
    "s_nop 1\n"
    "v_cndmask_b32 v28, v28, v5, vcc\n"
    "v_mov_b32 v44, v5\n"
    "v_cmp_gt_i32 vcc, 25, v1\n"
    "s_nop 1\n"
    "v_cndmask_b32 v29, v29, v5, vcc\n"
    "v_mov_b32 v45, v5\n"
    "v_cmp_gt_i32 vcc, 26, v1\n"
    "s_nop 1\n"
    "v_cndmask_b32 v30, v30, v5, vcc\n"
    "v_mov_b32 v46, v5\n"
    "v_cmp_gt_i32 vcc, 27, v1\n"
    "s_nop 1\n"
    "v_cndmask_b32 v31, v31, v5, vcc\n"
    "v_mov_b32 v47, v5\n"
    "s_branch .Lat_dgr9_%=\n"
    ".Lat_rs9_%=:\n"
    "v_max_f32 v12, 0, v12\n"
    "v_exp_f32 v5, -v12\n"
    "v_sub_f32 v16, v16, v12\n"
    "v_sub_f32 v32, v32, v12\n"
    "v_sub_f32 v80, v80, v12\n"
    "v_sub_f32 v17, v17, v12\n"
    "v_sub_f32 v33, v33, v12\n"
    "v_sub_f32 v81, v81, v12\n"
    "v_sub_f32 v18, v18, v12\n"
    "v_sub_f32 v34, v34, v12\n"
    "v_sub_f32 v82, v82, v12\n"
    "v_sub_f32 v19, v19, v12\n"
    "v_sub_f32 v35, v35, v12\n"
    "v_sub_f32 v83, v83, v12\n"
    "v_sub_f32 v20, v20, v12\n"
    "v_sub_f32 v36, v36, v12\n"
    "v_sub_f32 v84, v84, v12\n"
    "v_sub_f32 v21, v21, v12\n"
    "v_sub_f32 v37, v37, v12\n"
    "v_sub_f32 v85, v85, v12\n"
    "v_sub_f32 v22, v22, v12\n"
    "v_sub_f32 v38, v38, v12\n"
    "v_sub_f32 v86, v86, v12\n"
    "v_sub_f32 v23, v23, v12\n"
    "v_sub_f32 v39, v39, v12\n"
    "v_sub_f32 v87, v87, v12\n"
    "v_sub_f32 v24, v24, v12\n"
    "v_sub_f32 v40, v40, v12\n"
    "v_sub_f32 v88, v88, v12\n"
    "v_sub_f32 v25, v25, v12\n"
    "v_sub_f32 v41, v41, v12\n"
    "v_sub_f32 v89, v89, v12\n"
    "v_sub_f32 v26, v26, v12\n"
    "v_sub_f32 v42, v42, v12\n"
    "v_sub_f32 v90, v90, v12\n"
    "v_sub_f32 v27, v27, v12\n"
    "v_sub_f32 v43, v43, v12\n"
    "v_sub_f32 v91, v91, v12\n"
    "v_sub_f32 v28, v28, v12\n"
    "v_sub_f32 v44, v44, v12\n"
    "v_sub_f32 v92, v92, v12\n"
    "v_sub_f32 v29, v29, v12\n"
    "v_sub_f32 v45, v45, v12\n"
    "v_sub_f32 v93, v93, v12\n"
    "v_sub_f32 v30, v30, v12\n"
    "v_sub_f32 v46, v46, v12\n"
    "v_sub_f32 v94, v94, v12\n"
    "v_sub_f32 v31, v31, v12\n"
    "v_sub_f32 v47, v47, v12\n"
    "v_sub_f32 v95, v95, v12\n"
    "v_mul_f32 v8, v5, v8\n"
    "v_mul_f32 v9, v5, v9\n"
    "v_accvgpr_read_b32 v6, a0\n"
    "v_accvgpr_read_b32 v7, a1\n"
    "v_mul_f32 v6, v5, v6\n"
    "v_mul_f32 v7, v5, v7\n"
    "v_accvgpr_write_b32 a0, v6\n"
    "v_accvgpr_write_b32 a1, v7\n"
    "v_accvgpr_read_b32 v6, a2\n"
    "v_accvgpr_read_b32 v7, a3\n"
    "v_mul_f32 v6, v5, v6\n"
    "v_mul_f32 v7, v5, v7\n"
    "v_accvgpr_write_b32 a2, v6\n"
    "v_accvgpr_write_b32 a3, v7\n"
    "v_accvgpr_read_b32 v6, a4\n"
    "v_accvgpr_read_b32 v7, a5\n"
    "v_mul_f32 v6, v5, v6\n"
    "v_mul_f32 v7, v5, v7\n"
    "v_accvgpr_write_b32 a4, v6\n"
    "v_accvgpr_write_b32 a5, v7\n"
    "v_accvgpr_read_b32 v6, a6\n"
    "v_accvgpr_read_b32 v7, a7\n"
    "v_mul_f32 v6, v5, v6\n"
    "v_mul_f32 v7, v5, v7\n"
    "v_accvgpr_write_b32 a6, v6\n"
    "v_accvgpr_write_b32 a7, v7\n"
    "v_accvgpr_read_b32 v6, a8\n"
    "v_accvgpr_read_b32 v7, a9\n"
    "v_mul_f32 v6, v5, v6\n"
    "v_mul_f32 v7, v5, v7\n"
    "v_accvgpr_write_b32 a8, v6\n"
    "v_accvgpr_write_b32 a9, v7\n"
    "v_accvgpr_read_b32 v6, a10\n"
    "v_accvgpr_read_b32 v7, a11\n"
    "v_mul_f32 v6, v5, v6\n"
    "v_mul_f32 v7, v5, v7\n"
    "v_accvgpr_write_b32 a10, v6\n"
    "v_accvgpr_write_b32 a11, v7\n"
    "v_accvgpr_read_b32 v6, a12\n"
    "v_accvgpr_read_b32 v7, a13\n"
    "v_mul_f32 v6, v5, v6\n"
    "v_mul_f32 v7, v5, v7\n"
    "v_accvgpr_write_b32 a12, v6\n"
    "v_accvgpr_write_b32 a13, v7\n"
    "v_accvgpr_read_b32 v6, a14\n"
    "v_accvgpr_read_b32 v7, a15\n"
    "v_mul_f32 v6, v5, v6\n"
    "v_mul_f32 v7, v5, v7\n"
    "v_accvgpr_write_b32 a14, v6\n"
    "v_accvgpr_write_b32 a15, v7\n"
    "v_accvgpr_read_b32 v6, a16\n"
    "v_accvgpr_read_b32 v7, a17\n"
    "v_mul_f32 v6, v5, v6\n"
    "v_mul_f32 v7, v5, v7\n"
    "v_accvgpr_write_b32 a16, v6\n"
    "v_accvgpr_write_b32 a17, v7\n"
    "v_accvgpr_read_b32 v6, a18\n"
    "v_accvgpr_read_b32 v7, a19\n"
    "v_mul_f32 v6, v5, v6\n"
    "v_mul_f32 v7, v5, v7\n"
    "v_accvgpr_write_b32 a18, v6\n"
    "v_accvgpr_write_b32 a19, v7\n"
    "v_accvgpr_read_b32 v6, a20\n"
    "v_accvgpr_read_b32 v7, a21\n"
    "v_mul_f32 v6, v5, v6\n"
    "v_mul_f32 v7, v5, v7\n"
    "v_accvgpr_write_b32 a20, v6\n"
    "v_accvgpr_write_b32 a21, v7\n"
    "v_accvgpr_read_b32 v6, a22\n"
    "v_accvgpr_read_b32 v7, a23\n"
    "v_mul_f32 v6, v5, v6\n"
    "v_mul_f32 v7, v5, v7\n"
    "v_accvgpr_write_b32 a22, v6\n"
    "v_accvgpr_write_b32 a23, v7\n"
    "v_accvgpr_read_b32 v6, a24\n"
    "v_accvgpr_read_b32 v7, a25\n"
    "v_mul_f32 v6, v5, v6\n"
    "v_mul_f32 v7, v5, v7\n"
    "v_accvgpr_write_b32 a24, v6\n"
    "v_accvgpr_write_b32 a25, v7\n"
    "v_accvgpr_read_b32 v6, a26\n"
    "v_accvgpr_read_b32 v7, a27\n"
    "v_mul_f32 v6, v5, v6\n"
    "v_mul_f32 v7, v5, v7\n"
    "v_accvgpr_write_b32 a26, v6\n"
    "v_accvgpr_write_b32 a27, v7\n"
    "v_accvgpr_read_b32 v6, a28\n"
    "v_accvgpr_read_b32 v7, a29\n"
    "v_mul_f32 v6, v5, v6\n"
    "v_mul_f32 v7, v5, v7\n"
    "v_accvgpr_write_b32 a28, v6\n"
    "v_accvgpr_write_b32 a29, v7\n"
    "v_accvgpr_read_b32 v6, a30\n"
    "v_accvgpr_read_b32 v7, a31\n"
    "v_mul_f32 v6, v5, v6\n"
    "v_mul_f32 v7, v5, v7\n"
    "v_accvgpr_write_b32 a30, v6\n"
    "v_accvgpr_write_b32 a31, v7\n"
    "v_accvgpr_read_b32 v6, a32\n"
    "v_accvgpr_read_b32 v7, a33\n"
    "v_mul_f32 v6, v5, v6\n"
    "v_mul_f32 v7, v5, v7\n"
    "v_accvgpr_write_b32 a32, v6\n"
    "v_accvgpr_write_b32 a33, v7\n"
    "v_accvgpr_read_b32 v6, a34\n"
    "v_accvgpr_read_b32 v7, a35\n"
    "v_mul_f32 v6, v5, v6\n"
    "v_mul_f32 v7, v5, v7\n"
    "v_accvgpr_write_b32 a34, v6\n"
    "v_accvgpr_write_b32 a35, v7\n"
    "v_accvgpr_read_b32 v6, a36\n"
    "v_accvgpr_read_b32 v7, a37\n"
    "v_mul_f32 v6, v5, v6\n"
    "v_mul_f32 v7, v5, v7\n"
    "v_accvgpr_write_b32 a36, v6\n"
    "v_accvgpr_write_b32 a37, v7\n"
    "v_accvgpr_read_b32 v6, a38\n"
    "v_accvgpr_read_b32 v7, a39\n"
    "v_mul_f32 v6, v5, v6\n"
    "v_mul_f32 v7, v5, v7\n"
    "v_accvgpr_write_b32 a38, v6\n"
    "v_accvgpr_write_b32 a39, v7\n"
    "v_accvgpr_read_b32 v6, a40\n"
    "v_accvgpr_read_b32 v7, a41\n"
    "v_mul_f32 v6, v5, v6\n"
    "v_mul_f32 v7, v5, v7\n"
    "v_accvgpr_write_b32 a40, v6\n"
    "v_accvgpr_write_b32 a41, v7\n"
    "v_accvgpr_read_b32 v6, a42\n"
    "v_accvgpr_read_b32 v7, a43\n"
    "v_mul_f32 v6, v5, v6\n"
    "v_mul_f32 v7, v5, v7\n"
    "v_accvgpr_write_b32 a42, v6\n"
    "v_accvgpr_write_b32 a43, v7\n"
    "v_accvgpr_read_b32 v6, a44\n"
    "v_accvgpr_read_b32 v7, a45\n"
    "v_mul_f32 v6, v5, v6\n"
    "v_mul_f32 v7, v5, v7\n"
    "v_accvgpr_write_b32 a44, v6\n"
    "v_accvgpr_write_b32 a45, v7\n"
    "v_accvgpr_read_b32 v6, a46\n"
    "v_accvgpr_read_b32 v7, a47\n"
    "v_mul_f32 v6, v5, v6\n"
    "v_mul_f32 v7, v5, v7\n"
    "v_accvgpr_write_b32 a46, v6\n"
    "v_accvgpr_write_b32 a47, v7\n"
    "v_accvgpr_read_b32 v6, a48\n"
    "v_accvgpr_read_b32 v7, a49\n"
    "v_mul_f32 v6, v5, v6\n"
    "v_mul_f32 v7, v5, v7\n"
    "v_accvgpr_write_b32 a48, v6\n"
    "v_accvgpr_write_b32 a49, v7\n"
    "v_accvgpr_read_b32 v6, a50\n"
    "v_accvgpr_read_b32 v7, a51\n"
    "v_mul_f32 v6, v5, v6\n"
    "v_mul_f32 v7, v5, v7\n"
    "v_accvgpr_write_b32 a50, v6\n"
    "v_accvgpr_write_b32 a51, v7\n"
    "v_accvgpr_read_b32 v6, a52\n"
    "v_accvgpr_read_b32 v7, a53\n"
    "v_mul_f32 v6, v5, v6\n"
    "v_mul_f32 v7, v5, v7\n"
    "v_accvgpr_write_b32 a52, v6\n"
    "v_accvgpr_write_b32 a53, v7\n"
    "v_accvgpr_read_b32 v6, a54\n"
    "v_accvgpr_read_b32 v7, a55\n"
    "v_mul_f32 v6, v5, v6\n"
    "v_mul_f32 v7, v5, v7\n"
    "v_accvgpr_write_b32 a54, v6\n"
    "v_accvgpr_write_b32 a55, v7\n"
    "v_accvgpr_read_b32 v6, a56\n"
    "v_accvgpr_read_b32 v7, a57\n"
    "v_mul_f32 v6, v5, v6\n"
    "v_mul_f32 v7, v5, v7\n"
    "v_accvgpr_write_b32 a56, v6\n"
    "v_accvgpr_write_b32 a57, v7\n"
    "v_accvgpr_read_b32 v6, a58\n"
    "v_accvgpr_read_b32 v7, a59\n"
    "v_mul_f32 v6, v5, v6\n"
    "v_mul_f32 v7, v5, v7\n"
    "v_accvgpr_write_b32 a58, v6\n"
    "v_accvgpr_write_b32 a59, v7\n"
    "v_accvgpr_read_b32 v6, a60\n"
    "v_accvgpr_read_b32 v7, a61\n"
    "v_mul_f32 v6, v5, v6\n"
    "v_mul_f32 v7, v5, v7\n"
    "v_accvgpr_write_b32 a60, v6\n"
    "v_accvgpr_write_b32 a61, v7\n"
    "v_accvgpr_read_b32 v6, a62\n"
    "v_accvgpr_read_b32 v7, a63\n"
    "v_mul_f32 v6, v5, v6\n"
    "v_mul_f32 v7, v5, v7\n"
    "v_accvgpr_write_b32 a62, v6\n"
    "v_accvgpr_write_b32 a63, v7\n"
    "s_branch .Lat_rsr9_%=\n"
    ".Lat_dg10_%=:\n"
    "s_nop 11\n"
    "v_mov_b32 v5, 0xff800000\n"
    "v_cmp_gt_i32 vcc, 0, v1\n"
    "s_nop 1\n"
    "v_cndmask_b32 v64, v64, v5, vcc\n"
    "v_cmp_gt_i32 vcc, 1, v1\n"
    "s_nop 1\n"
    "v_cndmask_b32 v65, v65, v5, vcc\n"
    "v_cmp_gt_i32 vcc, 2, v1\n"
    "s_nop 1\n"
    "v_cndmask_b32 v66, v66, v5, vcc\n"
    "v_cmp_gt_i32 vcc, 3, v1\n"
    "s_nop 1\n"
    "v_cndmask_b32 v67, v67, v5, vcc\n"
    "v_cmp_gt_i32 vcc, 8, v1\n"
    "s_nop 1\n"
    "v_cndmask_b32 v68, v68, v5, vcc\n"
    "v_cmp_gt_i32 vcc, 9, v1\n"
    "s_nop 1\n"
    "v_cndmask_b32 v69, v69, v5, vcc\n"
    "v_cmp_gt_i32 vcc, 10, v1\n"
    "s_nop 1\n"
    "v_cndmask_b32 v70, v70, v5, vcc\n"
    "v_cmp_gt_i32 vcc, 11, v1\n"
    "s_nop 1\n"
    "v_cndmask_b32 v71, v71, v5, vcc\n"
    "v_cmp_gt_i32 vcc, 16, v1\n"
    "s_nop 1\n"
    "v_cndmask_b32 v72, v72, v5, vcc\n"
    "v_cmp_gt_i32 vcc, 17, v1\n"
    "s_nop 1\n"
    "v_cndmask_b32 v73, v73, v5, vcc\n"
    "v_cmp_gt_i32 vcc, 18, v1\n"
    "s_nop 1\n"
    "v_cndmask_b32 v74, v74, v5, vcc\n"
    "v_cmp_gt_i32 vcc, 19, v1\n"
    "s_nop 1\n"
    "v_cndmask_b32 v75, v75, v5, vcc\n"
    "v_cmp_gt_i32 vcc, 24, v1\n"
    "s_nop 1\n"
    "v_cndmask_b32 v76, v76, v5, vcc\n"
    "v_cmp_gt_i32 vcc, 25, v1\n"
    "s_nop 1\n"
    "v_cndmask_b32 v77, v77, v5, vcc\n"
    "v_cmp_gt_i32 vcc, 26, v1\n"
    "s_nop 1\n"
    "v_cndmask_b32 v78, v78, v5, vcc\n"
    "v_cmp_gt_i32 vcc, 27, v1\n"
    "s_nop 1\n"
    "v_cndmask_b32 v79, v79, v5, vcc\n"
    "s_branch .Lat_dgr10_%=\n"
    ".Lat_rs10_%=:\n"
    "v_max_f32 v14, 0, v14\n"
    "v_exp_f32 v5, -v14\n"
    "v_sub_f32 v48, v48, v14\n"
    "v_sub_f32 v64, v64, v14\n"
    "v_sub_f32 v96, v96, v14\n"
    "v_sub_f32 v49, v49, v14\n"
    "v_sub_f32 v65, v65, v14\n"
    "v_sub_f32 v97, v97, v14\n"
    "v_sub_f32 v50, v50, v14\n"
    "v_sub_f32 v66, v66, v14\n"
    "v_sub_f32 v98, v98, v14\n"
    "v_sub_f32 v51, v51, v14\n"
    "v_sub_f32 v67, v67, v14\n"
    "v_sub_f32 v99, v99, v14\n"
    "v_sub_f32 v52, v52, v14\n"
    "v_sub_f32 v68, v68, v14\n"
    "v_sub_f32 v100, v100, v14\n"
    "v_sub_f32 v53, v53, v14\n"
    "v_sub_f32 v69, v69, v14\n"
    "v_sub_f32 v101, v101, v14\n"
    "v_sub_f32 v54, v54, v14\n"
    "v_sub_f32 v70, v70, v14\n"
    "v_sub_f32 v102, v102, v14\n"
    "v_sub_f32 v55, v55, v14\n"
    "v_sub_f32 v71, v71, v14\n"
    "v_sub_f32 v103, v103, v14\n"
    "v_sub_f32 v56, v56, v14\n"
    "v_sub_f32 v72, v72, v14\n"
    "v_sub_f32 v104, v104, v14\n"
    "v_sub_f32 v57, v57, v14\n"
    "v_sub_f32 v73, v73, v14\n"
    "v_sub_f32 v105, v105, v14\n"
    "v_sub_f32 v58, v58, v14\n"
    "v_sub_f32 v74, v74, v14\n"
    "v_sub_f32 v106, v106, v14\n"
    "v_sub_f32 v59, v59, v14\n"
    "v_sub_f32 v75, v75, v14\n"
    "v_sub_f32 v107, v107, v14\n"
    "v_sub_f32 v60, v60, v14\n"
    "v_sub_f32 v76, v76, v14\n"
    "v_sub_f32 v108, v108, v14\n"
    "v_sub_f32 v61, v61, v14\n"
    "v_sub_f32 v77, v77, v14\n"
    "v_sub_f32 v109, v109, v14\n"
    "v_sub_f32 v62, v62, v14\n"
    "v_sub_f32 v78, v78, v14\n"
    "v_sub_f32 v110, v110, v14\n"
    "v_sub_f32 v63, v63, v14\n"
    "v_sub_f32 v79, v79, v14\n"
    "v_sub_f32 v111, v111, v14\n"
    "v_mul_f32 v10, v5, v10\n"
    "v_mul_f32 v11, v5, v11\n"
    "v_accvgpr_read_b32 v6, a64\n"
    "v_accvgpr_read_b32 v7, a65\n"
    "v_mul_f32 v6, v5, v6\n"
    "v_mul_f32 v7, v5, v7\n"
    "v_accvgpr_write_b32 a64, v6\n"
    "v_accvgpr_write_b32 a65, v7\n"
    "v_accvgpr_read_b32 v6, a66\n"
    "v_accvgpr_read_b32 v7, a67\n"
    "v_mul_f32 v6, v5, v6\n"
    "v_mul_f32 v7, v5, v7\n"
    "v_accvgpr_write_b32 a66, v6\n"
    "v_accvgpr_write_b32 a67, v7\n"
    "v_accvgpr_read_b32 v6, a68\n"
    "v_accvgpr_read_b32 v7, a69\n"
    "v_mul_f32 v6, v5, v6\n"
    "v_mul_f32 v7, v5, v7\n"
    "v_accvgpr_write_b32 a68, v6\n"
    "v_accvgpr_write_b32 a69, v7\n"
    "v_accvgpr_read_b32 v6, a70\n"
    "v_accvgpr_read_b32 v7, a71\n"
    "v_mul_f32 v6, v5, v6\n"
    "v_mul_f32 v7, v5, v7\n"
    "v_accvgpr_write_b32 a70, v6\n"
    "v_accvgpr_write_b32 a71, v7\n"
    "v_accvgpr_read_b32 v6, a72\n"
    "v_accvgpr_read_b32 v7, a73\n"
    "v_mul_f32 v6, v5, v6\n"
    "v_mul_f32 v7, v5, v7\n"
    "v_accvgpr_write_b32 a72, v6\n"
    "v_accvgpr_write_b32 a73, v7\n"
    "v_accvgpr_read_b32 v6, a74\n"
    "v_accvgpr_read_b32 v7, a75\n"
    "v_mul_f32 v6, v5, v6\n"
    "v_mul_f32 v7, v5, v7\n"
    "v_accvgpr_write_b32 a74, v6\n"
    "v_accvgpr_write_b32 a75, v7\n"
    "v_accvgpr_read_b32 v6, a76\n"
    "v_accvgpr_read_b32 v7, a77\n"
    "v_mul_f32 v6, v5, v6\n"
    "v_mul_f32 v7, v5, v7\n"
    "v_accvgpr_write_b32 a76, v6\n"
    "v_accvgpr_write_b32 a77, v7\n"
    "v_accvgpr_read_b32 v6, a78\n"
    "v_accvgpr_read_b32 v7, a79\n"
    "v_mul_f32 v6, v5, v6\n"
    "v_mul_f32 v7, v5, v7\n"
    "v_accvgpr_write_b32 a78, v6\n"
    "v_accvgpr_write_b32 a79, v7\n"
    "v_accvgpr_read_b32 v6, a80\n"
    "v_accvgpr_read_b32 v7, a81\n"
    "v_mul_f32 v6, v5, v6\n"
    "v_mul_f32 v7, v5, v7\n"
    "v_accvgpr_write_b32 a80, v6\n"
    "v_accvgpr_write_b32 a81, v7\n"
    "v_accvgpr_read_b32 v6, a82\n"
    "v_accvgpr_read_b32 v7, a83\n"
    "v_mul_f32 v6, v5, v6\n"
    "v_mul_f32 v7, v5, v7\n"
    "v_accvgpr_write_b32 a82, v6\n"
    "v_accvgpr_write_b32 a83, v7\n"
    "v_accvgpr_read_b32 v6, a84\n"
    "v_accvgpr_read_b32 v7, a85\n"
    "v_mul_f32 v6, v5, v6\n"
    "v_mul_f32 v7, v5, v7\n"
    "v_accvgpr_write_b32 a84, v6\n"
    "v_accvgpr_write_b32 a85, v7\n"
    "v_accvgpr_read_b32 v6, a86\n"
    "v_accvgpr_read_b32 v7, a87\n"
    "v_mul_f32 v6, v5, v6\n"
    "v_mul_f32 v7, v5, v7\n"
    "v_accvgpr_write_b32 a86, v6\n"
    "v_accvgpr_write_b32 a87, v7\n"
    "v_accvgpr_read_b32 v6, a88\n"
    "v_accvgpr_read_b32 v7, a89\n"
    "v_mul_f32 v6, v5, v6\n"
    "v_mul_f32 v7, v5, v7\n"
    "v_accvgpr_write_b32 a88, v6\n"
    "v_accvgpr_write_b32 a89, v7\n"
    "v_accvgpr_read_b32 v6, a90\n"
    "v_accvgpr_read_b32 v7, a91\n"
    "v_mul_f32 v6, v5, v6\n"
    "v_mul_f32 v7, v5, v7\n"
    "v_accvgpr_write_b32 a90, v6\n"
    "v_accvgpr_write_b32 a91, v7\n"
    "v_accvgpr_read_b32 v6, a92\n"
    "v_accvgpr_read_b32 v7, a93\n"
    "v_mul_f32 v6, v5, v6\n"
    "v_mul_f32 v7, v5, v7\n"
    "v_accvgpr_write_b32 a92, v6\n"
    "v_accvgpr_write_b32 a93, v7\n"
    "v_accvgpr_read_b32 v6, a94\n"
    "v_accvgpr_read_b32 v7, a95\n"
    "v_mul_f32 v6, v5, v6\n"
    "v_mul_f32 v7, v5, v7\n"
    "v_accvgpr_write_b32 a94, v6\n"
    "v_accvgpr_write_b32 a95, v7\n"
    "v_accvgpr_read_b32 v6, a96\n"
    "v_accvgpr_read_b32 v7, a97\n"
    "v_mul_f32 v6, v5, v6\n"
    "v_mul_f32 v7, v5, v7\n"
    "v_accvgpr_write_b32 a96, v6\n"
    "v_accvgpr_write_b32 a97, v7\n"
    "v_accvgpr_read_b32 v6, a98\n"
    "v_accvgpr_read_b32 v7, a99\n"
    "v_mul_f32 v6, v5, v6\n"
    "v_mul_f32 v7, v5, v7\n"
    "v_accvgpr_write_b32 a98, v6\n"
    "v_accvgpr_write_b32 a99, v7\n"
    "v_accvgpr_read_b32 v6, a100\n"
    "v_accvgpr_read_b32 v7, a101\n"
    "v_mul_f32 v6, v5, v6\n"
    "v_mul_f32 v7, v5, v7\n"
    "v_accvgpr_write_b32 a100, v6\n"
    "v_accvgpr_write_b32 a101, v7\n"
    "v_accvgpr_read_b32 v6, a102\n"
    "v_accvgpr_read_b32 v7, a103\n"
    "v_mul_f32 v6, v5, v6\n"
    "v_mul_f32 v7, v5, v7\n"
    "v_accvgpr_write_b32 a102, v6\n"
    "v_accvgpr_write_b32 a103, v7\n"
    "v_accvgpr_read_b32 v6, a104\n"
    "v_accvgpr_read_b32 v7, a105\n"
    "v_mul_f32 v6, v5, v6\n"
    "v_mul_f32 v7, v5, v7\n"
    "v_accvgpr_write_b32 a104, v6\n"
    "v_accvgpr_write_b32 a105, v7\n"
    "v_accvgpr_read_b32 v6, a106\n"
    "v_accvgpr_read_b32 v7, a107\n"
    "v_mul_f32 v6, v5, v6\n"
    "v_mul_f32 v7, v5, v7\n"
    "v_accvgpr_write_b32 a106, v6\n"
    "v_accvgpr_write_b32 a107, v7\n"
    "v_accvgpr_read_b32 v6, a108\n"
    "v_accvgpr_read_b32 v7, a109\n"
    "v_mul_f32 v6, v5, v6\n"
    "v_mul_f32 v7, v5, v7\n"
    "v_accvgpr_write_b32 a108, v6\n"
    "v_accvgpr_write_b32 a109, v7\n"
    "v_accvgpr_read_b32 v6, a110\n"
    "v_accvgpr_read_b32 v7, a111\n"
    "v_mul_f32 v6, v5, v6\n"
    "v_mul_f32 v7, v5, v7\n"
    "v_accvgpr_write_b32 a110, v6\n"
    "v_accvgpr_write_b32 a111, v7\n"
    "v_accvgpr_read_b32 v6, a112\n"
    "v_accvgpr_read_b32 v7, a113\n"
    "v_mul_f32 v6, v5, v6\n"
    "v_mul_f32 v7, v5, v7\n"
    "v_accvgpr_write_b32 a112, v6\n"
    "v_accvgpr_write_b32 a113, v7\n"
    "v_accvgpr_read_b32 v6, a114\n"
    "v_accvgpr_read_b32 v7, a115\n"
    "v_mul_f32 v6, v5, v6\n"
    "v_mul_f32 v7, v5, v7\n"
    "v_accvgpr_write_b32 a114, v6\n"
    "v_accvgpr_write_b32 a115, v7\n"
    "v_accvgpr_read_b32 v6, a116\n"
    "v_accvgpr_read_b32 v7, a117\n"
    "v_mul_f32 v6, v5, v6\n"
    "v_mul_f32 v7, v5, v7\n"
    "v_accvgpr_write_b32 a116, v6\n"
    "v_accvgpr_write_b32 a117, v7\n"
    "v_accvgpr_read_b32 v6, a118\n"
    "v_accvgpr_read_b32 v7, a119\n"
    "v_mul_f32 v6, v5, v6\n"
    "v_mul_f32 v7, v5, v7\n"
    "v_accvgpr_write_b32 a118, v6\n"
    "v_accvgpr_write_b32 a119, v7\n"
    "v_accvgpr_read_b32 v6, a120\n"
    "v_accvgpr_read_b32 v7, a121\n"
    "v_mul_f32 v6, v5, v6\n"
    "v_mul_f32 v7, v5, v7\n"
    "v_accvgpr_write_b32 a120, v6\n"
    "v_accvgpr_write_b32 a121, v7\n"
    "v_accvgpr_read_b32 v6, a122\n"
    "v_accvgpr_read_b32 v7, a123\n"
    "v_mul_f32 v6, v5, v6\n"
    "v_mul_f32 v7, v5, v7\n"
    "v_accvgpr_write_b32 a122, v6\n"
    "v_accvgpr_write_b32 a123, v7\n"
    "v_accvgpr_read_b32 v6, a124\n"
    "v_accvgpr_read_b32 v7, a125\n"
    "v_mul_f32 v6, v5, v6\n"
    "v_mul_f32 v7, v5, v7\n"
    "v_accvgpr_write_b32 a124, v6\n"
    "v_accvgpr_write_b32 a125, v7\n"
    "v_accvgpr_read_b32 v6, a126\n"
    "v_accvgpr_read_b32 v7, a127\n"
    "v_mul_f32 v6, v5, v6\n"
    "v_mul_f32 v7, v5, v7\n"
    "v_accvgpr_write_b32 a126, v6\n"
    "v_accvgpr_write_b32 a127, v7\n"
    "s_branch .Lat_rsr10_%=\n"
    ".Lat_bend_%=:\n"
    "s_cmp_eq_u32 s69, 0\n"
    "s_mov_b32 s69, 0\n"
    "s_cbranch_scc0 .Lat_blk_%=\n"
    "s_waitcnt vmcnt(0) lgkmcnt(0)\n"
    "s_barrier\n"
    : "+{v[0:3]}"(c0), "+s"(kp) : "{s[36:39]}"(sb), "{s[40:43]}"(mkrsrc(kp->ws, WS_NEED))
    : "memory", "vcc", "scc", "s56", "s57", "s58", "s59", S10(6), S10(7), S10(8), "v4", "v5", "v6", "v7", "v8", "v9", V10(1), V10(2), "v30", "v31", VCLOB, V120, ACLOB);
}

DI void role_b(KP kp, int xg, int r, int wid) {
    const int h = 8 * xg + (r >> 2), cq = r & 3;
    proj<2>(kp, h, 16 * cq, 8 * cq, 8 * (7 - cq), wid);
    int lane = lane_id();
    const rsrc_t ws = mkrsrc(kp->ws, WS_NEED);
    {
        bar();
        if (wid == 0) {
            gst32(1u, ws, lane == 0 ? W_FBP + h * 128 + cq * 4 : OOB, 0);
            wait_words(ws, lane < 4 ? W_FBP + h * 128 + lane * 4 : OOB, 1, lane);
            __builtin_amdgcn_fence(__ATOMIC_ACQUIRE, "agent");
            vmwait<0>();
        }
        bar();
    }
    STAMP(2);
    attention(kp, h, cq, wid);
    STAMP_END(3);
    oproj<false>(mkrsrc(kp->bwo, 4 * MB), mkrsrc(kp->ws, WS_NEED), mkrsrc(kp->out, 4 * MB), WS_OB, r & 15, 1024, 16 * (2 * xg + (r >> 4)), 16, 1, wid, lane_id());
    STAMP_END(4);
}

constexpr u32 L_WO = 0, L_MEM = 131072, L_KX = 139264, L_QK = 147456, L_O = 151552, L_VX = 153600;
constexpr u32 X_MEM = 1048576;
DI void role_chain(KP kp, int h, int wid, int lane) {
    const __attribute__((address_space(4))) Params& p = *kp;
    const rsrc_t ws = mkrsrc(p.ws, WS_NEED);
    const int n = lane & 15, q4 = lane >> 4;
    const u32 xch = (u32)((((size_t)p.ws + CTL_BYTES + 2 * MB - 1) & ~(size_t)(2 * MB - 1)) - (size_t)p.ws);
    const u32 vred = xch + h * 16384 + (lane >> 2) * 256 + wid * 64 + (lane & 3) * 16;
    {
        const u32x4 s4 = {SENT, SENT, SENT, SENT};
        sfor<8>([&](auto i) { gst128(s4, ws, vred, (CI(i) >> 2) * 524288 + (CI(i) & 3) * 4096); });
        sfor<8>([&](auto i) { gst128(s4, ws, xch + X_MEM + (wid * 16 + (CI(i) >> 1) * 4 + q4) * 16384 + h * 256 + n * 16, (CI(i) & 1) * 8192); });
    }
    vmwait<0>();
    bar();
    if (wid == 0 && lane == 0) __hip_atomic_fetch_add((GAS u32*)((char*)p.ws + W_READY), 1u, RLX_AGENT);
    u32x8 m0;
    {
        const rsrc_t rm = mkrsrc(p.mem0, 8192);
        sfor<2>([&](auto i) {
            const u32 o = (2 * (n >> 2) + (n & 1)) * 1024 + (64 * (2 * wid + CI(i)) + 4 * q4 + (n & 2)) * 2;
            u32 d[4];
            sfor<4>([&](auto j) { d[CI(j)] = __builtin_amdgcn_raw_buffer_load_b32(rm, o + 32 * CI(j), 0, 0); });
            m0[4 * CI(i)] = (d[0] & 0xffffu) | (d[1] << 16); m0[4 * CI(i) + 1] = (d[2] & 0xffffu) | (d[3] << 16);
            m0[4 * CI(i) + 2] = (d[0] >> 16) | (d[1] & 0xffff0000u); m0[4 * CI(i) + 3] = (d[2] >> 16) | (d[3] & 0xffff0000u);
        });
    }
    const u32 qq = (lane >> 2) & 3, tok = 2 * (n >> 2) + (n & 1);
    u32x16 c0, c1;
    c0[0] = n & 2 ? 0xEEEEu : 0x4444u;
    c0[1] = L_MEM + lane * 16;
    c0[2] = L_QK + (32 * wid + n) * 32 + (q4 ^ (n >> 2)) * 8;
    c0[3] = L_QK + (16 * (q4 >> 1) + 4 * (q4 & 1) + qq) * 32 + ((lane & 3) ^ (q4 & 1)) * 8; c0[4] = (c0[3] ^ 16) + 256;
    c0[5] = c0[3] + wid * 1024; c0[6] = c0[4] + wid * 1024;
    c0[7] = n & 2 ? L_MEM + lane * 16 : L_O + wid * 512 + (q4 >> 1) * 256 + (n + 2 * (q4 & 1)) * 16;
    c0[8] = L_WO + wid * 32768 + lane * 16;
    c0[9] = n & 2 ? WS_KM + h * 2048 + wid * 512 + q4 * 128 + tok * 16 : OOB;
    c0[10] = WS_VM + h * 2048 + (32 * wid + n) * 16 + q4 * 4;
    c0[11] = xch + (8 * wid + (n >> 2)) * 16384 + h * 256 + q4 * 64 + (n & 3) * 16;
    c0[12] = c0[13] = c0[14] = c0[15] = 0;
    c1[0] = (lane >> 2) * 256 + (lane & 3) * 16;
    c1[1] = (n & 10) == 0 ? (2 * (q4 >> 1) + (n & 1)) * 16 + (2 * (q4 & 1) + (n >> 2)) * 4 : OOB;
    c1[2] = (L_KX + q4 * 512 + (16 * (n >> 3) + (n & 7)) * 16) ^ ((q4 & 1) << 7); c1[3] = c1[2] ^ 128;
    c1[4] = (lane ^ ((lane >> 5) << 3)) * 16; c1[5] = q4 * 512 + n * 16; c1[6] = (lane & 3) * 4; c1[7] = lane * 16;
    sfor<4>([&](auto j) { c1[8 + CI(j)] = n == 2 * CI(j) ? 0x3F80u : (n == 2 * CI(j) + 1 ? 0x3F800000u : 0u); });
    c1[12] = q4 * 8192 + h * 256 + ((n ^ ((q4 | ((wid & 1) << 2)) << 1)) << 4);
    c1[13] = q4 * 1024 + ((n ^ (((q4 & 1) | ((wid & 1) << 2)) << 1)) << 4);
    c1[14] = ((qq & 1) * 16 + 4 * (q4 >> 1) + 2 * (qq >> 1) + (q4 & 1)) * 256 + (((2 * wid) ^ (2 * (qq >> 1) + (q4 & 1) + 4 * (qq & 1))) << 5) + (lane & 3) * 8;
    c1[15] = L_WO + wid * 32768 + (16 * (q4 & 1) + 8 * (q4 >> 1) + 4 * (qq >> 1) + (qq & 1)) * 256 + ((qq | ((q4 & 1) << 2)) << 5) + (lane & 3) * 8;
    u32x4 sb = {xch + h * 16384 + wid * 64, xch + X_MEM + h * 256 + wid * 64, xch + X_MEM - L_MEM, WS_KA + h * 524288 + wid * 2048};
    const rsrc_t rq = mkrsrc(p.awq, 4 * MB), rk = mkrsrc(p.awk, 4 * MB), rv = mkrsrc(p.awv, 4 * MB);
    const u32 prof = WS_PROF + (blockIdx.x * 4 + wid) * 1024;
    asm volatile(
    "s_mov_b32 s68, 0x20000\n"
    "s_sub_u32 s29, s58, 139264\n"
    "s_lshl_b32 s70, s29, 6\n"
    "s_lshl_b32 s67, s29, 1\n"
    "v_xor_b32 v32, 32, v30\n"
    "v_add_u32 v33, 0x10000, v30\n"
    "v_add_u32 v34, 0x10000, v32\n"
    "v_xor_b32 v35, 64, v29\n"
    "s_lshl_b32 s62, s56, 10\n"
    "s_lshl_b32 s71, s29, 3\n"
    "s_add_u32 s62, s62, s71\n"
    "s_add_u32 m0, s67, 0\n"
    "s_add_u32 s60, s70, 0x0\n"
    "buffer_load_dwordx4 v28, s[44:47], s60 offen offset:0 nt lds\n"
    "s_add_u32 s60, s70, 0x7c00\n"
    "buffer_load_dwordx4 v28, s[44:47], s60 offen offset:1024 nt lds\n"
    "s_add_u32 s60, s70, 0xf800\n"
    "buffer_load_dwordx4 v28, s[44:47], s60 offen offset:2048 nt lds\n"
    "s_add_u32 s60, s70, 0x17400\n"
    "buffer_load_dwordx4 v28, s[44:47], s60 offen offset:3072 nt lds\n"
    "s_add_u32 m0, s67, 16384\n"
    "s_add_u32 s60, s70, 0x80000\n"
    "buffer_load_dwordx4 v28, s[44:47], s60 offen offset:0 nt lds\n"
    "s_add_u32 s60, s70, 0x87c00\n"
    "buffer_load_dwordx4 v28, s[44:47], s60 offen offset:1024 nt lds\n"
    "s_add_u32 s60, s70, 0x8f800\n"
    "buffer_load_dwordx4 v28, s[44:47], s60 offen offset:2048 nt lds\n"
    "s_add_u32 s60, s70, 0x97400\n"
    "buffer_load_dwordx4 v28, s[44:47], s60 offen offset:3072 nt lds\n"
    "s_add_u32 m0, s67, 32768\n"
    "s_add_u32 s60, s70, 0x100000\n"
    "buffer_load_dwordx4 v28, s[44:47], s60 offen offset:0 nt lds\n"
    "s_add_u32 s60, s70, 0x107c00\n"
    "buffer_load_dwordx4 v28, s[44:47], s60 offen offset:1024 nt lds\n"
    "s_add_u32 s60, s70, 0x10f800\n"
    "buffer_load_dwordx4 v28, s[44:47], s60 offen offset:2048 nt lds\n"
    "s_add_u32 s60, s70, 0x117400\n"
    "buffer_load_dwordx4 v28, s[44:47], s60 offen offset:3072 nt lds\n"
    "s_add_u32 m0, s67, 49152\n"
    "s_add_u32 s60, s70, 0x180000\n"
    "buffer_load_dwordx4 v28, s[44:47], s60 offen offset:0 nt lds\n"
    "s_add_u32 s60, s70, 0x187c00\n"
    "buffer_load_dwordx4 v28, s[44:47], s60 offen offset:1024 nt lds\n"
    "s_add_u32 s60, s70, 0x18f800\n"
    "buffer_load_dwordx4 v28, s[44:47], s60 offen offset:2048 nt lds\n"
    "s_add_u32 s60, s70, 0x197400\n"
    "buffer_load_dwordx4 v28, s[44:47], s60 offen offset:3072 nt lds\n"
    "s_add_u32 m0, s67, 65536\n"
    "s_add_u32 s60, s70, 0x200000\n"
    "buffer_load_dwordx4 v28, s[44:47], s60 offen offset:0 nt lds\n"
    "s_add_u32 s60, s70, 0x207c00\n"
    "buffer_load_dwordx4 v28, s[44:47], s60 offen offset:1024 nt lds\n"
    "s_add_u32 s60, s70, 0x20f800\n"
    "buffer_load_dwordx4 v28, s[44:47], s60 offen offset:2048 nt lds\n"
    "s_add_u32 s60, s70, 0x217400\n"
    "buffer_load_dwordx4 v28, s[44:47], s60 offen offset:3072 nt lds\n"
    "s_add_u32 m0, s67, 81920\n"
    "s_add_u32 s60, s70, 0x280000\n"
    "buffer_load_dwordx4 v28, s[44:47], s60 offen offset:0 nt lds\n"
    "s_add_u32 s60, s70, 0x287c00\n"
    "buffer_load_dwordx4 v28, s[44:47], s60 offen offset:1024 nt lds\n"
    "s_add_u32 s60, s70, 0x28f800\n"
    "buffer_load_dwordx4 v28, s[44:47], s60 offen offset:2048 nt lds\n"
    "s_add_u32 s60, s70, 0x297400\n"
    "buffer_load_dwordx4 v28, s[44:47], s60 offen offset:3072 nt lds\n"
    "s_add_u32 m0, s67, 98304\n"
    "s_add_u32 s60, s70, 0x300000\n"
    "buffer_load_dwordx4 v28, s[44:47], s60 offen offset:0 nt lds\n"
    "s_add_u32 s60, s70, 0x307c00\n"
    "buffer_load_dwordx4 v28, s[44:47], s60 offen offset:1024 nt lds\n"
    "s_add_u32 s60, s70, 0x30f800\n"
    "buffer_load_dwordx4 v28, s[44:47], s60 offen offset:2048 nt lds\n"
    "s_add_u32 s60, s70, 0x317400\n"
    "buffer_load_dwordx4 v28, s[44:47], s60 offen offset:3072 nt lds\n"
    "s_waitcnt vmcnt(24)\n"
    "s_waitcnt lgkmcnt(0)\n"
    "s_barrier\n"
    "s_add_u32 m0, s67, 114688\n"
    "s_add_u32 s60, s70, 0x380000\n"
    "buffer_load_dwordx4 v28, s[44:47], s60 offen offset:0 nt lds\n"
    "s_add_u32 s60, s70, 0x387c00\n"
    "buffer_load_dwordx4 v28, s[44:47], s60 offen offset:1024 nt lds\n"
    "s_add_u32 s60, s70, 0x38f800\n"
    "buffer_load_dwordx4 v28, s[44:47], s60 offen offset:2048 nt lds\n"
    "s_add_u32 s60, s70, 0x397400\n"
    "buffer_load_dwordx4 v28, s[44:47], s60 offen offset:3072 nt lds\n"
    "ds_read_b64_tr_b16 a[0:1], v30 offset:0\n"
    "ds_read_b64_tr_b16 a[2:3], v30 offset:8192\n"
    "ds_read_b64_tr_b16 a[4:5], v30 offset:2048\n"
    "ds_read_b64_tr_b16 a[6:7], v30 offset:10240\n"
    "ds_read_b64_tr_b16 a[8:9], v32 offset:0\n"
    "ds_read_b64_tr_b16 a[10:11], v32 offset:8192\n"
    "ds_read_b64_tr_b16 a[12:13], v32 offset:2048\n"
    "ds_read_b64_tr_b16 a[14:15], v32 offset:10240\n"
    "s_waitcnt vmcnt(24)\n"
    "s_waitcnt lgkmcnt(0)\n"
    "s_barrier\n"
    "s_add_u32 m0, s67, 0\n"
    "s_add_u32 s60, s70, 0x0\n"
    "buffer_load_dwordx4 v28, s[48:51], s60 offen offset:0 nt lds\n"
    "s_add_u32 s60, s70, 0x7c00\n"
    "buffer_load_dwordx4 v28, s[48:51], s60 offen offset:1024 nt lds\n"
    "s_add_u32 s60, s70, 0xf800\n"
    "buffer_load_dwordx4 v28, s[48:51], s60 offen offset:2048 nt lds\n"
    "s_add_u32 s60, s70, 0x17400\n"
    "buffer_load_dwordx4 v28, s[48:51], s60 offen offset:3072 nt lds\n"
    "ds_read_b64_tr_b16 a[16:17], v30 offset:16384\n"
    "ds_read_b64_tr_b16 a[18:19], v30 offset:24576\n"
    "ds_read_b64_tr_b16 a[20:21], v30 offset:18432\n"
    "ds_read_b64_tr_b16 a[22:23], v30 offset:26624\n"
    "ds_read_b64_tr_b16 a[24:25], v32 offset:16384\n"
    "ds_read_b64_tr_b16 a[26:27], v32 offset:24576\n"
    "ds_read_b64_tr_b16 a[28:29], v32 offset:18432\n"
    "ds_read_b64_tr_b16 a[30:31], v32 offset:26624\n"
    "s_waitcnt vmcnt(24)\n"
    "s_waitcnt lgkmcnt(0)\n"
    "s_barrier\n"
    "s_add_u32 m0, s67, 16384\n"
    "s_add_u32 s60, s70, 0x80000\n"
    "buffer_load_dwordx4 v28, s[48:51], s60 offen offset:0 nt lds\n"
    "s_add_u32 s60, s70, 0x87c00\n"
    "buffer_load_dwordx4 v28, s[48:51], s60 offen offset:1024 nt lds\n"
    "s_add_u32 s60, s70, 0x8f800\n"
    "buffer_load_dwordx4 v28, s[48:51], s60 offen offset:2048 nt lds\n"
    "s_add_u32 s60, s70, 0x97400\n"
    "buffer_load_dwordx4 v28, s[48:51], s60 offen offset:3072 nt lds\n"
    "ds_read_b64_tr_b16 a[32:33], v30 offset:32768\n"
    "ds_read_b64_tr_b16 a[34:35], v30 offset:40960\n"
    "ds_read_b64_tr_b16 a[36:37], v30 offset:34816\n"
    "ds_read_b64_tr_b16 a[38:39], v30 offset:43008\n"
    "ds_read_b64_tr_b16 a[40:41], v32 offset:32768\n"
    "ds_read_b64_tr_b16 a[42:43], v32 offset:40960\n"
    "ds_read_b64_tr_b16 a[44:45], v32 offset:34816\n"
    "ds_read_b64_tr_b16 a[46:47], v32 offset:43008\n"
    "s_waitcnt vmcnt(24)\n"
    "s_waitcnt lgkmcnt(0)\n"
    "s_barrier\n"
    "s_add_u32 m0, s67, 32768\n"
    "s_add_u32 s60, s70, 0x100000\n"
    "buffer_load_dwordx4 v28, s[48:51], s60 offen offset:0 nt lds\n"
    "s_add_u32 s60, s70, 0x107c00\n"
    "buffer_load_dwordx4 v28, s[48:51], s60 offen offset:1024 nt lds\n"
    "s_add_u32 s60, s70, 0x10f800\n"
    "buffer_load_dwordx4 v28, s[48:51], s60 offen offset:2048 nt lds\n"
    "s_add_u32 s60, s70, 0x117400\n"
    "buffer_load_dwordx4 v28, s[48:51], s60 offen offset:3072 nt lds\n"
    "ds_read_b64_tr_b16 a[48:49], v30 offset:49152\n"
    "ds_read_b64_tr_b16 a[50:51], v30 offset:57344\n"
    "ds_read_b64_tr_b16 a[52:53], v30 offset:51200\n"
    "ds_read_b64_tr_b16 a[54:55], v30 offset:59392\n"
    "ds_read_b64_tr_b16 a[56:57], v32 offset:49152\n"
    "ds_read_b64_tr_b16 a[58:59], v32 offset:57344\n"
    "ds_read_b64_tr_b16 a[60:61], v32 offset:51200\n"
    "ds_read_b64_tr_b16 a[62:63], v32 offset:59392\n"
    "s_waitcnt vmcnt(24)\n"
    "s_waitcnt lgkmcnt(0)\n"
    "s_barrier\n"
    "s_add_u32 m0, s67, 49152\n"
    "s_add_u32 s60, s70, 0x180000\n"
    "buffer_load_dwordx4 v28, s[48:51], s60 offen offset:0 nt lds\n"
    "s_add_u32 s60, s70, 0x187c00\n"
    "buffer_load_dwordx4 v28, s[48:51], s60 offen offset:1024 nt lds\n"
    "s_add_u32 s60, s70, 0x18f800\n"
    "buffer_load_dwordx4 v28, s[48:51], s60 offen offset:2048 nt lds\n"
    "s_add_u32 s60, s70, 0x197400\n"
    "buffer_load_dwordx4 v28, s[48:51], s60 offen offset:3072 nt lds\n"
    "ds_read_b64_tr_b16 a[64:65], v33 offset:0\n"
    "ds_read_b64_tr_b16 a[66:67], v33 offset:8192\n"
    "ds_read_b64_tr_b16 a[68:69], v33 offset:2048\n"
    "ds_read_b64_tr_b16 a[70:71], v33 offset:10240\n"
    "ds_read_b64_tr_b16 a[72:73], v34 offset:0\n"
    "ds_read_b64_tr_b16 a[74:75], v34 offset:8192\n"
    "ds_read_b64_tr_b16 a[76:77], v34 offset:2048\n"
    "ds_read_b64_tr_b16 a[78:79], v34 offset:10240\n"
    "s_waitcnt vmcnt(24)\n"
    "s_waitcnt lgkmcnt(0)\n"
    "s_barrier\n"
    "s_add_u32 m0, s67, 65536\n"
    "s_add_u32 s60, s70, 0x200000\n"
    "buffer_load_dwordx4 v28, s[48:51], s60 offen offset:0 nt lds\n"
    "s_add_u32 s60, s70, 0x207c00\n"
    "buffer_load_dwordx4 v28, s[48:51], s60 offen offset:1024 nt lds\n"
    "s_add_u32 s60, s70, 0x20f800\n"
    "buffer_load_dwordx4 v28, s[48:51], s60 offen offset:2048 nt lds\n"
    "s_add_u32 s60, s70, 0x217400\n"
    "buffer_load_dwordx4 v28, s[48:51], s60 offen offset:3072 nt lds\n"
    "ds_read_b64_tr_b16 a[80:81], v33 offset:16384\n"
    "ds_read_b64_tr_b16 a[82:83], v33 offset:24576\n"
    "ds_read_b64_tr_b16 a[84:85], v33 offset:18432\n"
    "ds_read_b64_tr_b16 a[86:87], v33 offset:26624\n"
    "ds_read_b64_tr_b16 a[88:89], v34 offset:16384\n"
    "ds_read_b64_tr_b16 a[90:91], v34 offset:24576\n"
    "ds_read_b64_tr_b16 a[92:93], v34 offset:18432\n"
    "ds_read_b64_tr_b16 a[94:95], v34 offset:26624\n"
    "s_waitcnt vmcnt(24)\n"
    "s_waitcnt lgkmcnt(0)\n"
    "s_barrier\n"
    "s_add_u32 m0, s67, 81920\n"
    "s_add_u32 s60, s70, 0x280000\n"
    "buffer_load_dwordx4 v28, s[48:51], s60 offen offset:0 nt lds\n"
    "s_add_u32 s60, s70, 0x287c00\n"
    "buffer_load_dwordx4 v28, s[48:51], s60 offen offset:1024 nt lds\n"
    "s_add_u32 s60, s70, 0x28f800\n"
    "buffer_load_dwordx4 v28, s[48:51], s60 offen offset:2048 nt lds\n"
    "s_add_u32 s60, s70, 0x297400\n"
    "buffer_load_dwordx4 v28, s[48:51], s60 offen offset:3072 nt lds\n"
    "ds_read_b64_tr_b16 a[96:97], v33 offset:32768\n"
    "ds_read_b64_tr_b16 a[98:99], v33 offset:40960\n"
    "ds_read_b64_tr_b16 a[100:101], v33 offset:34816\n"
    "ds_read_b64_tr_b16 a[102:103], v33 offset:43008\n"
    "ds_read_b64_tr_b16 a[104:105], v34 offset:32768\n"
    "ds_read_b64_tr_b16 a[106:107], v34 offset:40960\n"
    "ds_read_b64_tr_b16 a[108:109], v34 offset:34816\n"
    "ds_read_b64_tr_b16 a[110:111], v34 offset:43008\n"
    "s_waitcnt vmcnt(24)\n"
    "s_waitcnt lgkmcnt(0)\n"
    "s_barrier\n"
    "s_add_u32 m0, s67, 98304\n"
    "s_add_u32 s60, s70, 0x300000\n"
    "buffer_load_dwordx4 v28, s[48:51], s60 offen offset:0 nt lds\n"
    "s_add_u32 s60, s70, 0x307c00\n"
    "buffer_load_dwordx4 v28, s[48:51], s60 offen offset:1024 nt lds\n"
    "s_add_u32 s60, s70, 0x30f800\n"
    "buffer_load_dwordx4 v28, s[48:51], s60 offen offset:2048 nt lds\n"
    "s_add_u32 s60, s70, 0x317400\n"
    "buffer_load_dwordx4 v28, s[48:51], s60 offen offset:3072 nt lds\n"
    "ds_read_b64_tr_b16 a[112:113], v33 offset:49152\n"
    "ds_read_b64_tr_b16 a[114:115], v33 offset:57344\n"
    "ds_read_b64_tr_b16 a[116:117], v33 offset:51200\n"
    "ds_read_b64_tr_b16 a[118:119], v33 offset:59392\n"
    "ds_read_b64_tr_b16 a[120:121], v34 offset:49152\n"
    "ds_read_b64_tr_b16 a[122:123], v34 offset:57344\n"
    "ds_read_b64_tr_b16 a[124:125], v34 offset:51200\n"
    "ds_read_b64_tr_b16 a[126:127], v34 offset:59392\n"
    "s_waitcnt vmcnt(24)\n"
    "s_waitcnt lgkmcnt(0)\n"
    "s_barrier\n"
    "s_add_u32 m0, s67, 114688\n"
    "s_add_u32 s60, s70, 0x380000\n"
    "buffer_load_dwordx4 v28, s[48:51], s60 offen offset:0 nt lds\n"
    "s_add_u32 s60, s70, 0x387c00\n"
    "buffer_load_dwordx4 v28, s[48:51], s60 offen offset:1024 nt lds\n"
    "s_add_u32 s60, s70, 0x38f800\n"
    "buffer_load_dwordx4 v28, s[48:51], s60 offen offset:2048 nt lds\n"
    "s_add_u32 s60, s70, 0x397400\n"
    "buffer_load_dwordx4 v28, s[48:51], s60 offen offset:3072 nt lds\n"
    "ds_read_b64_tr_b16 a[128:129], v30 offset:0\n"
    "ds_read_b64_tr_b16 a[130:131], v30 offset:8192\n"
    "ds_read_b64_tr_b16 a[132:133], v30 offset:2048\n"
    "ds_read_b64_tr_b16 a[134:135], v30 offset:10240\n"
    "ds_read_b64_tr_b16 a[136:137], v32 offset:0\n"
    "ds_read_b64_tr_b16 a[138:139], v32 offset:8192\n"
    "ds_read_b64_tr_b16 a[140:141], v32 offset:2048\n"
    "ds_read_b64_tr_b16 a[142:143], v32 offset:10240\n"
    "s_waitcnt vmcnt(24)\n"
    "s_waitcnt lgkmcnt(0)\n"
    "s_barrier\n"
    "s_add_u32 m0, s67, 0\n"
    "s_add_u32 s60, s70, 0x0\n"
    "buffer_load_dwordx4 v28, s[52:55], s60 offen offset:0 nt lds\n"
    "s_add_u32 s60, s70, 0x7c00\n"
    "buffer_load_dwordx4 v28, s[52:55], s60 offen offset:1024 nt lds\n"
    "s_add_u32 s60, s70, 0xf800\n"
    "buffer_load_dwordx4 v28, s[52:55], s60 offen offset:2048 nt lds\n"
    "s_add_u32 s60, s70, 0x17400\n"
    "buffer_load_dwordx4 v28, s[52:55], s60 offen offset:3072 nt lds\n"
    "ds_read_b64_tr_b16 a[144:145], v30 offset:16384\n"
    "ds_read_b64_tr_b16 a[146:147], v30 offset:24576\n"
    "ds_read_b64_tr_b16 a[148:149], v30 offset:18432\n"
    "ds_read_b64_tr_b16 a[150:151], v30 offset:26624\n"
    "ds_read_b64_tr_b16 a[152:153], v32 offset:16384\n"
    "ds_read_b64_tr_b16 a[154:155], v32 offset:24576\n"
    "ds_read_b64_tr_b16 a[156:157], v32 offset:18432\n"
    "ds_read_b64_tr_b16 a[158:159], v32 offset:26624\n"
    "s_waitcnt vmcnt(24)\n"
    "s_waitcnt lgkmcnt(0)\n"
    "s_barrier\n"
    "s_add_u32 m0, s67, 16384\n"
    "s_add_u32 s60, s70, 0x80000\n"
    "buffer_load_dwordx4 v28, s[52:55], s60 offen offset:0 nt lds\n"
    "s_add_u32 s60, s70, 0x87c00\n"
    "buffer_load_dwordx4 v28, s[52:55], s60 offen offset:1024 nt lds\n"
    "s_add_u32 s60, s70, 0x8f800\n"
    "buffer_load_dwordx4 v28, s[52:55], s60 offen offset:2048 nt lds\n"
    "s_add_u32 s60, s70, 0x97400\n"
    "buffer_load_dwordx4 v28, s[52:55], s60 offen offset:3072 nt lds\n"
    "ds_read_b64_tr_b16 a[160:161], v30 offset:32768\n"
    "ds_read_b64_tr_b16 a[162:163], v30 offset:40960\n"
    "ds_read_b64_tr_b16 a[164:165], v30 offset:34816\n"
    "ds_read_b64_tr_b16 a[166:167], v30 offset:43008\n"
    "ds_read_b64_tr_b16 a[168:169], v32 offset:32768\n"
    "ds_read_b64_tr_b16 a[170:171], v32 offset:40960\n"
    "ds_read_b64_tr_b16 a[172:173], v32 offset:34816\n"
    "ds_read_b64_tr_b16 a[174:175], v32 offset:43008\n"
    "s_waitcnt vmcnt(24)\n"
    "s_waitcnt lgkmcnt(0)\n"
    "s_barrier\n"
    "s_add_u32 m0, s67, 32768\n"
    "s_add_u32 s60, s70, 0x100000\n"
    "buffer_load_dwordx4 v28, s[52:55], s60 offen offset:0 nt lds\n"
    "s_add_u32 s60, s70, 0x107c00\n"
    "buffer_load_dwordx4 v28, s[52:55], s60 offen offset:1024 nt lds\n"
    "s_add_u32 s60, s70, 0x10f800\n"
    "buffer_load_dwordx4 v28, s[52:55], s60 offen offset:2048 nt lds\n"
    "s_add_u32 s60, s70, 0x117400\n"
    "buffer_load_dwordx4 v28, s[52:55], s60 offen offset:3072 nt lds\n"
    "ds_read_b64_tr_b16 a[176:177], v30 offset:49152\n"
    "ds_read_b64_tr_b16 a[178:179], v30 offset:57344\n"
    "ds_read_b64_tr_b16 a[180:181], v30 offset:51200\n"
    "ds_read_b64_tr_b16 a[182:183], v30 offset:59392\n"
    "ds_read_b64_tr_b16 a[184:185], v32 offset:49152\n"
    "ds_read_b64_tr_b16 a[186:187], v32 offset:57344\n"
    "ds_read_b64_tr_b16 a[188:189], v32 offset:51200\n"
    "ds_read_b64_tr_b16 a[190:191], v32 offset:59392\n"
    "s_waitcnt vmcnt(24)\n"
    "s_waitcnt lgkmcnt(0)\n"
    "s_barrier\n"
    "s_add_u32 m0, s67, 49152\n"
    "s_add_u32 s60, s70, 0x180000\n"
    "buffer_load_dwordx4 v28, s[52:55], s60 offen offset:0 nt lds\n"
    "s_add_u32 s60, s70, 0x187c00\n"
    "buffer_load_dwordx4 v28, s[52:55], s60 offen offset:1024 nt lds\n"
    "s_add_u32 s60, s70, 0x18f800\n"
    "buffer_load_dwordx4 v28, s[52:55], s60 offen offset:2048 nt lds\n"
    "s_add_u32 s60, s70, 0x197400\n"
    "buffer_load_dwordx4 v28, s[52:55], s60 offen offset:3072 nt lds\n"
    "ds_read_b64_tr_b16 a[192:193], v33 offset:0\n"
    "ds_read_b64_tr_b16 a[194:195], v33 offset:8192\n"
    "ds_read_b64_tr_b16 a[196:197], v33 offset:2048\n"
    "ds_read_b64_tr_b16 a[198:199], v33 offset:10240\n"
    "ds_read_b64_tr_b16 a[200:201], v34 offset:0\n"
    "ds_read_b64_tr_b16 a[202:203], v34 offset:8192\n"
    "ds_read_b64_tr_b16 a[204:205], v34 offset:2048\n"
    "ds_read_b64_tr_b16 a[206:207], v34 offset:10240\n"
    "s_waitcnt vmcnt(24)\n"
    "s_waitcnt lgkmcnt(0)\n"
    "s_barrier\n"
    "s_add_u32 m0, s67, 65536\n"
    "s_add_u32 s60, s70, 0x200000\n"
    "buffer_load_dwordx4 v28, s[52:55], s60 offen offset:0 nt lds\n"
    "s_add_u32 s60, s70, 0x207c00\n"
    "buffer_load_dwordx4 v28, s[52:55], s60 offen offset:1024 nt lds\n"
    "s_add_u32 s60, s70, 0x20f800\n"
    "buffer_load_dwordx4 v28, s[52:55], s60 offen offset:2048 nt lds\n"
    "s_add_u32 s60, s70, 0x217400\n"
    "buffer_load_dwordx4 v28, s[52:55], s60 offen offset:3072 nt lds\n"
    "ds_read_b64_tr_b16 a[208:209], v33 offset:16384\n"
    "ds_read_b64_tr_b16 a[210:211], v33 offset:24576\n"
    "ds_read_b64_tr_b16 a[212:213], v33 offset:18432\n"
    "ds_read_b64_tr_b16 a[214:215], v33 offset:26624\n"
    "ds_read_b64_tr_b16 a[216:217], v34 offset:16384\n"
    "ds_read_b64_tr_b16 a[218:219], v34 offset:24576\n"
    "ds_read_b64_tr_b16 a[220:221], v34 offset:18432\n"
    "ds_read_b64_tr_b16 a[222:223], v34 offset:26624\n"
    "s_waitcnt vmcnt(24)\n"
    "s_waitcnt lgkmcnt(0)\n"
    "s_barrier\n"
    "s_add_u32 m0, s67, 81920\n"
    "s_add_u32 s60, s70, 0x280000\n"
    "buffer_load_dwordx4 v28, s[52:55], s60 offen offset:0 nt lds\n"
    "s_add_u32 s60, s70, 0x287c00\n"
    "buffer_load_dwordx4 v28, s[52:55], s60 offen offset:1024 nt lds\n"
    "s_add_u32 s60, s70, 0x28f800\n"
    "buffer_load_dwordx4 v28, s[52:55], s60 offen offset:2048 nt lds\n"
    "s_add_u32 s60, s70, 0x297400\n"
    "buffer_load_dwordx4 v28, s[52:55], s60 offen offset:3072 nt lds\n"
    "ds_read_b64_tr_b16 a[224:225], v33 offset:32768\n"
    "ds_read_b64_tr_b16 a[226:227], v33 offset:40960\n"
    "ds_read_b64_tr_b16 a[228:229], v33 offset:34816\n"
    "ds_read_b64_tr_b16 a[230:231], v33 offset:43008\n"
    "ds_read_b64_tr_b16 a[232:233], v34 offset:32768\n"
    "ds_read_b64_tr_b16 a[234:235], v34 offset:40960\n"
    "ds_read_b64_tr_b16 a[236:237], v34 offset:34816\n"
    "ds_read_b64_tr_b16 a[238:239], v34 offset:43008\n"
    "s_waitcnt vmcnt(24)\n"
    "s_waitcnt lgkmcnt(0)\n"
    "s_barrier\n"
    "s_add_u32 m0, s67, 98304\n"
    "s_add_u32 s60, s70, 0x300000\n"
    "buffer_load_dwordx4 v28, s[52:55], s60 offen offset:0 nt lds\n"
    "s_add_u32 s60, s70, 0x307c00\n"
    "buffer_load_dwordx4 v28, s[52:55], s60 offen offset:1024 nt lds\n"
    "s_add_u32 s60, s70, 0x30f800\n"
    "buffer_load_dwordx4 v28, s[52:55], s60 offen offset:2048 nt lds\n"
    "s_add_u32 s60, s70, 0x317400\n"
    "buffer_load_dwordx4 v28, s[52:55], s60 offen offset:3072 nt lds\n"
    "ds_read_b64_tr_b16 a[240:241], v33 offset:49152\n"
    "ds_read_b64_tr_b16 a[242:243], v33 offset:57344\n"
    "ds_read_b64_tr_b16 a[244:245], v33 offset:51200\n"
    "ds_read_b64_tr_b16 a[246:247], v33 offset:59392\n"
    "ds_read_b64_tr_b16 a[248:249], v34 offset:49152\n"
    "ds_read_b64_tr_b16 a[250:251], v34 offset:57344\n"
    "ds_read_b64_tr_b16 a[252:253], v34 offset:51200\n"
    "ds_read_b64_tr_b16 a[254:255], v34 offset:59392\n"
    "s_waitcnt vmcnt(24)\n"
    "s_waitcnt lgkmcnt(0)\n"
    "s_barrier\n"
    "s_add_u32 m0, s67, 114688\n"
    "s_add_u32 s60, s70, 0x380000\n"
    "buffer_load_dwordx4 v28, s[52:55], s60 offen offset:0 nt lds\n"
    "s_add_u32 s60, s70, 0x387c00\n"
    "buffer_load_dwordx4 v28, s[52:55], s60 offen offset:1024 nt lds\n"
    "s_add_u32 s60, s70, 0x38f800\n"
    "buffer_load_dwordx4 v28, s[52:55], s60 offen offset:2048 nt lds\n"
    "s_add_u32 s60, s70, 0x397400\n"
    "buffer_load_dwordx4 v28, s[52:55], s60 offen offset:3072 nt lds\n"
    "ds_read_b64_tr_b16 v[128:129], v30 offset:0\n"
    "ds_read_b64_tr_b16 v[130:131], v30 offset:8192\n"
    "ds_read_b64_tr_b16 v[132:133], v30 offset:2048\n"
    "ds_read_b64_tr_b16 v[134:135], v30 offset:10240\n"
    "ds_read_b64_tr_b16 v[136:137], v32 offset:0\n"
    "ds_read_b64_tr_b16 v[138:139], v32 offset:8192\n"
    "ds_read_b64_tr_b16 v[140:141], v32 offset:2048\n"
    "ds_read_b64_tr_b16 v[142:143], v32 offset:10240\n"
    "s_waitcnt vmcnt(24)\n"
    "s_waitcnt lgkmcnt(0)\n"
    "s_barrier\n"
    "s_add_u32 m0, s67, 0\n"
    "s_add_u32 s60, s62, 0x0\n"
    "buffer_load_dwordx4 v29, s[20:23], s60 offen offset:0 nt lds\n"
    "s_add_u32 s60, s62, 0xc00\n"
    "buffer_load_dwordx4 v35, s[20:23], s60 offen offset:1024 nt lds\n"
    "s_add_u32 s60, s62, 0x1800\n"
    "buffer_load_dwordx4 v29, s[20:23], s60 offen offset:2048 nt lds\n"
    "s_add_u32 s60, s62, 0x2400\n"
    "buffer_load_dwordx4 v35, s[20:23], s60 offen offset:3072 nt lds\n"
    "ds_read_b64_tr_b16 v[144:145], v30 offset:16384\n"
    "ds_read_b64_tr_b16 v[146:147], v30 offset:24576\n"
    "ds_read_b64_tr_b16 v[148:149], v30 offset:18432\n"
    "ds_read_b64_tr_b16 v[150:151], v30 offset:26624\n"
    "ds_read_b64_tr_b16 v[152:153], v32 offset:16384\n"
    "ds_read_b64_tr_b16 v[154:155], v32 offset:24576\n"
    "ds_read_b64_tr_b16 v[156:157], v32 offset:18432\n"
    "ds_read_b64_tr_b16 v[158:159], v32 offset:26624\n"
    "s_waitcnt vmcnt(24)\n"
    "s_waitcnt lgkmcnt(0)\n"
    "s_barrier\n"
    "s_add_u32 m0, s67, 16384\n"
    "s_add_u32 s60, s62, 0x10000\n"
    "buffer_load_dwordx4 v29, s[20:23], s60 offen offset:0 nt lds\n"
    "s_add_u32 s60, s62, 0x10c00\n"
    "buffer_load_dwordx4 v35, s[20:23], s60 offen offset:1024 nt lds\n"
    "s_add_u32 s60, s62, 0x11800\n"
    "buffer_load_dwordx4 v29, s[20:23], s60 offen offset:2048 nt lds\n"
    "s_add_u32 s60, s62, 0x12400\n"
    "buffer_load_dwordx4 v35, s[20:23], s60 offen offset:3072 nt lds\n"
    "ds_read_b64_tr_b16 v[160:161], v30 offset:32768\n"
    "ds_read_b64_tr_b16 v[162:163], v30 offset:40960\n"
    "ds_read_b64_tr_b16 v[164:165], v30 offset:34816\n"
    "ds_read_b64_tr_b16 v[166:167], v30 offset:43008\n"
    "ds_read_b64_tr_b16 v[168:169], v32 offset:32768\n"
    "ds_read_b64_tr_b16 v[170:171], v32 offset:40960\n"
    "ds_read_b64_tr_b16 v[172:173], v32 offset:34816\n"
    "ds_read_b64_tr_b16 v[174:175], v32 offset:43008\n"
    "s_waitcnt vmcnt(24)\n"
    "s_waitcnt lgkmcnt(0)\n"
    "s_barrier\n"
    "s_add_u32 m0, s67, 32768\n"
    "s_add_u32 s60, s62, 0x100\n"
    "buffer_load_dwordx4 v29, s[20:23], s60 offen offset:0 nt lds\n"
    "s_add_u32 s60, s62, 0xd00\n"
    "buffer_load_dwordx4 v35, s[20:23], s60 offen offset:1024 nt lds\n"
    "s_add_u32 s60, s62, 0x1900\n"
    "buffer_load_dwordx4 v29, s[20:23], s60 offen offset:2048 nt lds\n"
    "s_add_u32 s60, s62, 0x2500\n"
    "buffer_load_dwordx4 v35, s[20:23], s60 offen offset:3072 nt lds\n"
    "ds_read_b64_tr_b16 v[176:177], v30 offset:49152\n"
    "ds_read_b64_tr_b16 v[178:179], v30 offset:57344\n"
    "ds_read_b64_tr_b16 v[180:181], v30 offset:51200\n"
    "ds_read_b64_tr_b16 v[182:183], v30 offset:59392\n"
    "ds_read_b64_tr_b16 v[184:185], v32 offset:49152\n"
    "ds_read_b64_tr_b16 v[186:187], v32 offset:57344\n"
    "ds_read_b64_tr_b16 v[188:189], v32 offset:51200\n"
    "ds_read_b64_tr_b16 v[190:191], v32 offset:59392\n"
    "s_waitcnt vmcnt(24)\n"
    "s_waitcnt lgkmcnt(0)\n"
    "s_barrier\n"
    "s_add_u32 m0, s67, 49152\n"
    "s_add_u32 s60, s62, 0x10100\n"
    "buffer_load_dwordx4 v29, s[20:23], s60 offen offset:0 nt lds\n"
    "s_add_u32 s60, s62, 0x10d00\n"
    "buffer_load_dwordx4 v35, s[20:23], s60 offen offset:1024 nt lds\n"
    "s_add_u32 s60, s62, 0x11900\n"
    "buffer_load_dwordx4 v29, s[20:23], s60 offen offset:2048 nt lds\n"
    "s_add_u32 s60, s62, 0x12500\n"
    "buffer_load_dwordx4 v35, s[20:23], s60 offen offset:3072 nt lds\n"
    "ds_read_b64_tr_b16 v[192:193], v33 offset:0\n"
    "ds_read_b64_tr_b16 v[194:195], v33 offset:8192\n"
    "ds_read_b64_tr_b16 v[196:197], v33 offset:2048\n"
    "ds_read_b64_tr_b16 v[198:199], v33 offset:10240\n"
    "ds_read_b64_tr_b16 v[200:201], v34 offset:0\n"
    "ds_read_b64_tr_b16 v[202:203], v34 offset:8192\n"
    "ds_read_b64_tr_b16 v[204:205], v34 offset:2048\n"
    "ds_read_b64_tr_b16 v[206:207], v34 offset:10240\n"
    "s_waitcnt vmcnt(24)\n"
    "s_waitcnt lgkmcnt(0)\n"
    "s_barrier\n"
    "s_add_u32 m0, s67, 65536\n"
    "s_add_u32 s60, s62, 0x200\n"
    "buffer_load_dwordx4 v29, s[20:23], s60 offen offset:0 nt lds\n"
    "s_add_u32 s60, s62, 0xe00\n"
    "buffer_load_dwordx4 v35, s[20:23], s60 offen offset:1024 nt lds\n"
    "s_add_u32 s60, s62, 0x1a00\n"
    "buffer_load_dwordx4 v29, s[20:23], s60 offen offset:2048 nt lds\n"
    "s_add_u32 s60, s62, 0x2600\n"
    "buffer_load_dwordx4 v35, s[20:23], s60 offen offset:3072 nt lds\n"
    "ds_read_b64_tr_b16 v[208:209], v33 offset:16384\n"
    "ds_read_b64_tr_b16 v[210:211], v33 offset:24576\n"
    "ds_read_b64_tr_b16 v[212:213], v33 offset:18432\n"
    "ds_read_b64_tr_b16 v[214:215], v33 offset:26624\n"
    "ds_read_b64_tr_b16 v[216:217], v34 offset:16384\n"
    "ds_read_b64_tr_b16 v[218:219], v34 offset:24576\n"
    "ds_read_b64_tr_b16 v[220:221], v34 offset:18432\n"
    "ds_read_b64_tr_b16 v[222:223], v34 offset:26624\n"
    "s_waitcnt vmcnt(24)\n"
    "s_waitcnt lgkmcnt(0)\n"
    "s_barrier\n"
    "s_add_u32 m0, s67, 81920\n"
    "s_add_u32 s60, s62, 0x10200\n"
    "buffer_load_dwordx4 v29, s[20:23], s60 offen offset:0 nt lds\n"
    "s_add_u32 s60, s62, 0x10e00\n"
    "buffer_load_dwordx4 v35, s[20:23], s60 offen offset:1024 nt lds\n"
    "s_add_u32 s60, s62, 0x11a00\n"
    "buffer_load_dwordx4 v29, s[20:23], s60 offen offset:2048 nt lds\n"
    "s_add_u32 s60, s62, 0x12600\n"
    "buffer_load_dwordx4 v35, s[20:23], s60 offen offset:3072 nt lds\n"
    "ds_read_b64_tr_b16 v[224:225], v33 offset:32768\n"
    "ds_read_b64_tr_b16 v[226:227], v33 offset:40960\n"
    "ds_read_b64_tr_b16 v[228:229], v33 offset:34816\n"
    "ds_read_b64_tr_b16 v[230:231], v33 offset:43008\n"
    "ds_read_b64_tr_b16 v[232:233], v34 offset:32768\n"
    "ds_read_b64_tr_b16 v[234:235], v34 offset:40960\n"
    "ds_read_b64_tr_b16 v[236:237], v34 offset:34816\n"
    "ds_read_b64_tr_b16 v[238:239], v34 offset:43008\n"
    "s_waitcnt vmcnt(24)\n"
    "s_waitcnt lgkmcnt(0)\n"
    "s_barrier\n"
    "s_add_u32 m0, s67, 98304\n"
    "s_add_u32 s60, s62, 0x300\n"
    "buffer_load_dwordx4 v29, s[20:23], s60 offen offset:0 nt lds\n"
    "s_add_u32 s60, s62, 0xf00\n"
    "buffer_load_dwordx4 v35, s[20:23], s60 offen offset:1024 nt lds\n"
    "s_add_u32 s60, s62, 0x1b00\n"
    "buffer_load_dwordx4 v29, s[20:23], s60 offen offset:2048 nt lds\n"
    "s_add_u32 s60, s62, 0x2700\n"
    "buffer_load_dwordx4 v35, s[20:23], s60 offen offset:3072 nt lds\n"
    "ds_read_b64_tr_b16 v[240:241], v33 offset:49152\n"
    "ds_read_b64_tr_b16 v[242:243], v33 offset:57344\n"
    "ds_read_b64_tr_b16 v[244:245], v33 offset:51200\n"
    "ds_read_b64_tr_b16 v[246:247], v33 offset:59392\n"
    "ds_read_b64_tr_b16 v[248:249], v34 offset:49152\n"
    "ds_read_b64_tr_b16 v[250:251], v34 offset:57344\n"
    "ds_read_b64_tr_b16 v[252:253], v34 offset:51200\n"
    "ds_read_b64_tr_b16 v[254:255], v34 offset:59392\n"
    "s_waitcnt lgkmcnt(0)\n"
    "s_barrier\n"
    "s_add_u32 m0, s67, 114688\n"
    "s_add_u32 s60, s62, 0x10300\n"
    "buffer_load_dwordx4 v29, s[20:23], s60 offen offset:0 nt lds\n"
    "s_add_u32 s60, s62, 0x10f00\n"
    "buffer_load_dwordx4 v35, s[20:23], s60 offen offset:1024 nt lds\n"
    "s_add_u32 s60, s62, 0x11b00\n"
    "buffer_load_dwordx4 v29, s[20:23], s60 offen offset:2048 nt lds\n"
    "s_add_u32 s60, s62, 0x12700\n"
    "buffer_load_dwordx4 v35, s[20:23], s60 offen offset:3072 nt lds\n"
    "s_waitcnt vmcnt(0)\n"
    "s_barrier\n"
    "v_xor_b32 v14, 0, v31\n"
    "ds_read_b64_tr_b16 v[32:33], v14 offset:0\n"
    "ds_read_b64_tr_b16 v[34:35], v14 offset:512\n"
    "ds_read_b64_tr_b16 v[36:37], v14 offset:8192\n"
    "ds_read_b64_tr_b16 v[38:39], v14 offset:8704\n"
    "v_xor_b32 v14, 32, v31\n"
    "ds_read_b64_tr_b16 v[40:41], v14 offset:0\n"
    "ds_read_b64_tr_b16 v[42:43], v14 offset:512\n"
    "ds_read_b64_tr_b16 v[44:45], v14 offset:8192\n"
    "ds_read_b64_tr_b16 v[46:47], v14 offset:8704\n"
    "v_xor_b32 v14, 64, v31\n"
    "ds_read_b64_tr_b16 v[48:49], v14 offset:0\n"
    "ds_read_b64_tr_b16 v[50:51], v14 offset:512\n"
    "ds_read_b64_tr_b16 v[52:53], v14 offset:8192\n"
    "ds_read_b64_tr_b16 v[54:55], v14 offset:8704\n"
    "v_xor_b32 v14, 96, v31\n"
    "ds_read_b64_tr_b16 v[56:57], v14 offset:0\n"
    "ds_read_b64_tr_b16 v[58:59], v14 offset:512\n"
    "ds_read_b64_tr_b16 v[60:61], v14 offset:8192\n"
    "ds_read_b64_tr_b16 v[62:63], v14 offset:8704\n"
    "v_xor_b32 v14, 128, v31\n"
    "ds_read_b64_tr_b16 v[64:65], v14 offset:0\n"
    "ds_read_b64_tr_b16 v[66:67], v14 offset:512\n"
    "ds_read_b64_tr_b16 v[68:69], v14 offset:8192\n"
    "ds_read_b64_tr_b16 v[70:71], v14 offset:8704\n"
    "v_xor_b32 v14, 160, v31\n"
    "ds_read_b64_tr_b16 v[72:73], v14 offset:0\n"
    "ds_read_b64_tr_b16 v[74:75], v14 offset:512\n"
    "ds_read_b64_tr_b16 v[76:77], v14 offset:8192\n"
    "ds_read_b64_tr_b16 v[78:79], v14 offset:8704\n"
    "v_xor_b32 v14, 192, v31\n"
    "ds_read_b64_tr_b16 v[80:81], v14 offset:0\n"
    "ds_read_b64_tr_b16 v[82:83], v14 offset:512\n"
    "ds_read_b64_tr_b16 v[84:85], v14 offset:8192\n"
    "ds_read_b64_tr_b16 v[86:87], v14 offset:8704\n"
    "v_xor_b32 v14, 224, v31\n"
    "ds_read_b64_tr_b16 v[88:89], v14 offset:0\n"
    "ds_read_b64_tr_b16 v[90:91], v14 offset:512\n"
    "ds_read_b64_tr_b16 v[92:93], v14 offset:8192\n"
    "ds_read_b64_tr_b16 v[94:95], v14 offset:8704\n"
    "s_waitcnt lgkmcnt(0)\n"
    "ds_write_b128 v8, v[32:35] offset:0\n"
    "ds_write_b128 v8, v[36:39] offset:1024\n"
    "ds_write_b128 v8, v[40:43] offset:2048\n"
    "ds_write_b128 v8, v[44:47] offset:3072\n"
    "ds_write_b128 v8, v[48:51] offset:4096\n"
    "ds_write_b128 v8, v[52:55] offset:5120\n"
    "ds_write_b128 v8, v[56:59] offset:6144\n"
    "ds_write_b128 v8, v[60:63] offset:7168\n"
    "ds_write_b128 v8, v[64:67] offset:8192\n"
    "ds_write_b128 v8, v[68:71] offset:9216\n"
    "ds_write_b128 v8, v[72:75] offset:10240\n"
    "ds_write_b128 v8, v[76:79] offset:11264\n"
    "ds_write_b128 v8, v[80:83] offset:12288\n"
    "ds_write_b128 v8, v[84:87] offset:13312\n"
    "ds_write_b128 v8, v[88:91] offset:14336\n"
    "ds_write_b128 v8, v[92:95] offset:15360\n"
    "v_xor_b32 v14, 0, v31\n"
    "ds_read_b64_tr_b16 v[32:33], v14 offset:16384\n"
    "ds_read_b64_tr_b16 v[34:35], v14 offset:16896\n"
    "ds_read_b64_tr_b16 v[36:37], v14 offset:24576\n"
    "ds_read_b64_tr_b16 v[38:39], v14 offset:25088\n"
    "v_xor_b32 v14, 32, v31\n"
    "ds_read_b64_tr_b16 v[40:41], v14 offset:16384\n"
    "ds_read_b64_tr_b16 v[42:43], v14 offset:16896\n"
    "ds_read_b64_tr_b16 v[44:45], v14 offset:24576\n"
    "ds_read_b64_tr_b16 v[46:47], v14 offset:25088\n"
    "v_xor_b32 v14, 64, v31\n"
    "ds_read_b64_tr_b16 v[48:49], v14 offset:16384\n"
    "ds_read_b64_tr_b16 v[50:51], v14 offset:16896\n"
    "ds_read_b64_tr_b16 v[52:53], v14 offset:24576\n"
    "ds_read_b64_tr_b16 v[54:55], v14 offset:25088\n"
    "v_xor_b32 v14, 96, v31\n"
    "ds_read_b64_tr_b16 v[56:57], v14 offset:16384\n"
    "ds_read_b64_tr_b16 v[58:59], v14 offset:16896\n"
    "ds_read_b64_tr_b16 v[60:61], v14 offset:24576\n"
    "ds_read_b64_tr_b16 v[62:63], v14 offset:25088\n"
    "v_xor_b32 v14, 128, v31\n"
    "ds_read_b64_tr_b16 v[64:65], v14 offset:16384\n"
    "ds_read_b64_tr_b16 v[66:67], v14 offset:16896\n"
    "ds_read_b64_tr_b16 v[68:69], v14 offset:24576\n"
    "ds_read_b64_tr_b16 v[70:71], v14 offset:25088\n"
    "v_xor_b32 v14, 160, v31\n"
    "ds_read_b64_tr_b16 v[72:73], v14 offset:16384\n"
    "ds_read_b64_tr_b16 v[74:75], v14 offset:16896\n"
    "ds_read_b64_tr_b16 v[76:77], v14 offset:24576\n"
    "ds_read_b64_tr_b16 v[78:79], v14 offset:25088\n"
    "v_xor_b32 v14, 192, v31\n"
    "ds_read_b64_tr_b16 v[80:81], v14 offset:16384\n"
    "ds_read_b64_tr_b16 v[82:83], v14 offset:16896\n"
    "ds_read_b64_tr_b16 v[84:85], v14 offset:24576\n"
    "ds_read_b64_tr_b16 v[86:87], v14 offset:25088\n"
    "v_xor_b32 v14, 224, v31\n"
    "ds_read_b64_tr_b16 v[88:89], v14 offset:16384\n"
    "ds_read_b64_tr_b16 v[90:91], v14 offset:16896\n"
    "ds_read_b64_tr_b16 v[92:93], v14 offset:24576\n"
    "ds_read_b64_tr_b16 v[94:95], v14 offset:25088\n"
    "s_waitcnt lgkmcnt(0)\n"
    "ds_write_b128 v8, v[32:35] offset:16384\n"
    "ds_write_b128 v8, v[36:39] offset:17408\n"
    "ds_write_b128 v8, v[40:43] offset:18432\n"
    "ds_write_b128 v8, v[44:47] offset:19456\n"
    "ds_write_b128 v8, v[48:51] offset:20480\n"
    "ds_write_b128 v8, v[52:55] offset:21504\n"
    "ds_write_b128 v8, v[56:59] offset:22528\n"
    "ds_write_b128 v8, v[60:63] offset:23552\n"
    "ds_write_b128 v8, v[64:67] offset:24576\n"
    "ds_write_b128 v8, v[68:71] offset:25600\n"
    "ds_write_b128 v8, v[72:75] offset:26624\n"
    "ds_write_b128 v8, v[76:79] offset:27648\n"
    "ds_write_b128 v8, v[80:83] offset:28672\n"
    "ds_write_b128 v8, v[84:87] offset:29696\n"
    "ds_write_b128 v8, v[88:91] offset:30720\n"
    "ds_write_b128 v8, v[92:95] offset:31744\n"
    "s_waitcnt lgkmcnt(0)\n"
    "s_barrier\n"
    "s_sub_u32 s29, s58, 139264\n"
    "s_add_u32 s28, s29, 131072\n"
    "v_add_u32 v28, s29, v1\n"
    "ds_write_b128 v1, v[16:19] offset:30720\n"
    "ds_write_b128 v1, v[20:23] offset:31744\n"
    "ds_write_b128 v28, v[120:123] offset:0\n"
    "ds_write_b128 v28, v[124:127] offset:1024\n"
    "s_add_u32 s34, s56, 0x1000\n"
    "s_lshr_b32 s60, s58, 9\n"
    "s_and_b32 s60, s60, 12\n"
    "s_add_u32 s35, s56, s60\n"
    "s_add_u32 s35, s35, 0x2000\n"
    "s_mov_b32 s70, 1\n"
    "s_mov_b32 s67, 0\n"
    ".Lch_h0_%=:\n"
    "buffer_load_dword v14, v22, s[40:43], s34 offen sc1\n"
    "s_waitcnt vmcnt(0)\n"
    "v_cmp_gt_u32 vcc, s70, v14\n"
    "s_cbranch_vccz .Lch_h0ok_%=\n"
    "s_sleep 2\n"
    "s_add_u32 s67, s67, 1\n"
    "s_cmp_lt_u32 s67, s68\n"
    "s_cbranch_scc1 .Lch_h0_%=\n"
    "s_mov_b32 s68, 0\n"
    "v_mov_b32 v62, 1\n"
    "buffer_store_dword v62, v13, s[40:43], 0 offen sc1\n"
    ".Lch_h0ok_%=:\n"
    "s_mov_b32 s61, s39\n"
    "s_add_u32 s96, s39, 0x1000000\n"
    "s_mov_b32 m0, s58\n"
    "s_nop 0\n"
    "buffer_load_dwordx4 v20, s[40:43], s61 offen offset:0 sc1 lds\n"
    "buffer_load_dwordx4 v20, s[40:43], s61 offen offset:1024 sc1 lds\n"
    "s_mov_b32 m0, s59\n"
    "s_nop 0\n"
    "buffer_load_dwordx4 v21, s[40:43], s96 offen offset:0 sc1 lds\n"
    "s_add_u32 m0, s59, 768\n"
    "s_nop 0\n"
    "buffer_load_dwordx4 v21, s[40:43], s96 offen offset:256 sc1 lds\n"
    "s_mov_b32 s70, 32\n"
    "s_movk_i32 s60, 0x100\n"
    "s_mov_b32 s67, 0\n"
    ".Lch_rdy_%=:\n"
    "buffer_load_dword v14, v13, s[40:43], s60 offen sc1\n"
    "s_waitcnt vmcnt(0)\n"
    "v_cmp_gt_u32 vcc, s70, v14\n"
    "s_cbranch_vccz .Lch_rdyok_%=\n"
    "s_sleep 2\n"
    "s_add_u32 s67, s67, 1\n"
    "s_cmp_lt_u32 s67, s68\n"
    "s_cbranch_scc1 .Lch_rdy_%=\n"
    "s_mov_b32 s68, 0\n"
    "v_mov_b32 v62, 1\n"
    "buffer_store_dword v62, v13, s[40:43], 0 offen sc1\n"
    ".Lch_rdyok_%=:\n"
    "s_mov_b32 s70, 1\n"
    "s_movk_i32 s60, 0x200\n"
    "s_mov_b32 s67, 0\n"
    ".Lch_pol_%=:\n"
    "buffer_load_dword v14, v13, s[40:43], s60 offen sc1\n"
    "s_waitcnt vmcnt(0)\n"
    "v_cmp_gt_u32 vcc, s70, v14\n"
    "s_cbranch_vccz .Lch_polok_%=\n"
    "s_sleep 2\n"
    "s_add_u32 s67, s67, 1\n"
    "s_cmp_lt_u32 s67, s68\n"
    "s_cbranch_scc1 .Lch_pol_%=\n"
    "s_mov_b32 s68, 0\n"
    "v_mov_b32 v62, 1\n"
    "buffer_store_dword v62, v13, s[40:43], 0 offen sc1\n"
    ".Lch_polok_%=:\n"
    "v_readfirstlane_b32 s33, v14\n"
    "s_and_b32 s33, s33, 2\n"
    "s_lshl_b32 s33, s33, 12\n"
    "s_add_u32 s36, s36, s33\n"
    "s_add_u32 s37, s37, s33\n"
    "s_add_u32 s38, s38, s33\n"
    "s_mov_b32 s71, 0\n"
    "s_mov_b32 s69, 0x3e0293ee\n"
    "s_mov_b32 s25, 0xffbfffbf\n"
    "s_lshl_b32 s62, s71, 16\n"
    "s_add_u32 s60, s71, 1\n"
    "s_lshl_b32 s61, s60, 13\n"
    "s_add_u32 s61, s61, s39\n"
    "s_add_u32 s96, s61, 0x1000000\n"
    "s_lshl_b32 s60, s60, 14\n"
    "s_add_u32 s97, s60, s37\n"
    "s_and_b32 s60, s71, 1\n"
    "s_lshl_b32 s60, s60, 19\n"
    "s_add_u32 s63, s60, s33\n"
    "s_add_u32 s64, s63, 0x10000\n"
    "s_add_u32 s65, s60, s36\n"
    "s_add_u32 s66, s65, 0x1000\n"
    "s_add_u32 s70, s71, 3\n"
    "v_mov_b64 v[96:97], 0\n"
    "v_mov_b64 v[98:99], 0\n"
    "v_mov_b64 v[100:101], 0\n"
    "v_mov_b64 v[102:103], 0\n"
    "v_mov_b64 v[104:105], 0\n"
    "v_mov_b64 v[106:107], 0\n"
    "v_mov_b64 v[108:109], 0\n"
    "v_mov_b64 v[110:111], 0\n"
    "v_mov_b64 v[112:113], 0\n"
    "v_mov_b64 v[114:115], 0\n"
    "v_mov_b64 v[116:117], 0\n"
    "v_mov_b64 v[118:119], 0\n"
    "s_waitcnt lgkmcnt(0)\n"
    "s_barrier\n"
    "ds_read_b128 v[64:67], v18 offset:0\n"
    "ds_read_b128 v[80:83], v19 offset:0\n"
    "ds_read_b128 v[68:71], v18 offset:2048\n"
    "ds_read_b128 v[84:87], v19 offset:2048\n"
    "ds_read_b128 v[72:75], v18 offset:4096\n"
    "ds_read_b128 v[88:91], v19 offset:4096\n"
    "ds_read_b128 v[76:79], v18 offset:6144\n"
    "ds_read_b128 v[92:95], v19 offset:6144\n"
    "v_add_u32 v60, s29, v1\n"
    "ds_read_b128 v[120:123], v60 offset:22528\n"
    "ds_read_b128 v[124:127], v60 offset:23552\n"
    "v_mov_b32 v15, v22\n"
    "v_mov_b32 v14, 0\n"
    "s_mov_b64 s[30:31], -1\n"
    "s_brev_b32 s18, 1\n"
    "s_brev_b32 s19, 1\n"
    "s_brev_b32 s26, 1\n"
    "s_brev_b32 s27, 1\n"
    "s_waitcnt lgkmcnt(0)\n"
    "s_mov_b32 s67, 0\n"
    ".Lch_loop_%=:\n"
    "ds_read_b128 v[32:35], v1 offset:0\n"
    "ds_read_b128 v[36:39], v1 offset:1024\n"
    "ds_read_b128 v[40:43], v1 offset:2048\n"
    "ds_read_b128 v[44:47], v1 offset:3072\n"
    "ds_read_b128 v[48:51], v1 offset:4096\n"
    "ds_read_b128 v[52:55], v1 offset:5120\n"
    "ds_read_b128 v[56:59], v1 offset:6144\n"
    "ds_read_b128 v[60:63], v1 offset:7168\n"
    "s_waitcnt lgkmcnt(7)\n"
    "v_smfmac_f32_16x16x64_bf16 v[96:99], v[32:35], a[0:7], v0\n"
    "buffer_store_dword v14, v13, s[40:43], s35 offen sc1\n"
    "v_smfmac_f32_16x16x64_bf16 v[100:103], v[32:35], a[8:15], v0\n"
    "s_waitcnt lgkmcnt(6)\n"
    "v_smfmac_f32_16x16x64_bf16 v[96:99], v[36:39], a[16:23], v0\n"
    "buffer_load_dword v15, v15, s[40:43], s34 offen sc1\n"
    "v_smfmac_f32_16x16x64_bf16 v[100:103], v[36:39], a[24:31], v0\n"
    "s_waitcnt lgkmcnt(5)\n"
    "v_smfmac_f32_16x16x64_bf16 v[96:99], v[40:43], a[32:39], v0\n"
    "buffer_store_dwordx4 v[24:27], v16, s[40:43], s18 offen sc1\n"
    "v_smfmac_f32_16x16x64_bf16 v[100:103], v[40:43], a[40:47], v0\n"
    "s_waitcnt lgkmcnt(4)\n"
    "v_smfmac_f32_16x16x64_bf16 v[96:99], v[44:47], a[48:55], v0\n"
    "v_smfmac_f32_16x16x64_bf16 v[100:103], v[44:47], a[56:63], v0\n"
    "s_waitcnt lgkmcnt(2)\n"
    "v_smfmac_f32_16x16x64_bf16 v[96:99], v[48:51], a[64:71], v0\n"
    "buffer_store_dwordx4 v[24:27], v16, s[40:43], s19 offen sc1\n"
    "v_smfmac_f32_16x16x64_bf16 v[100:103], v[48:51], a[72:79], v0\n"
    "v_smfmac_f32_16x16x64_bf16 v[96:99], v[52:55], a[80:87], v0\n"
    "s_mov_b32 m0, s58\n"
    "v_smfmac_f32_16x16x64_bf16 v[100:103], v[52:55], a[88:95], v0\n"
    "s_waitcnt lgkmcnt(0)\n"
    "v_smfmac_f32_16x16x64_bf16 v[96:99], v[56:59], a[96:103], v0\n"
    "buffer_load_dwordx4 v20, s[40:43], s26 offen offset:0 sc1 lds\n"
    "v_smfmac_f32_16x16x64_bf16 v[100:103], v[56:59], a[104:111], v0\n"
    "v_smfmac_f32_16x16x64_bf16 v[96:99], v[60:63], a[112:119], v0\n"
    "v_smfmac_f32_16x16x64_bf16 v[100:103], v[60:63], a[120:127], v0\n"
    "v_smfmac_f32_16x16x64_bf16 v[104:107], v[32:35], a[128:135], v0\n"
    "buffer_load_dwordx4 v20, s[40:43], s26 offen offset:1024 sc1 lds\n"
    "s_mov_b32 m0, s59\n"
    "v_smfmac_f32_16x16x64_bf16 v[108:111], v[32:35], a[136:143], v0\n"
    "v_add_f32 v96, v96, v98\n"
    "v_add_f32 v97, v97, v99\n"
    "v_smfmac_f32_16x16x64_bf16 v[104:107], v[36:39], a[144:151], v0\n"
    "v_add_f32 v100, v100, v102\n"
    "v_smfmac_f32_16x16x64_bf16 v[108:111], v[36:39], a[152:159], v0\n"
    "v_add_f32 v101, v101, v103\n"
    "v_smfmac_f32_16x16x64_bf16 v[104:107], v[40:43], a[160:167], v0\n"
    "v_cvt_pk_bf16_f32 v96, v96, v97\n"
    "v_cvt_pk_bf16_f32 v100, v100, v101\n"
    "ds_write2_b32 v2, v96, v100 offset0:0 offset1:128\n"
    "v_smfmac_f32_16x16x64_bf16 v[108:111], v[40:43], a[168:175], v0\n"
    "buffer_load_dwordx4 v21, s[40:43], s27 offen offset:0 sc1 lds\n"
    "v_smfmac_f32_16x16x64_bf16 v[104:107], v[44:47], a[176:183], v0\n"
    "s_add_u32 m0, s59, 768\n"
    "v_smfmac_f32_16x16x64_bf16 v[108:111], v[44:47], a[184:191], v0\n"
    "v_smfmac_f32_16x16x64_bf16 v[104:107], v[48:51], a[192:199], v0\n"
    "s_waitcnt lgkmcnt(0)\n"
    "s_barrier\n"
    "v_smfmac_f32_16x16x64_bf16 v[108:111], v[48:51], a[200:207], v0\n"
    "ds_read_b64_tr_b16 v[96:97], v3 offset:0\n"
    "ds_read_b64_tr_b16 v[98:99], v4 offset:0\n"
    "v_smfmac_f32_16x16x64_bf16 v[104:107], v[52:55], a[208:215], v0\n"
    "ds_read_b64_tr_b16 v[100:101], v3 offset:1024\n"
    "v_smfmac_f32_16x16x64_bf16 v[108:111], v[52:55], a[216:223], v0\n"
    "ds_read_b64_tr_b16 v[102:103], v4 offset:1024\n"
    "v_smfmac_f32_16x16x64_bf16 v[104:107], v[56:59], a[224:231], v0\n"
    "ds_read_b64_tr_b16 v[24:25], v3 offset:2048\n"
    "v_smfmac_f32_16x16x64_bf16 v[108:111], v[56:59], a[232:239], v0\n"
    "ds_read_b64_tr_b16 v[26:27], v4 offset:2048\n"
    "v_smfmac_f32_16x16x64_bf16 v[104:107], v[60:63], a[240:247], v0\n"
    "ds_read_b64_tr_b16 v[28:29], v3 offset:3072\n"
    "ds_read_b64_tr_b16 v[30:31], v4 offset:3072\n"
    "v_smfmac_f32_16x16x64_bf16 v[108:111], v[60:63], a[248:255], v0\n"
    "buffer_load_dwordx4 v21, s[40:43], s27 offen offset:256 sc1 lds\n"
    "s_waitcnt lgkmcnt(2)\n"
    "v_mfma_f32_16x16x32_bf16 v[16:19], v[64:67], v[96:99], 0\n"
    "s_waitcnt lgkmcnt(0)\n"
    "v_mfma_f32_16x16x32_bf16 v[16:19], v[68:71], v[100:103], v[16:19]\n"
    "v_add_f32 v104, v104, v106\n"
    "v_add_f32 v105, v105, v107\n"
    "v_mfma_f32_16x16x32_bf16 v[16:19], v[72:75], v[24:27], v[16:19]\n"
    "v_add_f32 v108, v108, v110\n"
    "v_add_f32 v109, v109, v111\n"
    "v_mfma_f32_16x16x32_bf16 v[16:19], v[76:79], v[28:31], v[16:19]\n"
    "v_cvt_pk_bf16_f32 v104, v104, v105\n"
    "v_cvt_pk_bf16_f32 v108, v108, v109\n"
    "ds_write2_b32 v2, v104, v108 offset0:1 offset1:129\n"
    "v_mfma_f32_16x16x32_bf16 v[20:23], v[80:83], v[96:99], 0\n"
    "v_mfma_f32_16x16x32_bf16 v[20:23], v[84:87], v[100:103], v[20:23]\n"
    "v_max3_f32 v110, v16, v17, v18\n"
    "v_mfma_f32_16x16x32_bf16 v[20:23], v[88:91], v[24:27], v[20:23]\n"
    "v_mfma_f32_16x16x32_bf16 v[20:23], v[92:95], v[28:31], v[20:23]\n"
    "v_smfmac_f32_16x16x64_bf16 v[112:115], v[32:35], v[128:135], v0\n"
    "s_waitcnt lgkmcnt(0)\n"
    "s_barrier\n"
    "v_smfmac_f32_16x16x64_bf16 v[116:119], v[32:35], v[136:143], v0\n"
    "ds_read_b64_tr_b16 v[96:97], v3 offset:0\n"
    "ds_read_b64_tr_b16 v[98:99], v4 offset:0\n"
    "v_smfmac_f32_16x16x64_bf16 v[112:115], v[36:39], v[144:151], v0\n"
    "ds_read_b64_tr_b16 v[100:101], v3 offset:1024\n"
    "ds_read_b64_tr_b16 v[102:103], v4 offset:1024\n"
    "v_smfmac_f32_16x16x64_bf16 v[116:119], v[36:39], v[152:159], v0\n"
    "ds_read_b64_tr_b16 v[24:25], v3 offset:2048\n"
    "ds_read_b64_tr_b16 v[26:27], v4 offset:2048\n"
    "v_smfmac_f32_16x16x64_bf16 v[112:115], v[40:43], v[160:167], v0\n"
    "ds_read_b64_tr_b16 v[28:29], v3 offset:3072\n"
    "ds_read_b64_tr_b16 v[30:31], v4 offset:3072\n"
    "s_waitcnt lgkmcnt(6)\n"
    "v_mfma_f32_16x16x32_bf16 v[104:107], v[96:99], v[96:99], 0\n"
    "s_waitcnt lgkmcnt(4)\n"
    "v_max3_f32 v111, v19, v20, v21\n"
    "v_mfma_f32_16x16x32_bf16 v[104:107], v[100:103], v[100:103], v[104:107]\n"
    "s_waitcnt lgkmcnt(2)\n"
    "v_max3_f32 v110, v110, v22, v23\n"
    "v_mfma_f32_16x16x32_bf16 v[104:107], v[24:27], v[24:27], v[104:107]\n"
    "s_waitcnt lgkmcnt(0)\n"
    "v_mfma_f32_16x16x32_bf16 v[104:107], v[28:31], v[28:31], v[104:107]\n"
    "ds_read_b64_tr_b16 v[32:33], v5 offset:0\n"
    "ds_read_b64_tr_b16 v[34:35], v6 offset:0\n"
    "v_smfmac_f32_16x16x64_bf16 v[116:119], v[40:43], v[168:175], v0\n"
    "ds_read_b128 v[64:67], v8 offset:0\n"
    "ds_read_b128 v[68:71], v8 offset:1024\n"
    "v_smfmac_f32_16x16x64_bf16 v[112:115], v[44:47], v[176:183], v0\n"
    "ds_read_b128 v[72:75], v8 offset:2048\n"
    "ds_read_b128 v[76:79], v8 offset:3072\n"
    "v_max3_f32 v111, v111, v106, v107\n"
    "v_max_f32 v110, v110, v111\n"
    "v_mov_b32 v111, v110\n"
    "v_smfmac_f32_16x16x64_bf16 v[116:119], v[44:47], v[184:191], v0\n"
    "ds_read_b128 v[80:83], v8 offset:4096\n"
    "ds_read_b128 v[84:87], v8 offset:5120\n"
    "v_permlane32_swap_b32 v110, v111\n"
    "v_max_f32 v110, v110, v111\n"
    "v_mov_b32 v111, v110\n"
    "v_smfmac_f32_16x16x64_bf16 v[112:115], v[48:51], v[192:199], v0\n"
    "ds_read_b128 v[88:91], v8 offset:6144\n"
    "ds_read_b128 v[92:95], v8 offset:7168\n"
    "v_permlane16_swap_b32 v110, v111\n"
    "v_max_f32 v110, v110, v111\n"
    "v_mul_f32 v110, s69, v110\n"
    "v_fma_f32 v16, v16, s69, -v110\n"
    "v_smfmac_f32_16x16x64_bf16 v[116:119], v[48:51], v[200:207], v0\n"
    "ds_read_b128 v[96:99], v8 offset:16384\n"
    "ds_read_b128 v[100:103], v8 offset:17408\n"
    "v_fma_f32 v17, v17, s69, -v110\n"
    "v_fma_f32 v18, v18, s69, -v110\n"
    "v_smfmac_f32_16x16x64_bf16 v[112:115], v[52:55], v[208:215], v0\n"
    "s_waitcnt lgkmcnt(10)\n"
    "buffer_store_dwordx4 v[32:35], v9, s[40:43], s62 offen sc1\n"
    "v_fma_f32 v19, v19, s69, -v110\n"
    "v_fma_f32 v20, v20, s69, -v110\n"
    "v_smfmac_f32_16x16x64_bf16 v[116:119], v[52:55], v[216:223], v0\n"
    "v_fma_f32 v21, v21, s69, -v110\n"
    "v_fma_f32 v22, v22, s69, -v110\n"
    "v_smfmac_f32_16x16x64_bf16 v[112:115], v[56:59], v[224:231], v0\n"
    "ds_read_b128 v[48:51], v8 offset:18432\n"
    "ds_read_b128 v[52:55], v8 offset:19456\n"
    "v_fma_f32 v23, v23, s69, -v110\n"
    "v_fma_f32 v106, v106, s69, -v110\n"
    "v_smfmac_f32_16x16x64_bf16 v[116:119], v[56:59], v[232:239], v0\n"
    "v_fma_f32 v107, v107, s69, -v110\n"
    "v_exp_f32 v16, v16\n"
    "v_smfmac_f32_16x16x64_bf16 v[112:115], v[60:63], v[240:247], v0\n"
    "v_exp_f32 v17, v17\n"
    "v_smfmac_f32_16x16x64_bf16 v[116:119], v[60:63], v[248:255], v0\n"
    "ds_read_b128 v[56:59], v8 offset:20480\n"
    "ds_read_b128 v[60:63], v8 offset:21504\n"
    "v_exp_f32 v18, v18\n"
    "v_exp_f32 v19, v19\n"
    "v_exp_f32 v20, v20\n"
    "v_exp_f32 v21, v21\n"
    "v_exp_f32 v22, v22\n"
    "v_exp_f32 v23, v23\n"
    "v_exp_f32 v106, v106\n"
    "v_exp_f32 v107, v107\n"
    "v_cvt_pk_bf16_f32 v36, v16, v17\n"
    "v_cvt_pk_bf16_f32 v37, v18, v19\n"
    "v_cvt_pk_bf16_f32 v38, v20, v21\n"
    "v_cvt_pk_bf16_f32 v39, v22, v23\n"
    "v_pk_add_f32 v[110:111], v[16:17], v[18:19]\n"
    "v_pk_add_f32 v[104:105], v[20:21], v[22:23]\n"
    "v_cvt_pk_bf16_f32 v34, v106, v107\n"
    "v_pk_add_f32 v[110:111], v[110:111], v[106:107]\n"
    "v_pk_add_f32 v[112:113], v[112:113], v[114:115]\n"
    "v_pk_add_f32 v[110:111], v[110:111], v[104:105]\n"
    "v_pk_add_f32 v[116:117], v[116:117], v[118:119]\n"
    "v_add_f32 v110, v110, v111\n"
    "v_mfma_f32_16x16x32_bf16 v[40:43], v[120:123], v[36:39], 0\n"
    "v_mov_b32 v111, v110\n"
    "v_cvt_pk_bf16_f32 v12, v112, v113\n"
    "v_mfma_f32_16x16x32_bf16 v[44:47], v[124:127], v[36:39], 0\n"
    "v_mov_b32 v35, 0\n"
    "v_permlane32_swap_b32 v110, v111\n"
    "v_cvt_pk_bf16_f32 v14, v116, v117\n"
    "v_add_f32 v110, v110, v111\n"
    "v_mfma_f32_16x16x16_bf16 v[40:43], v[12:13], v[34:35], v[40:43]\n"
    "ds_read_b128 v[120:123], v8 offset:10240\n"
    "v_mov_b32 v111, v110\n"
    "v_mfma_f32_16x16x16_bf16 v[44:47], v[14:15], v[34:35], v[44:47]\n"
    "ds_read_b128 v[124:127], v8 offset:11264\n"
    "v_permlane16_swap_b32 v110, v111\n"
    "v_add_f32 v110, v110, v111\n"
    "v_rcp_f32 v110, v110\n"
    "ds_read_b128 v[112:115], v8 offset:8192\n"
    "ds_read_b128 v[116:119], v8 offset:9216\n"
    "s_cmp_eq_u32 s71, 63\n"
    "s_cbranch_scc1 .Lch_done_%=\n"
    "v_pk_mul_f32 v[40:41], v[40:41], v[110:111] op_sel_hi:[1,0]\n"
    "v_pk_mul_f32 v[42:43], v[42:43], v[110:111] op_sel_hi:[1,0]\n"
    "v_pk_mul_f32 v[44:45], v[44:45], v[110:111] op_sel_hi:[1,0]\n"
    "v_pk_mul_f32 v[46:47], v[46:47], v[110:111] op_sel_hi:[1,0]\n"
    "v_cvt_pk_bf16_f32 v40, v40, v41\n"
    "v_cvt_pk_bf16_f32 v41, v42, v43\n"
    "v_cvt_pk_bf16_f32 v42, v44, v45\n"
    "v_cvt_pk_bf16_f32 v43, v46, v47\n"
    "ds_write_b128 v7, v[40:43] offset:0\n"
    "ds_read_b128 v[104:107], v8 offset:22528\n"
    "ds_read_b128 v[108:111], v8 offset:23552\n"
    "v_mov_b64 v[16:17], 0\n"
    "v_mov_b64 v[18:19], 0\n"
    "v_mov_b64 v[20:21], 0\n"
    "v_mov_b64 v[22:23], 0\n"
    "v_mov_b64 v[24:25], 0\n"
    "v_mov_b64 v[26:27], 0\n"
    "v_mov_b64 v[28:29], 0\n"
    "v_mov_b64 v[30:31], 0\n"
    "s_waitcnt lgkmcnt(2)\n"
    "s_barrier\n"
    "ds_read_b128 v[32:35], v1 offset:20480\n"
    "ds_read_b128 v[36:39], v1 offset:21504\n"
    "ds_read_b128 v[40:43], v8 offset:12288\n"
    "ds_read_b128 v[44:47], v8 offset:13312\n"
    "buffer_store_dword v12, v10, s[40:43], s62 offen sc1\n"
    "buffer_store_dword v14, v10, s[40:43], s62 offen offset:256 sc1\n"
    "s_waitcnt lgkmcnt(3)\n"
    "v_smfmac_f32_16x16x64_bf16 v[16:19], v[32:35], v[64:71], v0\n"
    "s_waitcnt lgkmcnt(2)\n"
    "v_smfmac_f32_16x16x64_bf16 v[20:23], v[32:35], v[72:79], v0\n"
    "ds_read_b128 v[64:67], v8 offset:14336\n"
    "v_smfmac_f32_16x16x64_bf16 v[24:27], v[32:35], v[80:87], v0\n"
    "ds_read_b128 v[68:71], v8 offset:15360\n"
    "v_mov_b64 v[80:81], 0\n"
    "v_smfmac_f32_16x16x64_bf16 v[28:31], v[32:35], v[88:95], v0\n"
    "ds_read_b128 v[72:75], v8 offset:24576\n"
    "v_mov_b64 v[82:83], 0\n"
    "v_smfmac_f32_16x16x64_bf16 v[16:19], v[36:39], v[96:103], v0\n"
    "ds_read_b128 v[76:79], v8 offset:25600\n"
    "v_mov_b64 v[84:85], 0\n"
    "v_smfmac_f32_16x16x64_bf16 v[20:23], v[36:39], v[48:55], v0\n"
    "ds_read_b128 v[96:99], v8 offset:26624\n"
    "v_mov_b64 v[86:87], 0\n"
    "v_smfmac_f32_16x16x64_bf16 v[24:27], v[36:39], v[56:63], v0\n"
    "ds_read_b128 v[100:103], v8 offset:27648\n"
    "v_mov_b64 v[88:89], 0\n"
    "v_smfmac_f32_16x16x64_bf16 v[28:31], v[36:39], v[104:111], v0\n"
    "ds_read_b128 v[48:51], v8 offset:28672\n"
    "v_mov_b64 v[90:91], 0\n"
    "v_smfmac_f32_16x16x64_bf16 v[80:83], v[32:35], v[112:119], v0\n"
    "ds_read_b128 v[52:55], v8 offset:29696\n"
    "v_mov_b64 v[92:93], 0\n"
    "v_smfmac_f32_16x16x64_bf16 v[84:87], v[32:35], v[120:127], v0\n"
    "ds_read_b128 v[56:59], v8 offset:30720\n"
    "v_mov_b64 v[94:95], 0\n"
    "s_waitcnt lgkmcnt(5)\n"
    "v_smfmac_f32_16x16x64_bf16 v[88:91], v[32:35], v[40:47], v0\n"
    "ds_read_b128 v[60:63], v8 offset:31744\n"
    "v_add_f32 v16, v16, v18\n"
    "v_smfmac_f32_16x16x64_bf16 v[92:95], v[32:35], v[64:71], v0\n"
    "s_waitcnt lgkmcnt(2)\n"
    "v_add_f32 v17, v17, v19\n"
    "v_smfmac_f32_16x16x64_bf16 v[80:83], v[36:39], v[72:79], v0\n"
    "v_add_f32 v20, v20, v22\n"
    "v_smfmac_f32_16x16x64_bf16 v[84:87], v[36:39], v[96:103], v0\n"
    "s_waitcnt lgkmcnt(0)\n"
    "v_add_f32 v21, v21, v23\n"
    "v_smfmac_f32_16x16x64_bf16 v[88:91], v[36:39], v[48:55], v0\n"
    "v_add_f32 v24, v24, v26\n"
    "v_smfmac_f32_16x16x64_bf16 v[92:95], v[36:39], v[56:63], v0\n"
    "v_add_f32 v25, v25, v27\n"
    "v_add_f32 v28, v28, v30\n"
    "v_add_f32 v29, v29, v31\n"
    "v_cvt_pk_bf16_f32 v16, v16, v17\n"
    "v_cvt_pk_bf16_f32 v17, v20, v21\n"
    "v_cvt_pk_bf16_f32 v18, v24, v25\n"
    "v_cvt_pk_bf16_f32 v19, v28, v29\n"
    "buffer_store_dwordx4 v[16:19], v11, s[40:43], s63 offen sc1\n"
    "v_pk_add_f32 v[80:81], v[80:81], v[82:83]\n"
    "v_pk_add_f32 v[84:85], v[84:85], v[86:87]\n"
    "v_pk_add_f32 v[88:89], v[88:89], v[90:91]\n"
    "v_pk_add_f32 v[92:93], v[92:93], v[94:95]\n"
    "v_cvt_pk_bf16_f32 v80, v80, v81\n"
    "v_cvt_pk_bf16_f32 v81, v84, v85\n"
    "v_cvt_pk_bf16_f32 v82, v88, v89\n"
    "v_cvt_pk_bf16_f32 v83, v92, v93\n"
    "buffer_store_dwordx4 v[80:83], v11, s[40:43], s64 offen sc1\n"
    "ds_read_b128 v[16:19], v1 offset:30720\n"
    "ds_read_b128 v[20:23], v1 offset:31744\n"
    "v_mov_b64 v[96:97], 0\n"
    "v_mov_b64 v[98:99], 0\n"
    "v_mov_b64 v[100:101], 0\n"
    "v_mov_b64 v[102:103], 0\n"
    "v_mov_b64 v[104:105], 0\n"
    "v_mov_b64 v[106:107], 0\n"
    "v_mov_b64 v[108:109], 0\n"
    "v_mov_b64 v[110:111], 0\n"
    "v_mov_b64 v[112:113], 0\n"
    "v_mov_b64 v[114:115], 0\n"
    "v_mov_b64 v[116:117], 0\n"
    "v_mov_b64 v[118:119], 0\n"
    "s_waitcnt lgkmcnt(0)\n"
    "s_cmp_eq_u64 s[30:31], 0\n"
    "s_cbranch_scc1 .Lch_kvdone_%=\n"
    "s_sub_u32 s60, s70, 1\n"
    "s_mov_b32 s67, 0\n"
    ".Lch_h_%=:\n"
    "buffer_load_dword v14, v22, s[40:43], s34 offen sc1\n"
    "s_waitcnt vmcnt(0)\n"
    "v_cmp_gt_u32 vcc, s60, v14\n"
    "s_cbranch_vccz .Lch_hok_%=\n"
    "s_sleep 2\n"
    "s_add_u32 s67, s67, 1\n"
    "s_cmp_lt_u32 s67, s68\n"
    "s_cbranch_scc1 .Lch_h_%=\n"
    "s_mov_b32 s68, 0\n"
    "v_mov_b32 v62, 1\n"
    "buffer_store_dword v62, v13, s[40:43], 0 offen sc1\n"
    ".Lch_hok_%=:\n"
    "s_mov_b32 m0, s58\n"
    "s_nop 0\n"
    "buffer_load_dwordx4 v20, s[40:43], s61 offen offset:0 sc1 lds\n"
    "buffer_load_dwordx4 v20, s[40:43], s61 offen offset:1024 sc1 lds\n"
    "s_mov_b32 m0, s59\n"
    "s_nop 0\n"
    "buffer_load_dwordx4 v21, s[40:43], s96 offen offset:0 sc1 lds\n"
    "s_add_u32 m0, s59, 768\n"
    "s_nop 0\n"
    "buffer_load_dwordx4 v21, s[40:43], s96 offen offset:256 sc1 lds\n"
    "s_waitcnt vmcnt(0)\n"
    ".Lch_kvdone_%=:\n"
    "s_waitcnt vmcnt(11)\n"
    "v_cmp_gt_u32 s[30:31], s70, v15\n"
    "v_and_b32 v28, 16, v1\n"
    "v_mov_b32 v29, 0x3f80\n"
    "v_lshlrev_b32 v28, v28, v29\n"
    "v_bfe_u32 v29, v1, 5, 3\n"
    "v_cmp_eq_u32 vcc, 0, v29\n"
    "s_nop 1\n"
    "v_cndmask_b32 v24, v13, v28, vcc\n"
    "v_cmp_eq_u32 vcc, 1, v29\n"
    "s_nop 1\n"
    "v_cndmask_b32 v25, v13, v28, vcc\n"
    "v_cmp_eq_u32 vcc, 2, v29\n"
    "s_nop 1\n"
    "v_cndmask_b32 v26, v13, v28, vcc\n"
    "v_cmp_eq_u32 vcc, 3, v29\n"
    "s_nop 1\n"
    "v_cndmask_b32 v27, v13, v28, vcc\n"
    "s_waitcnt vmcnt(0)\n"
    "s_nop 9\n"
    "s_mov_b32 s67, 0\n"
    ".Lch_h1_%=:\n"
    "buffer_load_dwordx4 v[40:43], v16, s[40:43], s65 offen sc1\n"
    "buffer_load_dwordx4 v[44:47], v16, s[40:43], s66 offen sc1\n"
    "s_waitcnt vmcnt(0)\n"
    "v_mfma_f32_16x16x32_bf16 v[48:51], v[40:43], v[24:27], 0\n"
    "v_mfma_f32_16x16x32_bf16 v[48:51], v[44:47], v[24:27], v[48:51]\n"
    "v_max3_u32 v53, v40, v41, v42\n"
    "v_max3_u32 v52, v43, v44, v45\n"
    "v_max3_u32 v53, v53, v46, v47\n"
    "v_max_u32 v53, v53, v52\n"
    "v_cmp_eq_u32 vcc, s25, v53\n"
    "s_cbranch_vccnz .Lch_h1re_%=\n"
    ".Lch_h1ok_%=:\n"
    "s_nop 1\n"
    "v_permlane32_swap_b32 v48, v50\n"
    "v_permlane32_swap_b32 v49, v51\n"
    "v_pk_add_f32 v[48:49], v[48:49], v[50:51]\n"
    "s_nop 1\n"
    "v_permlane16_swap_b32 v48, v49\n"
    "v_add_f32 v48, v48, v49\n"
    "s_nop 1\n"
    "v_mov_b32_dpp v49, v48 row_shl:2 row_mask:0xf bank_mask:0xf bound_ctrl:1\n"
    "v_cvt_pk_bf16_f32 v48, v48, v49\n"
    "buffer_store_dword v48, v17, s[40:43], s97 offen sc1\n"
    "s_barrier\n"
    "ds_read_b128 v[64:67], v18 offset:0\n"
    "ds_read_b128 v[80:83], v19 offset:0\n"
    "ds_read_b128 v[68:71], v18 offset:2048\n"
    "ds_read_b128 v[84:87], v19 offset:2048\n"
    "ds_read_b128 v[72:75], v18 offset:4096\n"
    "ds_read_b128 v[88:91], v19 offset:4096\n"
    "ds_read_b128 v[76:79], v18 offset:6144\n"
    "ds_read_b128 v[92:95], v19 offset:6144\n"
    "v_add_u32 v60, s29, v1\n"
    "ds_read_b128 v[120:123], v60 offset:22528\n"
    "ds_read_b128 v[124:127], v60 offset:23552\n"
    "v_mov_b32 v24, s25\n"
    "v_mov_b32 v25, s25\n"
    "v_mov_b32 v26, s25\n"
    "v_mov_b32 v27, s25\n"
    "s_mov_b32 s18, s65\n"
    "s_mov_b32 s19, s66\n"
    "s_add_u32 s71, s71, 1\n"
    "s_lshl_b32 s60, s71, 14\n"
    "s_add_u32 s98, s60, s38\n"
    "s_add_u32 s99, s98, 0x1000\n"
    "s_lshl_b32 s62, s71, 16\n"
    "s_add_u32 s60, s71, 1\n"
    "s_lshl_b32 s61, s60, 13\n"
    "s_add_u32 s61, s61, s39\n"
    "s_add_u32 s96, s61, 0x1000000\n"
    "s_lshl_b32 s60, s60, 14\n"
    "s_add_u32 s97, s60, s37\n"
    "s_and_b32 s60, s71, 1\n"
    "s_lshl_b32 s60, s60, 19\n"
    "s_add_u32 s63, s60, s33\n"
    "s_add_u32 s64, s63, 0x10000\n"
    "s_add_u32 s65, s60, s36\n"
    "s_add_u32 s66, s65, 0x1000\n"
    "s_add_u32 s70, s71, 3\n"
    "v_mov_b32 v14, s71\n"
    "v_mov_b32 v15, v22\n"
    "s_cmp_eq_u64 s[30:31], 0\n"
    "s_cselect_b32 s26, s61, 0x80000000\n"
    "s_cselect_b32 s27, s96, 0x80000000\n"
    "s_mov_b32 s67, 0\n"
    "s_waitcnt vmcnt(0)\n"
    "s_nop 15\n"
    "s_nop 15\n"
    "s_nop 15\n"
    "s_nop 1\n"
    ".Lch_h2_%=:\n"
    "buffer_load_dwordx4 v[40:43], v60, s[40:43], s98 offen sc1\n"
    "buffer_load_dwordx4 v[44:47], v60, s[40:43], s98 offen offset:1024 sc1\n"
    "s_mov_b32 m0, s28\n"
    "s_nop 0\n"
    "buffer_load_dwordx4 v60, s[40:43], s98 offen offset:0 sc1 lds\n"
    "buffer_load_dwordx4 v60, s[40:43], s98 offen offset:1024 sc1 lds\n"
    "s_waitcnt vmcnt(2)\n"
    "v_max3_u32 v53, v40, v41, v42\n"
    "v_max3_u32 v52, v43, v44, v45\n"
    "v_max3_u32 v53, v53, v46, v47\n"
    "v_max_u32 v53, v53, v52\n"
    "v_cmp_eq_u32 vcc, s25, v53\n"
    "s_cbranch_vccnz .Lch_h2re_%=\n"
    ".Lch_h2ok_%=:\n"
    "s_waitcnt vmcnt(0) lgkmcnt(0)\n"
    "s_barrier\n"
    "s_branch .Lch_loop_%=\n"
    ".Lch_h1re_%=:\n"
    "s_add_u32 s67, s67, 1\n"
    "s_cmp_lt_u32 s67, s68\n"
    "s_cbranch_scc1 .Lch_h1_%=\n"
    "s_mov_b32 s68, 0\n"
    "v_mov_b32 v62, 2\n"
    "buffer_store_dword v62, v13, s[40:43], 0 offen sc1\n"
    "s_branch .Lch_h1ok_%=\n"
    ".Lch_h2re_%=:\n"
    "s_add_u32 s67, s67, 1\n"
    "s_cmp_lt_u32 s67, s68\n"
    "s_cbranch_scc1 .Lch_h2_%=\n"
    "s_mov_b32 s68, 0\n"
    "v_mov_b32 v62, 3\n"
    "buffer_store_dword v62, v13, s[40:43], 0 offen sc1\n"
    "s_branch .Lch_h2ok_%=\n"
    ".Lch_done_%=:\n"
    "buffer_store_dword v12, v10, s[40:43], s62 offen sc1\n"
    "buffer_store_dword v14, v10, s[40:43], s62 offen offset:256 sc1\n"
    "s_waitcnt vmcnt(0) lgkmcnt(0)\n"
    "v_mov_b32 v14, 64\n"
    "buffer_store_dword v14, v13, s[40:43], s35 offen sc1\n"
    : "+{v[0:15]}"(c0), "+{v[16:31]}"(c1), "+{v[120:127]}"(m0), "+{s[36:39]}"(sb), "+s"(kp)
    : "{s[40:43]}"(ws), "{s[44:47]}"(rq), "{s[48:51]}"(rk), "{s[52:55]}"(rv), "{s[20:23]}"(mkrsrc(p.awo, 4 * MB)), "{s56}"(h * 128), "{s57}"(prof), "{s58}"(L_KX + wid * 2048), "{s59}"(L_VX + wid * 2048)
    : "memory", "vcc", "scc", "s18", "s19", "s24", "s25", "s26", "s27", "s28", "s29", "s30", "s31", "s33", "s34", "s35", VCLOB, ACLOB, S10(6), S10(7), S10(8), S10(9));
    STAMP_END(3);
}

DI void role_probe(KP kp, rsrc_t ws, int wid, int lane) {
    const __attribute__((address_space(4))) Params& p = *kp;
    const u32 xch = (u32)((((size_t)p.ws + CTL_BYTES + 2 * MB - 1) & ~(size_t)(2 * MB - 1)) - (size_t)p.ws);
    u32 best[2] = {~0u, ~0u};
    for (int i = 0; i < 8; ++i) {
        const u32 o = lane == 0 ? xch + X_MEM + (i & 1) * 8192 + (wid * 4 + (i >> 1)) * 512 : OOB;
        gst32(SENT, ws, o, 0); vmwait<0>();
        const unsigned long long t0 = __builtin_amdgcn_s_memtime(); lgkm0();
        u32 x = __builtin_amdgcn_raw_buffer_load_b32(ws, o, 0, SC1);
        asm volatile("s_waitcnt vmcnt(0)" : "+v"(x)::"memory");
        const u32 d = (u32)(__builtin_amdgcn_s_memtime() - t0);
        if (i & 1) best[1] = min(best[1], d); else best[0] = min(best[0], d);
    }
    if (lane == 0) *(LAS u32x2*)(size_t)(wid * 8) = u32x2{best[0], best[1]};
    bar();
    if (wid == 0) {
        const u32x4 a = ldsr128(0), b = ldsr128(16);
        const u32 n0 = min(min(a[0], a[2]), min(b[0], b[2])), n1 = min(min(a[1], a[3]), min(b[1], b[3]));
        gst32(n1 < n0 ? 3u : 1u, ws, lane == 0 ? W_POL : OOB, 0);
    }
}

#define NX 5
__global__ void __launch_bounds__(256, 1) fused_fwd(Params) {
    const int wid = rfl(threadIdx.x >> 6);
    KP kp = (KP)__builtin_amdgcn_kernarg_segment_ptr();
    if (threadIdx.x == 0) {
        u32 slot = (blockIdx.x & ~7u) | (__builtin_amdgcn_s_getreg(20 | (3 << 11)) & 7);
        GAS u32* claim = (GAS u32*)((char*)kp->ws + W_CLAIM);
        for (int k = 0; k < 256 && __hip_atomic_fetch_add(claim + slot, 1u, RLX_AGENT) != 0; ++k) slot = (slot + 1) & 255;
        *(LAS u32*)(size_t)(LDS_BYTES - 4) = slot;
    }
    bar();
    const int slot = rfl(*(const LAS u32*)(size_t)(LDS_BYTES - 4)), g = slot & 7, r = slot >> 3;
    lgkm0();
    own_agprs();
    int role, a, b = 0;
    if (g >= 4) { role = 4; a = g - 4; }
    else if (NX == 5) {
        if (g < 2) { if (r < 16) { role = 0; a = 16 * g + r; } else if (r < 24) { role = 2; a = 8 * g + r - 16; } else { role = g == 0 && r == 24 ? 5 : 6; a = 0; } }
        else if (r < 16) { role = 1; a = 16 * (g - 2) + r; }
        else { role = 3; a = r - 16; b = g - 2; }
    } else if (NX == 4) {
        if (r < 16) { role = r >> 3; a = 8 * g + (r & 7); }
        else if (r < 24) { role = 3; a = 8 * (g >> 1) + r - 16; b = g & 1; }
        else if (r < 28) { role = 2; a = 4 * g + r - 24; }
        else { role = g == 0 && r == 28 ? 5 : 6; a = 0; }
    } else if (g < 2) {
        if (NX == 2) { role = r >> 4; a = 16 * g + (r & 15); } else { role = g; a = r; }
    } else if (r < 16) { role = 3; a = r; b = g - 2; }
    else if (r < 24) { role = 2; a = (g - 2) * 8 + r - 16; }
    else { role = g == 2 && r == 24 ? 5 : 6; a = 0; }
#define DONE() __builtin_amdgcn_endpgm()
    if (role == 0) { if (ROLES & 1) role_chain(kp, a, wid, lane_id()); DONE(); }
    if (role == 1) { if (ROLES & 2) { proj<1>(kp, a, 0, 0, 0, wid); STAMP_END(3); } DONE(); }
    if (role == 4) { if (ROLES & 16) role_b(kp, a, r, wid); DONE(); }
    if (role == 2) { if (ROLES & 4) { role_f1(mkrsrc(kp->ws, WS_NEED), a * 4 + wid, lane_id()); STAMP_END(4); } DONE(); }
    if (role == 3) { if (ROLES & 8) { oproj<true>(mkrsrc(kp->awo, 4 * MB), mkrsrc(kp->ws, WS_NEED), mkrsrc(kp->out, 4 * MB), WS_OA, a, 0, b, 64, 2, wid, lane_id()); STAMP_END(4); } DONE(); }
    if (role == 5) role_probe(kp, mkrsrc(kp->ws, WS_NEED), wid, lane_id());
}

extern "C" void kernel_launch(void* const* d_in, const int* in_sizes, int n_in, void* d_out, int out_size, void* d_ws, size_t ws_size, hipStream_t stream) {
    (void)in_sizes; (void)out_size;
    if (n_in != 13 || ws_size < WS_NEED) return;
    Params p;
    p.ax = d_in[0]; p.mem0 = d_in[1]; p.awq = d_in[2]; p.awk = d_in[3]; p.awv = d_in[4]; p.awo = d_in[5];
    p.bx = d_in[7]; p.bc = d_in[8]; p.bwq = d_in[9]; p.bwk = d_in[10]; p.bwv = d_in[11]; p.bwo = d_in[12];
    p.out = d_out; p.ws = d_ws;
    (void)hipFuncSetAttribute((const void*)fused_fwd, hipFuncAttributeMaxDynamicSharedMemorySize, LDS_BYTES);
    (void)hipMemsetAsync(d_ws, 0, CTL_BYTES, stream);
    hipLaunchKernelGGL(fused_fwd, dim3(256), dim3(256), LDS_BYTES, stream, p);
}
```
